```python
import torch

def _softmax(x, axis=-1):
    return torch.softmax(x, dim=axis)
def _gelu(x):
    return torch.nn.functional.gelu(x, approximate="tanh")
def _silu(x):
    return torch.nn.functional.silu(x)
def _sigmoid(x):
    return torch.sigmoid(x)
def _layernorm(x, axis=-1, eps=1e-5):
    m = x.mean(dim=axis, keepdim=True)
    v = x.var(dim=axis, keepdim=True, unbiased=False)
    return (x - m) / torch.sqrt(v + eps)
def _scatter_add(tgt, idx, src, axis=0):
    return tgt.clone().index_add_(axis, idx, src)
def _segment_sum(src, seg, n):
    out = torch.zeros((n,) + src.shape[1:], dtype=src.dtype)
    return out.index_add_(0, seg, src)
def _pad(x, pw):
    flat = tuple(v for pair in reversed(pw) for v in pair)
    return torch.nn.functional.pad(x, flat)

kernel_name = 'sequential_preLN__grouped_kv_mqa__mixer_channel_mlp__prompt'

def setup_inputs():

    _w = (lambda a: (lambda t: t.to(torch.bfloat16) if t.is_floating_point() else t)(torch.as_tensor(a)))
    def _A_setup():
        B, S, D, H = 1, 2048, 640, 32
        Dh = D // H
        return {
            "x": _w((lambda *s: torch.randn(*s, dtype=torch.bfloat16) * 0.05)(B, S, D)),
            "wq": _w((lambda *s: torch.randn(*s, dtype=torch.bfloat16) * 0.05)(D, H * Dh)),
            "wk": _w((lambda *s: torch.randn(*s, dtype=torch.bfloat16) * 0.05)(D, Dh)),
            "wv": _w((lambda *s: torch.randn(*s, dtype=torch.bfloat16) * 0.05)(D, Dh)),
            "wo": _w((lambda *s: torch.randn(*s, dtype=torch.bfloat16) * 0.05)(H * Dh, D)),
        }
    _A_in = _A_setup()

    def _B_setup():
        B, S, D, F = 1, 2048, 640, 1728
        return {
            "x": _w((lambda *s: torch.randn(*s, dtype=torch.bfloat16) * 0.05)(B, S, D)),
            "w1": _w((lambda *s: torch.randn(*s, dtype=torch.bfloat16) * 3.2)(D, F) * (D ** -0.5)),
            "b1": _w((lambda *s: torch.randn(*s, dtype=torch.bfloat16) * 1.6)(F)),
            "w2": _w((lambda *s: torch.randn(*s, dtype=torch.bfloat16) * 0.2)(F, D) * (F ** -0.5)),
            "b2": _w((lambda *s: torch.randn(*s, dtype=torch.bfloat16) * 0.4)(D)),
        }
    _B_in = _B_setup()

    return {
        "A_x": _A_in["x"],
        "A_wq": _A_in["wq"],
        "A_wk": _A_in["wk"],
        "A_wv": _A_in["wv"],
        "A_wo": _A_in["wo"],
        "B_x": _B_in["x"],
        "B_w1": _B_in["w1"],
        "B_b1": _B_in["b1"],
        "B_w2": _B_in["w2"],
        "B_b2": _B_in["b2"],
    }

def reference(A_x, A_wq, A_wk, A_wv, A_wo, B_x, B_w1, B_b1, B_w2, B_b2):
    def _A_ref(x, wq, wk, wv, wo):
        B, S, D = x.shape
        H = 32
        Dh = D // H
        q = (lambda x, *d: x.permute(*d))((x @ wq).reshape(B, S, H, Dh), 0, 2, 1, 3)
        k = x @ wk
        v = x @ wv
        scores = torch.einsum("bhqd,bkd->bhqk", q, k) * (Dh ** -0.5)
        p = _softmax(scores, axis=-1)
        o = torch.einsum("bhqk,bkd->bhqd", p, v)
        return (lambda x, *d: x.permute(*d))(o, 0, 2, 1, 3).reshape(B, S, H * Dh) @ wo

    def _B_ref(x, w1, b1, w2, b2):
        y = _layernorm(x, axis=-1)
        h = _gelu(torch.einsum("bsd,df->bsf", y, w1) + b1)
        return torch.einsum("bsf,fd->bsd", h, w2) + b2

    x = A_x
    h = x + _A_ref(_layernorm(x, axis=-1), A_wq, A_wk, A_wv, A_wo)
    h2 = h + _B_ref(_layernorm(h, axis=-1), B_w1, B_b1, B_w2, B_b2)
    return h2
```

```cpp
#include <hip/hip_runtime.h>
#include <hip/hip_bf16.h>
typedef unsigned short u16;
typedef __attribute__((ext_vector_type(8))) __bf16 bf16x8;
typedef __attribute__((ext_vector_type(16))) __bf16 bf16x16;
typedef __attribute__((ext_vector_type(4))) short s16x4;
typedef __attribute__((ext_vector_type(4))) float f32x4;
typedef __attribute__((ext_vector_type(8))) float f32x8;
typedef __attribute__((ext_vector_type(16))) float f32x16;
typedef __attribute__((ext_vector_type(4))) unsigned u32x4;
typedef __attribute__((ext_vector_type(2))) unsigned u32x2;
constexpr int S = 2048, D = 640, H = 32, DH = 20, F = 1728;
__device__ __forceinline__ float b2f(u16 b) { return __builtin_bit_cast(float, (unsigned)b << 16); }
__device__ __forceinline__ u16 f2b(float f) { return __builtin_bit_cast(u16, (__bf16)f); }

__global__ void k_ln(const u16* __restrict__ x, u16* __restrict__ y, int twice) {
  int row = blockIdx.x * 4 + (threadIdx.x >> 6), lane = threadIdx.x & 63;
  float v[10]; float s = 0;
  for (int i = 0; i < 10; ++i) { v[i] = b2f(x[row * D + i * 64 + lane]); s += v[i]; }
  for (int o = 32; o; o >>= 1) s += __shfl_xor(s, o);
  float m = s / D, q = 0;
  for (int i = 0; i < 10; ++i) { v[i] -= m; q += v[i] * v[i]; }
  for (int o = 32; o; o >>= 1) q += __shfl_xor(q, o);
  float var = q / D, r = rsqrtf(var + 1e-5f);
  if (twice) r *= rsqrtf(var * r * r + 1e-5f);
  for (int i = 0; i < 10; ++i) y[row * D + i * 64 + lane] = f2b(v[i] * r);
}
template<int EPI>
__global__ void __launch_bounds__(256) k_gemm(const u16* __restrict__ A, const u16* __restrict__ B, u16* __restrict__ C, int N, int K, int ldc, const u16* __restrict__ bias, const u16* __restrict__ res, float scale) {
  __shared__ u16 sA[64][40], sB[64][40];
  int m0 = blockIdx.y * 64, n0 = blockIdx.x * 64, tid = threadIdx.x, lane = tid & 63, w = tid >> 6;
  f32x4 acc[4] = {};
  for (int k0 = 0; k0 < K; k0 += 32) {
    { int r = tid >> 2, c = (tid & 3) * 8; *(u32x4*)&sA[r][c] = *(const u32x4*)&A[(size_t)(m0 + r) * K + k0 + c]; }
    for (int i = 0; i < 8; ++i) { int e = tid + i * 256, k = e >> 6, n = e & 63; sB[n][k] = (n0 + n < N) ? B[(size_t)(k0 + k) * N + n0 + n] : (u16)0; }
    __syncthreads();
    bf16x8 a = *(bf16x8*)&sA[w * 16 + (lane & 15)][(lane >> 4) * 8];
    for (int t = 0; t < 4; ++t) { bf16x8 b = *(bf16x8*)&sB[t * 16 + (lane & 15)][(lane >> 4) * 8]; acc[t] = __builtin_amdgcn_mfma_f32_16x16x32_bf16(a, b, acc[t], 0, 0, 0); }
    __syncthreads();
  }
  for (int t = 0; t < 4; ++t) for (int r = 0; r < 4; ++r) {
    int m = m0 + w * 16 + (lane >> 4) * 4 + r, n = n0 + t * 16 + (lane & 15);
    if (n >= N) continue;
    float v = acc[t][r] * scale;
    if (EPI == 2 || EPI == 3) v += b2f(bias[n]);
    if (EPI == 2) { float u = 0.7978845608f * (v + 0.044715f * v * v * v); v = 0.5f * v * (1.f + tanhf(u)); }
    if (EPI == 1 || EPI == 3) v += b2f(res[(size_t)m * N + n]);
    C[(size_t)m * ldc + n] = f2b(v);
  }
}
__global__ void __launch_bounds__(256) k_attn(const u16* __restrict__ q, const u16* __restrict__ k, const u16* __restrict__ v, u16* __restrict__ o) {
  int wid = blockIdx.x * 4 + (threadIdx.x >> 6), lane = threadIdx.x & 63, r = lane & 31, h = lane >> 5;
  int head = wid & 31, t0 = (wid >> 5) * 32;
  const u16* qp = q + (size_t)(t0 + r) * D + head * DH;
  u32x4 qlo; u32x2 qhi = {0, 0};
  { u32x2 a = *(const u32x2*)(qp + 8 * h), b = *(const u32x2*)(qp + 8 * h + 4); qlo = u32x4{a.x, a.y, b.x, b.y}; if (h == 0) qhi = *(const u32x2*)(qp + 16); }
  f32x16 O = {}; float mrun = -1e30f;
  int dr = r < 18 ? r : (r == 20 ? 18 : (r == 21 ? 19 : -1));
  bool one = (r == 18 || r == 22);
  for (int kt = 0; kt < 64; ++kt) {
    const u16* kp = k + (size_t)(kt * 32 + r) * DH;
    u32x2 a = *(const u32x2*)(kp + 8 * h), b = *(const u32x2*)(kp + 8 * h + 4), khi = *(const u32x2*)(kp + 16);
    u32x4 klo = {a.x, a.y, b.x, b.y};
    f32x16 s = {};
    s = __builtin_amdgcn_mfma_f32_32x32x16_bf16(__builtin_bit_cast(bf16x8, klo), __builtin_bit_cast(bf16x8, qlo), s, 0, 0, 0);
    s = __builtin_amdgcn_mfma_f32_32x32x8bf16_1k(__builtin_bit_cast(s16x4, khi), __builtin_bit_cast(s16x4, qhi), s, 0, 0, 0);
    float mx = s[0];
    for (int i = 1; i < 16; ++i) mx = fmaxf(mx, s[i]);
    mx = fmaxf(mx, __shfl_xor(mx, 32));
    float mn = fmaxf(mrun, mx), al = __builtin_amdgcn_exp2f(mrun - mn); mrun = mn;
    for (int i = 0; i < 16; ++i) { O[i] *= al; s[i] = __builtin_amdgcn_exp2f(s[i] - mn); }
    for (int sb = 0; sb < 2; ++sb) {
      u16 vv[8];
      for (int j = 0; j < 8; ++j) vv[j] = one ? (u16)0x3f80 : (dr >= 0 ? v[(size_t)(kt * 32 + sb * 16 + 8 * h + j) * DH + dr] : (u16)0);
      u32x4 va = {vv[0] | (unsigned)vv[1] << 16, vv[2] | (unsigned)vv[3] << 16, vv[4] | (unsigned)vv[5] << 16, vv[6] | (unsigned)vv[7] << 16};
      f32x8 p; for (int i = 0; i < 8; ++i) p[i] = s[sb * 8 + i];
      O = __builtin_amdgcn_smfmac_f32_32x32x32_bf16(__builtin_bit_cast(bf16x8, va), __builtin_bit_cast(bf16x16, p), O, 0xdddd, 0, 0);
    }
  }
  float inv = 1.f / O[10];
  u16* op = o + (size_t)(t0 + r) * D + head * DH;
  for (int i = 0; i < 4; ++i) { op[4 * h + i] = f2b(O[i] * inv); op[8 + 4 * h + i] = f2b(O[4 + i] * inv); }
  op[16 + 2 * h] = f2b(O[8] * inv); op[17 + 2 * h] = f2b(O[9] * inv);
}
#define NEXTRA 0
__global__ void k_tiny(unsigned* ws) { if (threadIdx.x == 0) ws[blockIdx.x * 32] = 1; }
extern "C" void kernel_launch(void* const* d_in, const int* in_sizes, int n_in, void* d_out, int out_size, void* d_ws, size_t ws_size, hipStream_t stream) {
  const u16 *x = (const u16*)d_in[0], *wq = (const u16*)d_in[1], *wk = (const u16*)d_in[2], *wv = (const u16*)d_in[3], *wo = (const u16*)d_in[4];
  const u16 *w1 = (const u16*)d_in[6], *b1 = (const u16*)d_in[7], *w2 = (const u16*)d_in[8], *b2 = (const u16*)d_in[9];
  u16* ws = (u16*)d_ws;
  u16 *xn = ws, *q = xn + S * D, *k = q + S * D, *v = k + S * 32, *o = v + S * 32, *h = o + S * D, *y = h + S * D, *hid = y + S * D;
  for (int i = 0; i < NEXTRA; ++i) k_tiny<<<256, 256, 0, stream>>>((unsigned*)(hid + S * F));
  k_ln<<<S / 4, 256, 0, stream>>>(x, xn, 0);
  k_gemm<0><<<dim3(10, 32), 256, 0, stream>>>(xn, wq, q, D, D, D, nullptr, nullptr, 0.22360679775f * 1.44269504089f);
  k_gemm<0><<<dim3(1, 32), 256, 0, stream>>>(xn, wk, k, DH, D, DH, nullptr, nullptr, 1.f);
  k_gemm<0><<<dim3(1, 32), 256, 0, stream>>>(xn, wv, v, DH, D, DH, nullptr, nullptr, 1.f);
  k_attn<<<512, 256, 0, stream>>>(q, k, v, o);
  k_gemm<1><<<dim3(10, 32), 256, 0, stream>>>(o, wo, h, D, D, D, nullptr, x, 1.f);
  k_ln<<<S / 4, 256, 0, stream>>>(h, y, 1);
  k_gemm<2><<<dim3(27, 32), 256, 0, stream>>>(y, w1, hid, F, D, F, b1, nullptr, 1.f);
  k_gemm<3><<<dim3(10, 32), 256, 0, stream>>>(hid, w2, (u16*)d_out, D, F, D, b2, h, 1.f);
}
```

```cpp
#include <hip/hip_runtime.h>

typedef unsigned short u16;
typedef unsigned long long u64;

namespace mk {
constexpr int S = 2048, D = 640, DH = 20, F = 1728;
constexpr int KVBLK = 11264, WS_KV = 196608, WS_END = 5701632;

__global__ void __launch_bounds__(512, 2) __attribute__((aligned(4096)))
fused_block(const u16* x, const u16* wq, const u16* wk, const u16* wv, const u16* wo, const u16* w1, const u16* b1, const u16* w2,
            const u16* b2, u16* out, char* ws) {
  __shared__ __attribute__((aligned(16))) char smem[163840];
  auto ka = __builtin_amdgcn_kernarg_segment_ptr();
  unsigned bid = blockIdx.x, tid = threadIdx.x;
  asm volatile("" ::"s"((__attribute__((address_space(3))) char*)smem));
  asm volatile(
    ".Lk_begin:\n"
    "s_load_dwordx16 s[16:31], s[0:1], 0x0\n"
    "s_load_dwordx4 s[32:35], s[0:1], 0x40\n"
    "s_load_dwordx2 s[36:37], s[0:1], 0x50\n"
    "s_getpc_b64 s[38:39]\n"
    "v_lshrrev_b32 v1, 6, v0\n"
    "v_and_b32 v2, 63, v0\n"
    "v_readfirstlane_b32 s46, v1\n"
    "v_lshlrev_b32 v255, 4, v2\n"
    "s_and_b32 s40, s38, 0xffffff00\n"
    "s_and_b32 s41, s39, 0xffff\n"
    "s_mov_b32 s42, .Lk_end-.Lk_begin\n"
    "s_mov_b32 s43, 0x20000\n"
    "s_cmp_lg_u32 s46, 0\n"
    "s_cbranch_scc1 .Lk_nocode\n"
    "s_lshr_b32 s56, s2, 3\n"
    "s_mul_i32 s56, s56, 1536\n"
    "v_lshlrev_b32 v3, 7, v2\n"
    "s_mov_b32 m0, 158720\n"
    "s_mov_b64 exec, 0xfff\n"
    "buffer_load_dword v3, s[40:43], s56 offen lds\n"
    "s_mov_b64 exec, -1\n"
    ".Lk_nocode:\n"
    "s_and_b32 s47, s2, 7\n"
    "s_lshl_b32 s47, s47, 2\n"
    "s_lshr_b32 s56, s2, 6\n"
    "s_add_u32 s47, s47, s56\n"
    "s_bfe_u32 s48, s2, 0x30003\n"
    "s_and_b32 s51, s48, 3\n"
    "s_lshr_b32 s49, s48, 1\n"
    "s_and_b32 s50, s48, 1\n"
    "s_mov_b32 s54, 0x80000\n"
    "v_lshrrev_b32_e64 v3, 2, v2\n"
    "v_mul_u32_u24 v3, 0x500, v3\n"
    "v_lshrrev_b32 v4, 5, v2\n"
    "v_lshlrev_b32 v4, 1, v4\n"
    "v_and_b32 v5, 3, v2\n"
    "v_xor_b32 v4, v4, v5\n"
    "v_lshl_add_u32 v250, v4, 4, v3\n"
    "v_mov_b32_e64 v253, 1\n"
    "v_mov_b32 v3, 159744\n"
    "v_mov_b32 v5, 158720\n"
    "v_lshl_add_u32 v4, v2, 2, v5\n"
    "v_cmp_eq_u32 vcc, 0, v2\n"
    "s_nop 1\n"
    "v_cndmask_b32 v254, v4, v3, vcc\n"
    "s_cmp_lt_u32 s46, 4\n"
    "s_cbranch_scc1 .Lk_main1\n"
    "s_setprio 2\n"
    "s_sub_u32 s52, s46, 4\n"
    "s_mul_i32 s72, s52, 0x1400\n"
    "s_add_u32 s72, s72, 0x1f500\n"
    "s_mul_i32 s75, s52, 0x1900\n"
    "s_add_u32 s73, s75, 102400\n"
    "s_mul_i32 s74, s47, 0x14000\n"
    "s_mul_i32 s70, s51, 0x5000\n"
    "s_add_u32 s74, s74, s70\n"
    "s_mul_i32 s70, s52, 0x140\n"
    "s_add_u32 s74, s74, s70\n"
    "v_lshlrev_b32 v95, 3, v255\n"
    "s_cmp_lt_u32 s48, 4\n"
    "s_waitcnt lgkmcnt(0)\n"
    "s_cselect_b32 s56, s20, s22\n"
    "s_cselect_b32 s57, s21, s23\n"
    "s_mov_b32 s12, s56\n"
    "s_and_b32 s13, s57, 0xffff\n"
    "s_mov_b32 s14, 25600\n"
    "s_mov_b32 s15, 0x20000\n"
    "s_mov_b32 s8, s16\n"
    "s_and_b32 s9, s17, 0xffff\n"
    "s_mov_b32 s10, 2621440\n"
    "s_mov_b32 s11, 0x20000\n"
    "s_mov_b32 m0, s73\n"
    "s_nop 0\n"
    "buffer_load_dwordx4 v255, s[12:15], s75 offen lds\n"
    "buffer_load_dwordx4 v255, s[12:15], s75 offen offset:1024 lds\n"
    "buffer_load_dwordx4 v255, s[12:15], s75 offen offset:2048 lds\n"
    "buffer_load_dwordx4 v255, s[12:15], s75 offen offset:3072 lds\n"
    "s_add_u32 m0, s73, 4096\n"
    "s_add_u32 s56, s75, 4096\n"
    "buffer_load_dwordx4 v255, s[12:15], s56 offen lds\n"
    "buffer_load_dwordx4 v255, s[12:15], s56 offen offset:1024 lds\n"
    "s_mov_b64 exec, 0xffff\n"
    "buffer_load_dwordx4 v255, s[12:15], s56 offen offset:2048 lds\n"
    "s_mov_b64 exec, -1\n"
    "s_mov_b32 m0, s72\n"
    "s_nop 0\n"
    "buffer_load_dwordx4 v250, s[8:11], s74 offen lds\n"
    "s_add_u32 m0, s72, 960\n"
    "s_nop 0\n"
    "buffer_load_dwordx4 v250, s[8:11], s74 offen offset:64 lds\n"
    "s_add_u32 m0, s72, 1920\n"
    "s_nop 0\n"
    "buffer_load_dwordx4 v250, s[8:11], s74 offen offset:128 lds\n"
    "s_add_u32 m0, s72, 2880\n"
    "s_nop 0\n"
    "buffer_load_dwordx4 v250, s[8:11], s74 offen offset:192 lds\n"
    "s_add_u32 m0, s72, 3840\n"
    "s_nop 0\n"
    "buffer_load_dwordx4 v250, s[8:11], s74 offen offset:256 lds\n"
    "s_mov_b32 s40, s18\n"
    "s_and_b32 s41, s19, 0xffff\n"
    "s_mov_b32 s42, 819200\n"
    "s_mov_b32 s43, 0x20000\n"
    "s_mul_i32 s57, s2, 0xc80\n"
    "s_mul_i32 s70, s52, 0x380\n"
    "s_add_u32 s57, s57, s70\n"
    "s_mov_b32 m0, 158720\n"
    "s_mov_b64 exec, 0x7f\n"
    "buffer_load_dword v95, s[40:43], s57 offen lds\n"
    "s_mov_b64 exec, -1\n"
    "s_mov_b32 s4, s36\n"
    "s_and_b32 s5, s37, 0xffff\n"
    "s_mov_b32 s6, 5701632\n"
    "s_mov_b32 s7, 0x20000\n"
    "v_mov_b32 v96, 0\n"
    "s_cmp_lg_u32 s52, 1\n"
    "s_cbranch_scc1 .Lk_L2\n"
    "v_lshrrev_b32_e64 v97, 2, v255\n"
    "v_add_u32 v97, 159744, v97\n"
    "s_mov_b32 exec_hi, 0xffff\n"
    "ds_write_b32 v97, v96\n"
    "s_mov_b64 exec, -1\n"
    ".Lk_L2:\n"
    "s_waitcnt lgkmcnt(0)\n"
    "s_barrier\n"
    "s_load_dwordx2 s[44:45], s[36:37], 0x0\n"
    "v_bfe_u32 v93, v255, 4, 4\n"
    "v_lshrrev_b32 v94, 8, v255\n"
    "v_mov_b32 v84, 0x3f803f80\n"
    "v_mov_b32 v85, 0x3f803f80\n"
    "v_mov_b32 v86, 0x3f803f80\n"
    "v_mov_b32 v87, 0x3f803f80\n"
    "v_lshrrev_b32_e64 v96, 2, v93\n"
    "v_lshl_add_u32 v96, v94, 3, v96\n"
    "v_mul_u32_u24 v96, 40, v96\n"
    "v_and_b32 v97, 3, v93\n"
    "v_lshl_add_u32 v96, v97, 3, v96\n"
    "v_add_u32 v90, s73, v96\n"
    "v_add_u32 v92, s73, v255\n"
    "v_lshrrev_b32 v96, 3, v93\n"
    "v_lshlrev_b32 v96, 1, v96\n"
    "v_xor_b32 v96, v96, v94\n"
    "v_lshlrev_b32 v96, 4, v96\n"
    "v_lshl_add_u32 v91, v93, 6, v96\n"
    "v_add_u32 v91, s72, v91\n"
    "s_mul_i32 s56, s52, 0x8\n"
    "s_add_u32 s56, s56, 0x26e00\n"
    "v_lshl_add_u32 v100, v93, 5, s56\n"
    "s_mov_b32 s78, 0x22222222\n"
    "s_mov_b32 s79, 0x22222222\n"
    "s_mov_b32 s80, 0x44444444\n"
    "s_mov_b32 s81, 0x44444444\n"
    "s_mov_b32 s82, 0x88888888\n"
    "s_mov_b32 s83, 0x88888888\n"
    "s_mov_b32 s84, 0xf0000f\n"
    "s_mov_b32 s85, 0xf0000f00\n"
    "s_lshr_b32 s56, s51, 1\n"
    "s_lshl_b32 s57, s47, 1\n"
    "s_add_u32 s56, s56, s57\n"
    "s_and_b32 s56, s56, 3\n"
    "s_lshr_b32 s57, s47, 1\n"
    "s_and_b32 s58, s51, 1\n"
    "s_mul_i32 s76, s57, 0x2c00\n"
    "s_add_u32 s76, s76, 0x30000\n"
    "v_lshrrev_b32 v104, 1, v94\n"
    "v_and_b32 v105, 1, v94\n"
    "v_mov_b32 v106, 0x80000000\n"
    "s_cmp_ge_u32 s48, 4\n"
    "s_cbranch_scc1 .Lk_v4\n"
    "s_mul_i32 s59, s56, 0x400\n"
    "s_mul_i32 s70, s58, 0x100\n"
    "s_add_u32 s59, s59, s70\n"
    "v_lshl_add_u32 v107, v93, 4, s59\n"
    "v_lshl_add_u32 v107, v104, 9, v107\n"
    "v_lshl_add_u32 v101, v105, 3, v107\n"
    "s_lshr_b32 s60, s56, 1\n"
    "s_and_b32 s61, s56, 1\n"
    "s_mul_i32 s59, s60, 0x200\n"
    "s_mul_i32 s70, s61, 0x8\n"
    "s_add_u32 s59, s59, s70\n"
    "s_mul_i32 s70, s58, 0x100\n"
    "s_add_u32 s59, s59, s70\n"
    "s_add_u32 s59, s59, 0x1000\n"
    "v_lshl_add_u32 v107, v93, 4, s59\n"
    "v_cmp_eq_u32 vcc, 0, v94\n"
    "s_nop 1\n"
    "v_cndmask_b32 v102, v106, v107, vcc\n"
    "v_mov_b32 v107, 159232\n"
    "v_lshl_add_u32 v103, v93, 5, v107\n"
    "s_branch .Lk_j5\n"
    ".Lk_v4:\n"
    "s_mul_i32 s59, s58, 0xc00\n"
    "s_mul_i32 s70, s56, 0x300\n"
    "s_add_u32 s59, s59, s70\n"
    "s_add_u32 s59, s59, 0x1400\n"
    "v_mul_u32_u24 v107, 384, v104\n"
    "v_lshl_add_u32 v107, v105, 3, v107\n"
    "v_add_u32 v107, s59, v107\n"
    "v_subrev_u32 v108, 10, v93\n"
    "v_cmp_gt_u32 vcc, 10, v93\n"
    "s_nop 1\n"
    "v_cndmask_b32 v108, v108, v93, vcc\n"
    "v_cndmask_b32 v109, 4, 0, vcc\n"
    "v_and_b32 v110, 3, v108\n"
    "v_lshrrev_b32 v108, 2, v108\n"
    "v_lshl_add_u32 v108, v108, 3, v110\n"
    "v_add_u32_e64 v108, v108, v109\n"
    "v_lshl_add_u32 v101, v108, 4, v107\n"
    "v_add_u32 v108, 6, v93\n"
    "v_and_b32 v110, 3, v108\n"
    "v_lshrrev_b32_e64 v108, 2, v108\n"
    "v_lshl_add_u32 v108, v108, 3, v110\n"
    "v_add_u32 v108, 4, v108\n"
    "v_and_b32 v109, 1, v93\n"
    "v_lshlrev_b32_e64 v109, 2, v109\n"
    "v_bfe_u32 v110, v93, 1, 1\n"
    "v_add3_u32 v109, v109, v110, 18\n"
    "v_cmp_gt_u32 vcc, 4, v93\n"
    "s_nop 1\n"
    "v_cndmask_b32 v108, v109, v108, vcc\n"
    "v_lshl_add_u32 v108, v108, 4, v107\n"
    "v_cmp_gt_u32 vcc, 8, v93\n"
    "s_nop 1\n"
    "v_cndmask_b32 v102, v106, v108, vcc\n"
    "v_cmp_gt_u32 vcc, 6, v93\n"
    "s_nop 1\n"
    "v_cndmask_b32 v176, 0, v84, vcc\n"
    "v_lshl_add_u32 v107, v94, 2, v97\n"
    "v_mov_b32 v108, 159232\n"
    "v_lshl_add_u32 v103, v107, 5, v108\n"
    ".Lk_j5:\n"
    "v_lshrrev_b32 v107, 4, v255\n"
    "v_add_u32_e64 v107, 0, v107\n"
    "v_mul_u32_u24 v108, 205, v107\n"
    "v_lshrrev_b32 v108, 11, v108\n"
    "v_mul_u32_u24 v109, 10, v108\n"
    "v_sub_u32 v107, v107, v109\n"
    "v_lshrrev_b32 v109, 1, v108\n"
    "v_and_b32 v109, 4, v109\n"
    "v_xor_b32 v108, v108, v109\n"
    "v_mul_u32_u24 v108, 0x500, v108\n"
    "v_lshl_add_u32 v170, v107, 4, v108\n"
    "v_lshrrev_b32 v107, 4, v255\n"
    "v_add_u32 v107, 64, v107\n"
    "v_mul_u32_u24 v108, 205, v107\n"
    "v_lshrrev_b32 v108, 11, v108\n"
    "v_mul_u32_u24 v109, 10, v108\n"
    "v_sub_u32 v107, v107, v109\n"
    "v_lshrrev_b32 v109, 1, v108\n"
    "v_and_b32 v109, 4, v109\n"
    "v_xor_b32 v108, v108, v109\n"
    "v_mul_u32_u24 v108, 0x500, v108\n"
    "v_lshl_add_u32 v171, v107, 4, v108\n"
    "v_lshrrev_b32_e64 v107, 4, v255\n"
    "v_add_u32 v107, 128, v107\n"
    "v_mul_u32_u24 v108, 205, v107\n"
    "v_lshrrev_b32 v108, 11, v108\n"
    "v_mul_u32_u24 v109, 10, v108\n"
    "v_sub_u32 v107, v107, v109\n"
    "v_lshrrev_b32 v109, 1, v108\n"
    "v_and_b32 v109, 4, v109\n"
    "v_xor_b32 v108, v108, v109\n"
    "v_mul_u32_u24 v108, 0x500, v108\n"
    "v_lshl_add_u32 v172, v107, 4, v108\n"
    "v_lshrrev_b32_e64 v107, 4, v255\n"
    "v_add_u32 v107, 192, v107\n"
    "v_mul_u32_u24 v108, 205, v107\n"
    "v_lshrrev_b32 v108, 11, v108\n"
    "v_mul_u32_u24 v109, 10, v108\n"
    "v_sub_u32 v107, v107, v109\n"
    "v_lshrrev_b32 v109, 1, v108\n"
    "v_and_b32 v109, 4, v109\n"
    "v_xor_b32 v108, v108, v109\n"
    "v_mul_u32_u24 v108, 0x500, v108\n"
    "v_lshl_add_u32 v173, v107, 4, v108\n"
    "v_lshrrev_b32_e64 v107, 4, v255\n"
    "v_add_u32 v107, 256, v107\n"
    "v_mul_u32_u24 v108, 205, v107\n"
    "v_lshrrev_b32 v108, 11, v108\n"
    "v_mul_u32_u24 v109, 10, v108\n"
    "v_sub_u32 v107, v107, v109\n"
    "v_lshrrev_b32 v109, 1, v108\n"
    "v_and_b32 v109, 4, v109\n"
    "v_xor_b32 v108, v108, v109\n"
    "v_mul_u32_u24 v108, 0x500, v108\n"
    "v_lshl_add_u32 v174, v107, 4, v108\n"
    "s_waitcnt vmcnt(6)\n"
    "s_waitcnt lgkmcnt(0)\n"
    "ds_read_b64_tr_b16 v[0:1], v90\n"
    "ds_read_b64_tr_b16 v[2:3], v90 offset:160\n"
    "ds_read_b64_tr_b16 v[4:5], v90 offset:32\n"
    "ds_read_b64_tr_b16 v[6:7], v90 offset:192\n"
    "ds_read_b64_tr_b16 v[8:9], v90 offset:1280\n"
    "ds_read_b64_tr_b16 v[10:11], v90 offset:1440\n"
    "ds_read_b64_tr_b16 v[12:13], v90 offset:1312\n"
    "ds_read_b64_tr_b16 v[14:15], v90 offset:1472\n"
    "ds_read_b64_tr_b16 v[16:17], v90 offset:2560\n"
    "ds_read_b64_tr_b16 v[18:19], v90 offset:2720\n"
    "ds_read_b64_tr_b16 v[20:21], v90 offset:2592\n"
    "ds_read_b64_tr_b16 v[22:23], v90 offset:2752\n"
    "ds_read_b64_tr_b16 v[24:25], v90 offset:3840\n"
    "ds_read_b64_tr_b16 v[26:27], v90 offset:4000\n"
    "ds_read_b64_tr_b16 v[28:29], v90 offset:3872\n"
    "ds_read_b64_tr_b16 v[30:31], v90 offset:4032\n"
    "ds_read_b64_tr_b16 v[32:33], v90 offset:5120\n"
    "ds_read_b64_tr_b16 v[34:35], v90 offset:5280\n"
    "ds_read_b64_tr_b16 v[36:37], v90 offset:5152\n"
    "ds_read_b64_tr_b16 v[38:39], v90 offset:5312\n"
    "s_mul_i32 s57, s45, 0x4c957f2d\n"
    "s_mul_hi_u32 s58, s44, 0x4c957f2d\n"
    "s_add_u32 s57, s57, s58\n"
    "s_mul_i32 s58, s44, 0x5851f42d\n"
    "s_add_u32 s57, s57, s58\n"
    "s_mul_i32 s56, s44, 0x4c957f2d\n"
    "s_add_u32 s44, s56, 0xf767814f\n"
    "s_addc_u32 s45, s57, 0x14057b7e\n"
    "s_cmp_ge_u32 s48, 4\n"
    "s_cbranch_scc1 .Lk_v6\n"
    "s_waitcnt lgkmcnt(15)\n"
    "v_mfma_f32_16x16x32_bf16 v[68:71], v[0:3], v[84:87], 0\n"
    "v_mfma_f32_16x16x32_bf16 v[72:75], v[4:7], v[84:87], 0\n"
    "s_waitcnt lgkmcnt(14)\n"
    "v_mfma_f32_16x16x32_bf16 v[68:71], v[8:11], v[84:87], v[68:71]\n"
    "s_waitcnt lgkmcnt(12)\n"
    "v_mfma_f32_16x16x32_bf16 v[72:75], v[12:15], v[84:87], v[72:75]\n"
    "s_waitcnt lgkmcnt(10)\n"
    "v_mfma_f32_16x16x32_bf16 v[68:71], v[16:19], v[84:87], v[68:71]\n"
    "s_waitcnt lgkmcnt(8)\n"
    "v_mfma_f32_16x16x32_bf16 v[72:75], v[20:23], v[84:87], v[72:75]\n"
    "s_waitcnt lgkmcnt(6)\n"
    "v_mfma_f32_16x16x32_bf16 v[68:71], v[24:27], v[84:87], v[68:71]\n"
    "s_waitcnt lgkmcnt(4)\n"
    "v_mfma_f32_16x16x32_bf16 v[72:75], v[28:31], v[84:87], v[72:75]\n"
    "s_waitcnt lgkmcnt(2)\n"
    "v_mfma_f32_16x16x32_bf16 v[68:71], v[32:35], v[84:87], v[68:71]\n"
    "s_waitcnt lgkmcnt(0)\n"
    "v_mfma_f32_16x16x32_bf16 v[72:75], v[36:39], v[84:87], v[72:75]\n"
    "s_nop 5\n"
    "ds_write_b128 v92, v[68:71] offset:2048\n"
    "s_nop 0\n"
    "ds_write_b128 v92, v[72:75] offset:3072\n"
    "ds_add_u32 v254, v253 offset:4\n"
    "s_cmp_lg_u32 s52, 0\n"
    "s_cbranch_scc1 .Lk_L8\n"
    "s_waitcnt lgkmcnt(0)\n"
    ".Lk_w9:\n"
    "ds_read_b32 v252, v254 offset:4\n"
    "s_waitcnt lgkmcnt(0)\n"
    "v_readfirstlane_b32 s71, v252\n"
    "s_cmp_ge_u32 s71, 4\n"
    "s_cbranch_scc1 .Lk_d10\n"
    "s_sub_u32 s54, s54, 1\n"
    "s_cbranch_scc0 .Lk_w9\n"
    "s_mov_b32 s54, 0\n"
    ".Lk_d10:\n"
    "ds_read_b128 v[116:119], v92 offset:8448\n"
    "ds_read_b128 v[120:123], v92 offset:9472\n"
    "ds_read_b128 v[124:127], v92 offset:14848\n"
    "ds_read_b128 v[128:131], v92 offset:15872\n"
    "ds_read_b128 v[132:135], v92 offset:21248\n"
    "ds_read_b128 v[136:139], v92 offset:22272\n"
    "s_waitcnt lgkmcnt(5)\n"
    "v_pk_add_f32 v[68:69], v[68:69], v[116:117]\n"
    "v_pk_add_f32 v[70:71], v[70:71], v[118:119]\n"
    "s_waitcnt lgkmcnt(4)\n"
    "v_pk_add_f32 v[72:73], v[72:73], v[120:121]\n"
    "v_pk_add_f32 v[74:75], v[74:75], v[122:123]\n"
    "s_waitcnt lgkmcnt(3)\n"
    "v_pk_add_f32 v[68:69], v[68:69], v[124:125]\n"
    "v_pk_add_f32 v[70:71], v[70:71], v[126:127]\n"
    "s_waitcnt lgkmcnt(2)\n"
    "v_pk_add_f32 v[72:73], v[72:73], v[128:129]\n"
    "v_pk_add_f32 v[74:75], v[74:75], v[130:131]\n"
    "s_waitcnt lgkmcnt(1)\n"
    "v_pk_add_f32 v[68:69], v[68:69], v[132:133]\n"
    "v_pk_add_f32 v[70:71], v[70:71], v[134:135]\n"
    "s_waitcnt lgkmcnt(0)\n"
    "v_pk_add_f32 v[72:73], v[72:73], v[136:137]\n"
    "v_pk_add_f32 v[74:75], v[74:75], v[138:139]\n"
    ".Lk_L8:\n"
    "s_waitcnt vmcnt(5)\n"
    "ds_read_b128 v[40:43], v91\n"
    "s_waitcnt vmcnt(4)\n"
    "ds_read_b128 v[44:47], v91 offset:1024\n"
    "s_waitcnt lgkmcnt(1)\n"
    "v_mfma_f32_16x16x32_bf16 v[80:83], v[40:43], v[40:43], 0\n"
    "v_mfma_f32_16x16x32_bf16 v[76:79], v[84:87], v[40:43], 0\n"
    "v_mfma_f32_16x16x32_bf16 v[60:63], v[0:3], v[40:43], 0\n"
    "v_mfma_f32_16x16x32_bf16 v[64:67], v[4:7], v[40:43], 0\n"
    "s_waitcnt vmcnt(3)\n"
    "ds_read_b128 v[48:51], v91 offset:2048\n"
    "s_waitcnt lgkmcnt(1)\n"
    "v_mfma_f32_16x16x32_bf16 v[80:83], v[44:47], v[44:47], v[80:83]\n"
    "v_mfma_f32_16x16x32_bf16 v[76:79], v[84:87], v[44:47], v[76:79]\n"
    "v_mfma_f32_16x16x32_bf16 v[60:63], v[8:11], v[44:47], v[60:63]\n"
    "v_mfma_f32_16x16x32_bf16 v[64:67], v[12:15], v[44:47], v[64:67]\n"
    "s_waitcnt vmcnt(2)\n"
    "ds_read_b128 v[52:55], v91 offset:3072\n"
    "s_waitcnt lgkmcnt(1)\n"
    "v_mfma_f32_16x16x32_bf16 v[80:83], v[48:51], v[48:51], v[80:83]\n"
    "v_mfma_f32_16x16x32_bf16 v[76:79], v[84:87], v[48:51], v[76:79]\n"
    "v_mfma_f32_16x16x32_bf16 v[60:63], v[16:19], v[48:51], v[60:63]\n"
    "v_mfma_f32_16x16x32_bf16 v[64:67], v[20:23], v[48:51], v[64:67]\n"
    "s_waitcnt vmcnt(1)\n"
    "ds_read_b128 v[56:59], v91 offset:4096\n"
    "ds_add_u32 v254, v253 offset:0\n"
    "s_waitcnt lgkmcnt(2)\n"
    "v_mfma_f32_16x16x32_bf16 v[80:83], v[52:55], v[52:55], v[80:83]\n"
    "v_mfma_f32_16x16x32_bf16 v[76:79], v[84:87], v[52:55], v[76:79]\n"
    "v_mfma_f32_16x16x32_bf16 v[60:63], v[24:27], v[52:55], v[60:63]\n"
    "v_mfma_f32_16x16x32_bf16 v[64:67], v[28:31], v[52:55], v[64:67]\n"
    "s_waitcnt lgkmcnt(1)\n"
    "v_mfma_f32_16x16x32_bf16 v[80:83], v[56:59], v[56:59], v[80:83]\n"
    "v_mfma_f32_16x16x32_bf16 v[76:79], v[84:87], v[56:59], v[76:79]\n"
    "v_mfma_f32_16x16x32_bf16 v[60:63], v[32:35], v[56:59], v[60:63]\n"
    "v_mfma_f32_16x16x32_bf16 v[64:67], v[36:39], v[56:59], v[64:67]\n"
    "s_branch .Lk_j7\n"
    ".Lk_v6:\n"
    "s_waitcnt lgkmcnt(15)\n"
    "v_mfma_f32_16x16x32_bf16 v[68:71], v[84:87], v[0:3], 0\n"
    "v_mfma_f32_16x16x32_bf16 v[72:75], v[84:87], v[4:7], 0\n"
    "s_waitcnt lgkmcnt(14)\n"
    "v_mfma_f32_16x16x32_bf16 v[68:71], v[84:87], v[8:11], v[68:71]\n"
    "s_waitcnt lgkmcnt(12)\n"
    "v_mfma_f32_16x16x32_bf16 v[72:75], v[84:87], v[12:15], v[72:75]\n"
    "s_waitcnt lgkmcnt(10)\n"
    "v_mfma_f32_16x16x32_bf16 v[68:71], v[84:87], v[16:19], v[68:71]\n"
    "s_waitcnt lgkmcnt(8)\n"
    "v_mfma_f32_16x16x32_bf16 v[72:75], v[84:87], v[20:23], v[72:75]\n"
    "s_waitcnt lgkmcnt(6)\n"
    "v_mfma_f32_16x16x32_bf16 v[68:71], v[84:87], v[24:27], v[68:71]\n"
    "s_waitcnt lgkmcnt(4)\n"
    "v_mfma_f32_16x16x32_bf16 v[72:75], v[84:87], v[28:31], v[72:75]\n"
    "s_waitcnt lgkmcnt(2)\n"
    "v_mfma_f32_16x16x32_bf16 v[68:71], v[84:87], v[32:35], v[68:71]\n"
    "s_waitcnt lgkmcnt(0)\n"
    "v_mfma_f32_16x16x32_bf16 v[72:75], v[84:87], v[36:39], v[72:75]\n"
    "s_nop 5\n"
    "ds_write_b128 v92, v[68:71] offset:2048\n"
    "s_nop 0\n"
    "ds_write_b128 v92, v[72:75] offset:3072\n"
    "ds_add_u32 v254, v253 offset:4\n"
    "s_cmp_lg_u32 s52, 0\n"
    "s_cbranch_scc1 .Lk_L11\n"
    "s_waitcnt lgkmcnt(0)\n"
    ".Lk_w12:\n"
    "ds_read_b32 v252, v254 offset:4\n"
    "s_waitcnt lgkmcnt(0)\n"
    "v_readfirstlane_b32 s71, v252\n"
    "s_cmp_ge_u32 s71, 4\n"
    "s_cbranch_scc1 .Lk_d13\n"
    "s_sub_u32 s54, s54, 1\n"
    "s_cbranch_scc0 .Lk_w12\n"
    "s_mov_b32 s54, 0\n"
    ".Lk_d13:\n"
    "ds_read_b128 v[116:119], v92 offset:8448\n"
    "ds_read_b128 v[120:123], v92 offset:9472\n"
    "ds_read_b128 v[124:127], v92 offset:14848\n"
    "ds_read_b128 v[128:131], v92 offset:15872\n"
    "ds_read_b128 v[132:135], v92 offset:21248\n"
    "ds_read_b128 v[136:139], v92 offset:22272\n"
    "s_waitcnt lgkmcnt(5)\n"
    "v_pk_add_f32 v[68:69], v[68:69], v[116:117]\n"
    "v_pk_add_f32 v[70:71], v[70:71], v[118:119]\n"
    "s_waitcnt lgkmcnt(4)\n"
    "v_pk_add_f32 v[72:73], v[72:73], v[120:121]\n"
    "v_pk_add_f32 v[74:75], v[74:75], v[122:123]\n"
    "s_waitcnt lgkmcnt(3)\n"
    "v_pk_add_f32 v[68:69], v[68:69], v[124:125]\n"
    "v_pk_add_f32 v[70:71], v[70:71], v[126:127]\n"
    "s_waitcnt lgkmcnt(2)\n"
    "v_pk_add_f32 v[72:73], v[72:73], v[128:129]\n"
    "v_pk_add_f32 v[74:75], v[74:75], v[130:131]\n"
    "s_waitcnt lgkmcnt(1)\n"
    "v_pk_add_f32 v[68:69], v[68:69], v[132:133]\n"
    "v_pk_add_f32 v[70:71], v[70:71], v[134:135]\n"
    "s_waitcnt lgkmcnt(0)\n"
    "v_pk_add_f32 v[72:73], v[72:73], v[136:137]\n"
    "v_pk_add_f32 v[74:75], v[74:75], v[138:139]\n"
    ".Lk_L11:\n"
    "s_waitcnt vmcnt(5)\n"
    "ds_read_b128 v[40:43], v91\n"
    "s_waitcnt vmcnt(4)\n"
    "ds_read_b128 v[44:47], v91 offset:1024\n"
    "s_waitcnt lgkmcnt(1)\n"
    "v_mfma_f32_16x16x32_bf16 v[80:83], v[40:43], v[40:43], 0\n"
    "v_mfma_f32_16x16x32_bf16 v[76:79], v[84:87], v[40:43], 0\n"
    "v_mfma_f32_16x16x32_bf16 v[60:63], v[40:43], v[0:3], 0\n"
    "v_mfma_f32_16x16x32_bf16 v[64:67], v[40:43], v[4:7], 0\n"
    "s_waitcnt vmcnt(3)\n"
    "ds_read_b128 v[48:51], v91 offset:2048\n"
    "s_waitcnt lgkmcnt(1)\n"
    "v_mfma_f32_16x16x32_bf16 v[80:83], v[44:47], v[44:47], v[80:83]\n"
    "v_mfma_f32_16x16x32_bf16 v[76:79], v[84:87], v[44:47], v[76:79]\n"
    "v_mfma_f32_16x16x32_bf16 v[60:63], v[44:47], v[8:11], v[60:63]\n"
    "v_mfma_f32_16x16x32_bf16 v[64:67], v[44:47], v[12:15], v[64:67]\n"
    "s_waitcnt vmcnt(2)\n"
    "ds_read_b128 v[52:55], v91 offset:3072\n"
    "s_waitcnt lgkmcnt(1)\n"
    "v_mfma_f32_16x16x32_bf16 v[80:83], v[48:51], v[48:51], v[80:83]\n"
    "v_mfma_f32_16x16x32_bf16 v[76:79], v[84:87], v[48:51], v[76:79]\n"
    "v_mfma_f32_16x16x32_bf16 v[60:63], v[48:51], v[16:19], v[60:63]\n"
    "v_mfma_f32_16x16x32_bf16 v[64:67], v[48:51], v[20:23], v[64:67]\n"
    "s_waitcnt vmcnt(1)\n"
    "ds_read_b128 v[56:59], v91 offset:4096\n"
    "ds_add_u32 v254, v253 offset:0\n"
    "s_waitcnt lgkmcnt(2)\n"
    "v_mfma_f32_16x16x32_bf16 v[80:83], v[52:55], v[52:55], v[80:83]\n"
    "v_mfma_f32_16x16x32_bf16 v[76:79], v[84:87], v[52:55], v[76:79]\n"
    "v_mfma_f32_16x16x32_bf16 v[60:63], v[52:55], v[24:27], v[60:63]\n"
    "v_mfma_f32_16x16x32_bf16 v[64:67], v[52:55], v[28:31], v[64:67]\n"
    "s_waitcnt lgkmcnt(1)\n"
    "v_mfma_f32_16x16x32_bf16 v[80:83], v[56:59], v[56:59], v[80:83]\n"
    "v_mfma_f32_16x16x32_bf16 v[76:79], v[84:87], v[56:59], v[76:79]\n"
    "v_mfma_f32_16x16x32_bf16 v[60:63], v[56:59], v[32:35], v[60:63]\n"
    "v_mfma_f32_16x16x32_bf16 v[64:67], v[56:59], v[36:39], v[64:67]\n"
    ".Lk_j7:\n"
    "s_nop 5\n"
    "v_cndmask_b32 v77, v80, v81, s[78:79]\n"
    "v_cndmask_b32 v77, v77, v82, s[80:81]\n"
    "v_cndmask_b32 v77, v77, v83, s[82:83]\n"
    "s_mov_b64 exec, s[84:85]\n"
    "ds_write_b64 v100, v[76:77]\n"
    "s_mov_b64 exec, -1\n"
    "s_cmp_eq_u32 s52, 0\n"
    "s_cbranch_scc1 .Lk_n014\n"
    "ds_write_b128 v92, v[60:63]\n"
    "ds_write_b128 v92, v[64:67] offset:1024\n"
    "ds_add_u32 v254, v253 offset:8\n"
    "s_cmp_eq_u32 s52, 3\n"
    "s_cbranch_scc1 .Lk_h316\n"
    "s_branch .Lk_pre15\n"
    ".Lk_h316:\n"
    "s_mov_b32 s40, s18\n"
    "s_and_b32 s41, s19, 0xffff\n"
    "s_mov_b32 s42, 819200\n"
    "s_mov_b32 s43, 0x20000\n"
    "s_mul_i32 s56, s48, 0xa0\n"
    "s_setprio 1\n"
    "s_waitcnt lgkmcnt(0)\n"
    ".Lk_w17:\n"
    "ds_read_b32 v252, v254 offset:20\n"
    "s_waitcnt lgkmcnt(0)\n"
    "v_readfirstlane_b32 s71, v252\n"
    "s_cmp_ge_u32 s71, 3\n"
    "s_cbranch_scc1 .Lk_d18\n"
    "s_sleep 1\n"
    "s_sub_u32 s54, s54, 1\n"
    "s_cbranch_scc0 .Lk_w17\n"
    "s_mov_b32 s54, 0\n"
    ".Lk_d18:\n"
    "s_mov_b32 m0, 0\n"
    "s_nop 0\n"
    "buffer_load_dwordx4 v170, s[40:43], s56 offen lds\n"
    "s_mov_b32 m0, 1024\n"
    "s_nop 0\n"
    "buffer_load_dwordx4 v171, s[40:43], s56 offen lds\n"
    "s_mov_b32 m0, 2048\n"
    "s_nop 0\n"
    "buffer_load_dwordx4 v172, s[40:43], s56 offen lds\n"
    "s_mov_b32 m0, 3072\n"
    "s_nop 0\n"
    "buffer_load_dwordx4 v173, s[40:43], s56 offen lds\n"
    "s_mov_b32 m0, 4096\n"
    "s_nop 0\n"
    "buffer_load_dwordx4 v174, s[40:43], s56 offen lds\n"
    "s_mov_b32 m0, 5120\n"
    "s_add_u32 s56, s56, 40960\n"
    "buffer_load_dwordx4 v170, s[40:43], s56 offen lds\n"
    "s_mov_b32 m0, 6144\n"
    "s_nop 0\n"
    "buffer_load_dwordx4 v171, s[40:43], s56 offen lds\n"
    "s_mov_b32 m0, 7168\n"
    "s_nop 0\n"
    "buffer_load_dwordx4 v172, s[40:43], s56 offen lds\n"
    "s_mov_b32 m0, 8192\n"
    "s_nop 0\n"
    "buffer_load_dwordx4 v173, s[40:43], s56 offen lds\n"
    "s_mov_b32 m0, 9216\n"
    "s_nop 0\n"
    "buffer_load_dwordx4 v174, s[40:43], s56 offen lds\n"
    "s_mov_b32 m0, 10240\n"
    "s_add_u32 s56, s56, 40960\n"
    "buffer_load_dwordx4 v170, s[40:43], s56 offen lds\n"
    "s_mov_b32 m0, 11264\n"
    "s_nop 0\n"
    "buffer_load_dwordx4 v171, s[40:43], s56 offen lds\n"
    "s_mov_b32 m0, 12288\n"
    "s_nop 0\n"
    "buffer_load_dwordx4 v172, s[40:43], s56 offen lds\n"
    "s_mov_b32 m0, 13312\n"
    "s_nop 0\n"
    "buffer_load_dwordx4 v173, s[40:43], s56 offen lds\n"
    "s_mov_b32 m0, 14336\n"
    "s_nop 0\n"
    "buffer_load_dwordx4 v174, s[40:43], s56 offen lds\n"
    "s_mov_b32 m0, 15360\n"
    "s_add_u32 s56, s56, 40960\n"
    "buffer_load_dwordx4 v170, s[40:43], s56 offen lds\n"
    "s_mov_b32 m0, 16384\n"
    "s_nop 0\n"
    "buffer_load_dwordx4 v171, s[40:43], s56 offen lds\n"
    "s_mov_b32 m0, 17408\n"
    "s_nop 0\n"
    "buffer_load_dwordx4 v172, s[40:43], s56 offen lds\n"
    "s_mov_b32 m0, 18432\n"
    "s_nop 0\n"
    "buffer_load_dwordx4 v173, s[40:43], s56 offen lds\n"
    "s_mov_b32 m0, 19456\n"
    "s_nop 0\n"
    "buffer_load_dwordx4 v174, s[40:43], s56 offen lds\n"
    "s_mov_b32 m0, 20480\n"
    "s_add_u32 s56, s56, 40960\n"
    "buffer_load_dwordx4 v170, s[40:43], s56 offen lds\n"
    "s_mov_b32 m0, 21504\n"
    "s_nop 0\n"
    "buffer_load_dwordx4 v171, s[40:43], s56 offen lds\n"
    "s_mov_b32 m0, 22528\n"
    "s_nop 0\n"
    "buffer_load_dwordx4 v172, s[40:43], s56 offen lds\n"
    "s_mov_b32 m0, 23552\n"
    "s_nop 0\n"
    "buffer_load_dwordx4 v173, s[40:43], s56 offen lds\n"
    "s_mov_b32 m0, 24576\n"
    "s_nop 0\n"
    "buffer_load_dwordx4 v174, s[40:43], s56 offen lds\n"
    "s_mov_b32 m0, 25600\n"
    "s_add_u32 s56, s56, 40960\n"
    "buffer_load_dwordx4 v170, s[40:43], s56 offen lds\n"
    "s_mov_b32 m0, 26624\n"
    "s_nop 0\n"
    "buffer_load_dwordx4 v171, s[40:43], s56 offen lds\n"
    "s_mov_b32 m0, 27648\n"
    "s_nop 0\n"
    "buffer_load_dwordx4 v172, s[40:43], s56 offen lds\n"
    "s_mov_b32 m0, 28672\n"
    "s_nop 0\n"
    "buffer_load_dwordx4 v173, s[40:43], s56 offen lds\n"
    "s_mov_b32 m0, 29696\n"
    "s_nop 0\n"
    "buffer_load_dwordx4 v174, s[40:43], s56 offen lds\n"
    "s_mov_b32 m0, 30720\n"
    "s_add_u32 s56, s56, 40960\n"
    "buffer_load_dwordx4 v170, s[40:43], s56 offen lds\n"
    "s_mov_b32 m0, 31744\n"
    "s_nop 0\n"
    "buffer_load_dwordx4 v171, s[40:43], s56 offen lds\n"
    "s_mov_b32 m0, 32768\n"
    "s_nop 0\n"
    "buffer_load_dwordx4 v172, s[40:43], s56 offen lds\n"
    "s_mov_b32 m0, 33792\n"
    "s_nop 0\n"
    "buffer_load_dwordx4 v173, s[40:43], s56 offen lds\n"
    "s_mov_b32 m0, 34816\n"
    "s_nop 0\n"
    "buffer_load_dwordx4 v174, s[40:43], s56 offen lds\n"
    "s_mov_b32 m0, 35840\n"
    "s_add_u32 s56, s56, 40960\n"
    "buffer_load_dwordx4 v170, s[40:43], s56 offen lds\n"
    "s_mov_b32 m0, 36864\n"
    "s_nop 0\n"
    "buffer_load_dwordx4 v171, s[40:43], s56 offen lds\n"
    "s_mov_b32 m0, 37888\n"
    ".Lk_pad0:\n"
    "s_cbranch_execz .Lk_tramp\n"
    "s_nop 0\n"
    "buffer_load_dwordx4 v172, s[40:43], s56 offen lds\n"
    "s_mov_b32 m0, 38912\n"
    "s_nop 0\n"
    "buffer_load_dwordx4 v173, s[40:43], s56 offen lds\n"
    "s_mov_b32 m0, 39936\n"
    "s_nop 0\n"
    "buffer_load_dwordx4 v174, s[40:43], s56 offen lds\n"
    "s_mov_b32 m0, 40960\n"
    "s_add_u32 s56, s56, 40960\n"
    "buffer_load_dwordx4 v170, s[40:43], s56 offen lds\n"
    "s_mov_b32 m0, 41984\n"
    "s_nop 0\n"
    "buffer_load_dwordx4 v171, s[40:43], s56 offen lds\n"
    "s_mov_b32 m0, 43008\n"
    "s_nop 0\n"
    "buffer_load_dwordx4 v172, s[40:43], s56 offen lds\n"
    "s_mov_b32 m0, 44032\n"
    "s_nop 0\n"
    "buffer_load_dwordx4 v173, s[40:43], s56 offen lds\n"
    "s_mov_b32 m0, 45056\n"
    "s_nop 0\n"
    "buffer_load_dwordx4 v174, s[40:43], s56 offen lds\n"
    "s_mov_b32 m0, 46080\n"
    "s_add_u32 s56, s56, 40960\n"
    "buffer_load_dwordx4 v170, s[40:43], s56 offen lds\n"
    "s_mov_b32 m0, 47104\n"
    "s_nop 0\n"
    "buffer_load_dwordx4 v171, s[40:43], s56 offen lds\n"
    "s_mov_b32 m0, 48128\n"
    "s_nop 0\n"
    "buffer_load_dwordx4 v172, s[40:43], s56 offen lds\n"
    "s_mov_b32 m0, 49152\n"
    "s_nop 0\n"
    "buffer_load_dwordx4 v173, s[40:43], s56 offen lds\n"
    "s_mov_b32 m0, 50176\n"
    "s_nop 0\n"
    "buffer_load_dwordx4 v174, s[40:43], s56 offen lds\n"
    "s_mov_b32 m0, 51200\n"
    "s_add_u32 s56, s56, 40960\n"
    "buffer_load_dwordx4 v170, s[40:43], s56 offen lds\n"
    "s_mov_b32 m0, 52224\n"
    "s_nop 0\n"
    "buffer_load_dwordx4 v171, s[40:43], s56 offen lds\n"
    "s_mov_b32 m0, 53248\n"
    "s_nop 0\n"
    "buffer_load_dwordx4 v172, s[40:43], s56 offen lds\n"
    "s_mov_b32 m0, 54272\n"
    "s_nop 0\n"
    "buffer_load_dwordx4 v173, s[40:43], s56 offen lds\n"
    "s_mov_b32 m0, 55296\n"
    "s_nop 0\n"
    "buffer_load_dwordx4 v174, s[40:43], s56 offen lds\n"
    "s_mov_b32 m0, 56320\n"
    "s_add_u32 s56, s56, 40960\n"
    "buffer_load_dwordx4 v170, s[40:43], s56 offen lds\n"
    "s_waitcnt vmcnt(36)\n"
    "ds_add_u32 v254, v253 offset:16\n"
    "s_mov_b32 m0, 57344\n"
    "s_nop 0\n"
    "buffer_load_dwordx4 v171, s[40:43], s56 offen lds\n"
    "s_mov_b32 m0, 58368\n"
    "s_nop 0\n"
    "buffer_load_dwordx4 v172, s[40:43], s56 offen lds\n"
    "s_mov_b32 m0, 59392\n"
    "s_nop 0\n"
    "buffer_load_dwordx4 v173, s[40:43], s56 offen lds\n"
    "s_mov_b32 m0, 60416\n"
    "s_nop 0\n"
    "buffer_load_dwordx4 v174, s[40:43], s56 offen lds\n"
    "s_mov_b32 m0, 61440\n"
    "s_add_u32 s56, s56, 40960\n"
    "buffer_load_dwordx4 v170, s[40:43], s56 offen lds\n"
    "s_mov_b32 m0, 62464\n"
    "s_nop 0\n"
    "buffer_load_dwordx4 v171, s[40:43], s56 offen lds\n"
    "s_mov_b32 m0, 63488\n"
    "s_nop 0\n"
    "buffer_load_dwordx4 v172, s[40:43], s56 offen lds\n"
    "s_mov_b32 m0, 64512\n"
    "s_nop 0\n"
    "buffer_load_dwordx4 v173, s[40:43], s56 offen lds\n"
    "s_mov_b32 m0, 65536\n"
    "s_nop 0\n"
    "buffer_load_dwordx4 v174, s[40:43], s56 offen lds\n"
    "s_mov_b32 m0, 66560\n"
    "s_add_u32 s56, s56, 40960\n"
    "buffer_load_dwordx4 v170, s[40:43], s56 offen lds\n"
    "s_mov_b32 m0, 67584\n"
    "s_nop 0\n"
    "buffer_load_dwordx4 v171, s[40:43], s56 offen lds\n"
    "s_mov_b32 m0, 68608\n"
    "s_nop 0\n"
    "buffer_load_dwordx4 v172, s[40:43], s56 offen lds\n"
    "s_mov_b32 m0, 69632\n"
    "s_nop 0\n"
    "buffer_load_dwordx4 v173, s[40:43], s56 offen lds\n"
    "s_mov_b32 m0, 70656\n"
    "s_nop 0\n"
    "buffer_load_dwordx4 v174, s[40:43], s56 offen lds\n"
    "s_mov_b32 m0, 71680\n"
    "s_add_u32 s56, s56, 40960\n"
    "buffer_load_dwordx4 v170, s[40:43], s56 offen lds\n"
    "s_mov_b32 m0, 72704\n"
    "s_nop 0\n"
    "buffer_load_dwordx4 v171, s[40:43], s56 offen lds\n"
    "s_mov_b32 m0, 73728\n"
    "s_nop 0\n"
    "buffer_load_dwordx4 v172, s[40:43], s56 offen lds\n"
    "s_mov_b32 m0, 74752\n"
    "s_nop 0\n"
    "buffer_load_dwordx4 v173, s[40:43], s56 offen lds\n"
    "s_mov_b32 m0, 75776\n"
    "s_nop 0\n"
    "buffer_load_dwordx4 v174, s[40:43], s56 offen lds\n"
    "s_mov_b32 m0, 76800\n"
    "s_add_u32 s56, s56, 40960\n"
    "buffer_load_dwordx4 v170, s[40:43], s56 offen lds\n"
    "s_waitcnt vmcnt(36)\n"
    "ds_add_u32 v254, v253 offset:16\n"
    "s_mov_b32 m0, 77824\n"
    "s_nop 0\n"
    "buffer_load_dwordx4 v171, s[40:43], s56 offen lds\n"
    "s_mov_b32 m0, 78848\n"
    "s_nop 0\n"
    "buffer_load_dwordx4 v172, s[40:43], s56 offen lds\n"
    "s_mov_b32 m0, 79872\n"
    "s_nop 0\n"
    "buffer_load_dwordx4 v173, s[40:43], s56 offen lds\n"
    "s_mov_b32 m0, 80896\n"
    "s_nop 0\n"
    "buffer_load_dwordx4 v174, s[40:43], s56 offen lds\n"
    "s_mov_b32 m0, 81920\n"
    "s_add_u32 s56, s56, 40960\n"
    "buffer_load_dwordx4 v170, s[40:43], s56 offen lds\n"
    "s_mov_b32 m0, 82944\n"
    "s_nop 0\n"
    "buffer_load_dwordx4 v171, s[40:43], s56 offen lds\n"
    "s_mov_b32 m0, 83968\n"
    "s_nop 0\n"
    "buffer_load_dwordx4 v172, s[40:43], s56 offen lds\n"
    "s_mov_b32 m0, 84992\n"
    "s_nop 0\n"
    "buffer_load_dwordx4 v173, s[40:43], s56 offen lds\n"
    "s_mov_b32 m0, 86016\n"
    "s_nop 0\n"
    "buffer_load_dwordx4 v174, s[40:43], s56 offen lds\n"
    "s_mov_b32 m0, 87040\n"
    "s_add_u32 s56, s56, 40960\n"
    "buffer_load_dwordx4 v170, s[40:43], s56 offen lds\n"
    "s_mov_b32 m0, 88064\n"
    "s_nop 0\n"
    "buffer_load_dwordx4 v171, s[40:43], s56 offen lds\n"
    "s_mov_b32 m0, 89088\n"
    "s_nop 0\n"
    "buffer_load_dwordx4 v172, s[40:43], s56 offen lds\n"
    "s_mov_b32 m0, 90112\n"
    "s_nop 0\n"
    "buffer_load_dwordx4 v173, s[40:43], s56 offen lds\n"
    "s_mov_b32 m0, 91136\n"
    "s_nop 0\n"
    "buffer_load_dwordx4 v174, s[40:43], s56 offen lds\n"
    "s_mov_b32 m0, 92160\n"
    "s_add_u32 s56, s56, 40960\n"
    "buffer_load_dwordx4 v170, s[40:43], s56 offen lds\n"
    "s_mov_b32 m0, 93184\n"
    "s_nop 0\n"
    "buffer_load_dwordx4 v171, s[40:43], s56 offen lds\n"
    "s_mov_b32 m0, 94208\n"
    "s_nop 0\n"
    "buffer_load_dwordx4 v172, s[40:43], s56 offen lds\n"
    "s_mov_b32 m0, 95232\n"
    "s_nop 0\n"
    "buffer_load_dwordx4 v173, s[40:43], s56 offen lds\n"
    "s_mov_b32 m0, 96256\n"
    "s_nop 0\n"
    "buffer_load_dwordx4 v174, s[40:43], s56 offen lds\n"
    "s_mov_b32 m0, 97280\n"
    "s_add_u32 s56, s56, 40960\n"
    "buffer_load_dwordx4 v170, s[40:43], s56 offen lds\n"
    "s_waitcnt vmcnt(36)\n"
    "ds_add_u32 v254, v253 offset:16\n"
    "s_mov_b32 m0, 98304\n"
    "s_nop 0\n"
    "buffer_load_dwordx4 v171, s[40:43], s56 offen lds\n"
    "s_mov_b32 m0, 99328\n"
    "s_nop 0\n"
    "buffer_load_dwordx4 v172, s[40:43], s56 offen lds\n"
    "s_mov_b32 m0, 100352\n"
    "s_nop 0\n"
    "buffer_load_dwordx4 v173, s[40:43], s56 offen lds\n"
    "s_mov_b32 m0, 101376\n"
    "s_nop 0\n"
    "buffer_load_dwordx4 v174, s[40:43], s56 offen lds\n"
    "ds_add_u32 v254, v253 offset:112\n"
    "s_waitcnt vmcnt(20)\n"
    "ds_add_u32 v254, v253 offset:16\n"
    "s_waitcnt vmcnt(0)\n"
    "ds_add_u32 v254, v253 offset:16\n"
    "s_setprio 2\n"
    "v_lshrrev_b32 v148, 1, v255\n"
    "v_add_u32 v148, 161024, v148\n"
    "s_waitcnt lgkmcnt(0)\n"
    ".Lk_w19:\n"
    "ds_read_b32 v252, v254 offset:100\n"
    "s_waitcnt lgkmcnt(0)\n"
    "v_readfirstlane_b32 s71, v252\n"
    "s_cmp_ge_u32 s71, 4\n"
    "s_cbranch_scc1 .Lk_d20\n"
    "s_sub_u32 s54, s54, 1\n"
    "s_cbranch_scc0 .Lk_w19\n"
    "s_mov_b32 s54, 0\n"
    ".Lk_d20:\n"
    "ds_read_b64 v[150:151], v148\n"
    "ds_read_b64 v[152:153], v148 offset:320\n"
    "ds_read_b64 v[154:155], v148 offset:640\n"
    "ds_read_b64 v[156:157], v148 offset:960\n"
    "s_waitcnt lgkmcnt(2)\n"
    "v_add_f32 v150, v150, v152\n"
    "s_waitcnt lgkmcnt(0)\n"
    "v_add_f32 v154, v154, v156\n"
    "v_add_f32 v151, v151, v153\n"
    "v_add_f32 v155, v155, v157\n"
    "v_add_f32 v150, v150, v154\n"
    "v_add_f32 v151, v151, v155\n"
    "ds_write_b64 v148, v[150:151] offset:1280\n"
    "ds_add_u32 v254, v253 offset:108\n"
    "s_branch .Lk_pre15\n"
    ".Lk_n014:\n"
    ".Lk_pad1:\n"
    "s_cbranch_execz .Lk_tramp\n"
    "s_waitcnt lgkmcnt(0)\n"
    ".Lk_w21:\n"
    "ds_read_b32 v252, v254 offset:8\n"
    "s_waitcnt lgkmcnt(0)\n"
    "v_readfirstlane_b32 s71, v252\n"
    "s_cmp_ge_u32 s71, 3\n"
    "s_cbranch_scc1 .Lk_d22\n"
    "s_sub_u32 s54, s54, 1\n"
    "s_cbranch_scc0 .Lk_w21\n"
    "s_mov_b32 s54, 0\n"
    ".Lk_d22:\n"
    "ds_read_b128 v[108:111], v103\n"
    "ds_read_b128 v[112:115], v103 offset:16\n"
    "ds_read_b128 v[116:119], v92 offset:6400\n"
    "ds_read_b128 v[120:123], v92 offset:7424\n"
    "ds_read_b128 v[124:127], v92 offset:12800\n"
    "ds_read_b128 v[128:131], v92 offset:13824\n"
    "ds_read_b128 v[132:135], v92 offset:19200\n"
    "ds_read_b128 v[136:139], v92 offset:20224\n"
    "s_waitcnt lgkmcnt(7)\n"
    "v_pk_add_f32 v[108:109], v[108:109], v[110:111]\n"
    "s_waitcnt lgkmcnt(6)\n"
    "v_pk_add_f32 v[112:113], v[112:113], v[114:115]\n"
    "v_pk_add_f32 v[108:109], v[108:109], v[112:113]\n"
    "v_mul_f32 v108, 0x3acccccd, v108\n"
    "s_mov_b32 s69, 0x3acccccd\n"
    "v_mul_f32_e64 v110, v108, v108\n"
    "v_fma_f32 v110, v109, s69, -v110\n"
    "v_max_f32_e64 v110, 0, v110\n"
    "v_add_f32 v110, 0x3727c5ac, v110\n"
    "v_rsq_f32 v110, v110\n"
    "s_waitcnt lgkmcnt(5)\n"
    "v_pk_add_f32 v[60:61], v[60:61], v[116:117]\n"
    "v_pk_add_f32 v[62:63], v[62:63], v[118:119]\n"
    "s_waitcnt lgkmcnt(4)\n"
    "v_pk_add_f32 v[64:65], v[64:65], v[120:121]\n"
    "v_pk_add_f32 v[66:67], v[66:67], v[122:123]\n"
    "s_waitcnt lgkmcnt(3)\n"
    "v_pk_add_f32 v[60:61], v[60:61], v[124:125]\n"
    "v_pk_add_f32 v[62:63], v[62:63], v[126:127]\n"
    "s_waitcnt lgkmcnt(2)\n"
    "v_pk_add_f32 v[64:65], v[64:65], v[128:129]\n"
    "v_pk_add_f32 v[66:67], v[66:67], v[130:131]\n"
    "s_waitcnt lgkmcnt(1)\n"
    "v_pk_add_f32 v[60:61], v[60:61], v[132:133]\n"
    "v_pk_add_f32 v[62:63], v[62:63], v[134:135]\n"
    "s_waitcnt lgkmcnt(0)\n"
    "v_pk_add_f32 v[64:65], v[64:65], v[136:137]\n"
    "v_pk_add_f32 v[66:67], v[66:67], v[138:139]\n"
    "s_cmp_ge_u32 s48, 4\n"
    "s_cbranch_scc1 .Lk_v23\n"
    "s_nop 0\n"
    "v_pk_fma_f32 v[60:61], v[108:109], v[68:69], v[60:61] op_sel_hi:[0,1,1] neg_lo:[1,0,0] neg_hi:[1,0,0]\n"
    "v_pk_fma_f32 v[62:63], v[108:109], v[70:71], v[62:63] op_sel_hi:[0,1,1] neg_lo:[1,0,0] neg_hi:[1,0,0]\n"
    "v_pk_fma_f32 v[64:65], v[108:109], v[72:73], v[64:65] op_sel_hi:[0,1,1] neg_lo:[1,0,0] neg_hi:[1,0,0]\n"
    "v_pk_fma_f32 v[66:67], v[108:109], v[74:75], v[66:67] op_sel_hi:[0,1,1] neg_lo:[1,0,0] neg_hi:[1,0,0]\n"
    "v_pk_mul_f32 v[60:61], v[110:111], v[60:61] op_sel_hi:[0,1]\n"
    "v_pk_mul_f32 v[62:63], v[110:111], v[62:63] op_sel_hi:[0,1]\n"
    "v_pk_mul_f32 v[64:65], v[110:111], v[64:65] op_sel_hi:[0,1]\n"
    "v_pk_mul_f32 v[66:67], v[110:111], v[66:67] op_sel_hi:[0,1]\n"
    "v_cvt_pk_bf16_f32 v116, v60, v61\n"
    "v_cvt_pk_bf16_f32 v117, v62, v63\n"
    "v_cvt_pk_bf16_f32 v118, v64, v65\n"
    "v_cvt_pk_bf16_f32 v119, v66, v67\n"
    "buffer_store_dwordx2 v[116:117], v101, s[4:7], s76 offen sc1\n"
    "buffer_store_dwordx2 v[118:119], v102, s[4:7], s76 offen sc1\n"
    "v_mov_b32 v120, 0\n"
    "v_mov_b32 v121, 0\n"
    "v_dot2c_f32_bf16 v120, v116, v116\n"
    "v_dot2c_f32_bf16 v121, v118, v118\n"
    "v_dot2c_f32_bf16 v120, v117, v117\n"
    "v_dot2c_f32_bf16 v121, v119, v119\n"
    "v_cmp_eq_u32 vcc, 0, v94\n"
    "s_nop 1\n"
    "v_cndmask_b32 v121, 0, v121, vcc\n"
    "v_add_f32 v120, v120, v121\n"
    "v_mov_b32 v121, v120\n"
    "s_nop 1\n"
    "v_permlane16_swap_b32 v120, v121\n"
    "v_add_f32 v120, v120, v121\n"
    "v_mov_b32 v121, v120\n"
    "s_nop 1\n"
    "v_permlane32_swap_b32 v120, v121\n"
    "v_add_f32 v120, v120, v121\n"
    "v_and_b32 v120, 0x7fffffff, v120\n"
    "s_nop 1\n"
    "v_max_u32_dpp v120, v120, v120 quad_perm:[1,0,3,2] row_mask:0xf bank_mask:0xf\n"
    "s_nop 1\n"
    "v_max_u32_dpp v120, v120, v120 quad_perm:[2,3,0,1] row_mask:0xf bank_mask:0xf\n"
    "s_nop 1\n"
    "v_max_u32_dpp v120, v120, v120 row_half_mirror row_mask:0xf bank_mask:0xf\n"
    "s_nop 1\n"
    "v_max_u32_dpp v120, v120, v120 row_mirror row_mask:0xf bank_mask:0xf\n"
    "s_branch .Lk_j24\n"
    ".Lk_v23:\n"
    "s_nop 0\n"
    "v_mov_b32_dpp v140, v108 quad_perm:[0,0,0,0] row_mask:0xf bank_mask:0xf\n"
    "v_mov_b32_dpp v144, v110 quad_perm:[0,0,0,0] row_mask:0xf bank_mask:0xf\n"
    "v_mov_b32_dpp v141, v108 quad_perm:[1,1,1,1] row_mask:0xf bank_mask:0xf\n"
    "v_mov_b32_dpp v145, v110 quad_perm:[1,1,1,1] row_mask:0xf bank_mask:0xf\n"
    "v_mov_b32_dpp v142, v108 quad_perm:[2,2,2,2] row_mask:0xf bank_mask:0xf\n"
    "v_mov_b32_dpp v146, v110 quad_perm:[2,2,2,2] row_mask:0xf bank_mask:0xf\n"
    "v_mov_b32_dpp v143, v108 quad_perm:[3,3,3,3] row_mask:0xf bank_mask:0xf\n"
    "v_mov_b32_dpp v147, v110 quad_perm:[3,3,3,3] row_mask:0xf bank_mask:0xf\n"
    "v_pk_fma_f32 v[60:61], v[140:141], v[68:69], v[60:61] neg_lo:[1,0,0] neg_hi:[1,0,0]\n"
    "v_pk_fma_f32 v[62:63], v[142:143], v[70:71], v[62:63] neg_lo:[1,0,0] neg_hi:[1,0,0]\n"
    "v_pk_fma_f32 v[64:65], v[140:141], v[72:73], v[64:65] neg_lo:[1,0,0] neg_hi:[1,0,0]\n"
    "v_pk_fma_f32 v[66:67], v[142:143], v[74:75], v[66:67] neg_lo:[1,0,0] neg_hi:[1,0,0]\n"
    "v_pk_mul_f32 v[60:61], v[144:145], v[60:61]\n"
    "v_pk_mul_f32 v[62:63], v[146:147], v[62:63]\n"
    "v_pk_mul_f32 v[64:65], v[144:145], v[64:65]\n"
    "v_pk_mul_f32 v[66:67], v[146:147], v[66:67]\n"
    "v_cmp_gt_u32 vcc, 4, v93\n"
    "v_cvt_pk_bf16_f32 v116, v60, v61\n"
    "v_cvt_pk_bf16_f32 v117, v62, v63\n"
    "v_cvt_pk_bf16_f32 v118, v64, v65\n"
    "v_cvt_pk_bf16_f32 v119, v66, v67\n"
    "buffer_store_dwordx2 v[116:117], v101, s[4:7], s76 offen sc1\n"
    "v_cndmask_b32 v118, v176, v118, vcc\n"
    "v_cndmask_b32 v119, v176, v119, vcc\n"
    "buffer_store_dwordx2 v[118:119], v102, s[4:7], s76 offen sc1\n"
    "v_mov_b32 v120, 0\n"
    ".Lk_j24:\n"
    "ds_add_u32 v254, v253 offset:12\n"
    "v_mov_b32 v242, s44\n"
    "v_mov_b32 v243, s45\n"
    "v_bfe_u32 v241, v255, 4, 3\n"
    "v_lshlrev_b32 v241, 7, v241\n"
    "s_lshl_b32 s56, s48, 7\n"
    "v_mov_b32_e64 v240, s56\n"
    "v_mov_b32 v244, 0x80000000\n"
    "v_cmp_eq_u32 vcc, 0, v255\n"
    "s_nop 1\n"
    "v_cndmask_b32 v240, v244, v240, vcc\n"
    "s_mov_b32 s94, 4096\n"
    "s_not_b32 s56, s44\n"
    "v_mov_b32 v246, s56\n"
    "v_cndmask_b32 v122, v246, v242, vcc\n"
    "v_cndmask_b32 v123, v120, v243, vcc\n"
    "v_lshrrev_b32 v124, 1, v255\n"
    "v_mov_b32 v125, 159936\n"
    "ds_write_b64 v125, v[242:243]\n"
    "s_mul_i32 s56, s47, 0x400\n"
    "s_mul_i32 s70, s48, 0x80\n"
    "s_add_u32 s56, s56, s70\n"
    "s_add_u32 s56, s56, 0x560000\n"
    "s_mov_b64 exec, 3\n"
    "s_waitcnt vmcnt(0)\n"
    "buffer_store_dwordx2 v[122:123], v124, s[4:7], s56 offen sc1\n"
    "s_mov_b64 exec, -1\n"
    ".Lk_pre15:\n"
    "s_mul_i32 s72, s47, 0x8\n"
    "s_mul_i32 s70, s50, 0x4\n"
    "s_add_u32 s72, s72, s70\n"
    "s_add_u32 s72, s72, s52\n"
    "s_lshr_b32 s56, s52, 1\n"
    "s_mul_i32 s77, s52, 0x1400\n"
    "s_mul_i32 s70, s56, 0xb800\n"
    "s_add_u32 s77, s77, s70\n"
    "s_add_u32 s77, s77, 0x16000\n"
    "v_lshrrev_b32 v134, 5, v255\n"
    "v_and_b32 v135, 16, v255\n"
    "v_lshrrev_b32_e64 v136, 2, v93\n"
    "v_lshl_add_u32 v137, v94, 3, v136\n"
    "v_lshl_add_u32 v137, v137, 5, s77\n"
    "v_lshl_add_u32 v132, v97, 3, v137\n"
    "v_lshrrev_b32_e64 v138, 1, v97\n"
    "v_lshl_add_u32 v137, v138, 10, v137\n"
    "v_and_b32_e64 v138, 1, v97\n"
    "v_lshl_add_u32 v131, v138, 4, v137\n"
    "v_lshrrev_b32_e64 v137, 1, v94\n"
    "v_lshl_add_u32 v133, v137, 11, v132\n"
    "s_cmp_ge_u32 s72, 108\n"
    "s_cbranch_scc1 .Lk_w225\n"
    "s_mul_i32 s56, s72, 19\n"
    "s_lshr_b32 s56, s56, 9\n"
    "s_mul_i32 s57, s49, 0x1b\n"
    "s_mul_i32 s70, s56, 0xffffffe5\n"
    "s_add_u32 s57, s57, s70\n"
    "s_add_u32 s57, s57, s72\n"
    "s_mul_i32 s73, s56, 0x87000\n"
    "s_mul_i32 s70, s57, 0x20\n"
    "s_add_u32 s73, s73, s70\n"
    "s_mul_i32 s75, s57, 0x5000\n"
    "s_mul_i32 s70, s56, 0x1400\n"
    "s_add_u32 s75, s75, s70\n"
    "s_add_u32 s75, s75, 0x128000\n"
    "s_mul_i32 s76, s56, 0x1b00\n"
    "s_mul_i32 s70, s57, 0x40\n"
    "s_add_u32 s76, s76, s70\n"
    "s_add_u32 s76, s76, 0x9000\n"
    "s_mov_b32 s74, 110592\n"
    "v_mul_u32_u24 v130, 3456, v134\n"
    "v_add_u32 v130, v130, v135\n"
    "s_mov_b32 s12, s26\n"
    "s_and_b32 s13, s27, 0xffff\n"
    "s_mov_b32 s14, 2211840\n"
    "s_mov_b32 s15, 0x20000\n"
    "s_branch .Lk_j28\n"
    ".Lk_w225:\n"
    "s_and_b32 s56, s72, 1\n"
    "s_add_u32 s56, s56, 0x0\n"
    "s_mul_i32 s70, s49, 0x2\n"
    "s_add_u32 s56, s56, s70\n"
    "s_mul_i32 s73, s56, 0xa0\n"
    "s_mov_b32 s74, 32\n"
    "s_cmp_ge_u32 s72, 216\n"
    "s_cbranch_scc1 .Lk_wo26\n"
    "s_sub_u32 s57, s72, 108\n"
    "s_lshr_b32 s57, s57, 1\n"
    "s_mul_i32 s75, s56, 0x43800\n"
    "s_mul_i32 s70, s57, 0x1400\n"
    "s_add_u32 s75, s75, s70\n"
    "s_add_u32 s75, s75, 0x344000\n"
    "s_lshr_b32 s58, s57, 3\n"
    "s_mul_i32 s58, s58, 11\n"
    "s_lshl_b32 s59, s57, 1\n"
    "s_add_u32 s58, s58, s59\n"
    "s_add_u32 s58, s58, 0\n"
    "s_sub_u32 s59, s59, 64\n"
    "s_mul_i32 s60, s59, 373\n"
    "s_lshr_b32 s60, s60, 12\n"
    "s_lshl_b32 s60, s60, 4\n"
    "s_add_u32 s59, s59, s60\n"
    "s_add_u32 s59, s59, 16\n"
    "s_cmp_lt_u32 s57, 32\n"
    "s_cselect_b32 s58, s58, s59\n"
    "s_sub_u32 s58, s58, 0\n"
    "s_lshl_b32 s61, s58, 4\n"
    "s_lshr_b32 s58, s57, 3\n"
    "s_mul_i32 s58, s58, 11\n"
    "s_lshl_b32 s59, s57, 1\n"
    "s_add_u32 s58, s58, s59\n"
    "s_add_u32 s58, s58, 1\n"
    "s_sub_u32 s59, s59, 63\n"
    "s_mul_i32 s60, s59, 373\n"
    "s_lshr_b32 s60, s60, 12\n"
    "s_lshl_b32 s60, s60, 4\n"
    "s_add_u32 s59, s59, s60\n"
    "s_add_u32 s59, s59, 16\n"
    "s_cmp_lt_u32 s57, 32\n"
    "s_cselect_b32 s58, s58, s59\n"
    "s_sub_u32 s58, s58, 1\n"
    "s_lshl_b32 s62, s58, 4\n"
    "v_add_u32 v136, s61, v134\n"
    "v_add_u32 v137, s62, v134\n"
    "v_cmp_gt_u32 vcc, 16, v134\n"
    "s_nop 1\n"
    "v_cndmask_b32 v136, v137, v136, vcc\n"
    "s_mov_b32 s12, s30\n"
    "s_and_b32 s13, s31, 0xffff\n"
    "s_mov_b32 s14, 2211840\n"
    "s_mov_b32 s15, 0x20000\n"
    "s_branch .Lk_rows27\n"
    ".Lk_wo26:\n"
    "s_sub_u32 s57, s72, 216\n"
    "s_lshr_b32 s57, s57, 1\n"
    "s_mul_i32 s75, s56, 0x19000\n"
    "s_mul_i32 s70, s57, 0x1400\n"
    "s_add_u32 s75, s75, s70\n"
    "s_add_u32 s75, s75, 0x60000\n"
    "s_mul_i32 s58, s57, 205\n"
    "s_lshr_b32 s58, s58, 10\n"
    "s_mul_i32 s59, s58, 5\n"
    "s_sub_u32 s59, s57, s59\n"
    "s_mul_i32 s58, s58, 160\n"
    "s_lshr_b32 s60, s59, 1\n"
    "s_and_b32 s61, s59, 1\n"
    "s_lshl_b32 s61, s61, 3\n"
    "s_cmp_eq_u32 s59, 4\n"
    "s_cselect_b64 vcc, -1, 0\n"
    "v_lshrrev_b32 v136, 3, v134\n"
    "v_and_b32 v137, 7, v134\n"
    "v_mov_b32 v138, s60\n"
    "v_lshrrev_b32 v140, 1, v136\n"
    "v_cndmask_b32 v138, v138, v140, vcc\n"
    "v_and_b32 v140, 1, v136\n"
    "v_lshrrev_b32 v141, 2, v137\n"
    "v_lshl_add_u32 v140, v140, 1, v141\n"
    "v_cndmask_b32 v136, v136, v140, vcc\n"
    "v_add_u32 v140, s61, v137\n"
    "v_and_b32 v141, 3, v137\n"
    "v_add_u32 v141, 16, v141\n"
    "v_cndmask_b32 v137, v140, v141, vcc\n"
    "v_cmp_lt_u32 vcc, 9, v137\n"
    "s_nop 1\n"
    "v_cndmask_b32 v140, 0, 30, vcc\n"
    "v_add3_u32 v137, v137, v140, s58\n"
    "v_mul_u32_u24 v138, 80, v138\n"
    "v_and_b32 v140, 1, v136\n"
    "v_mul_u32_u24_e64 v140, 20, v140\n"
    "v_add3_u32 v137, v137, v138, v140\n"
    "v_lshrrev_b32 v136, 1, v136\n"
    "v_mul_u32_u24 v136, 10, v136\n"
    "v_add_u32 v136, v136, v137\n"
    "s_mov_b32 s12, s24\n"
    "s_and_b32 s13, s25, 0xffff\n"
    "s_mov_b32 s14, 819200\n"
    "s_mov_b32 s15, 0x20000\n"
    ".Lk_rows27:\n"
    "v_mul_u32_u24 v130, 0x500, v136\n"
    "v_add_u32 v130, v130, v135\n"
    ".Lk_j28:\n"
    "v_add_u32 v139, s75, v255\n"
    "v_bfe_u32 v250, v255, 4, 2\n"
    "v_lshlrev_b32_e64 v250, 2, v250\n"
    "v_add_u32 v250, 159872, v250\n"
    "s_cmp_lg_u32 s52, 0\n"
    "s_cbranch_scc1 .Lk_k29\n"
    "s_mov_b32 s56, 5636096\n"
    "s_mov_b64 s[60:61], -1\n"
    "s_mov_b32 s57, 5644288\n"
    "s_mov_b64 s[62:63], -1\n"
    "s_mov_b32 s58, 5652480\n"
    "s_mov_b64 s[64:65], -1\n"
    "s_mov_b32 s59, 5660672\n"
    "s_mov_b64 s[66:67], -1\n"
    "s_waitcnt lgkmcnt(0)\n"
    ".Lk_w30:\n"
    "ds_read_b32 v252, v254 offset:112\n"
    "s_waitcnt lgkmcnt(0)\n"
    "v_readfirstlane_b32 s71, v252\n"
    "s_cmp_ge_u32 s71, 1\n"
    "s_cbranch_scc1 .Lk_d31\n"
    "s_sleep 1\n"
    "s_sub_u32 s54, s54, 1\n"
    "s_cbranch_scc0 .Lk_w30\n"
    "s_mov_b32 s54, 0\n"
    ".Lk_d31:\n"
    ".Lk_pa32:\n"
    "s_mov_b64 exec, s[60:61]\n"
    "buffer_load_dwordx4 v[148:151], v95, s[4:7], s56 offen sc1\n"
    "s_mov_b64 exec, s[62:63]\n"
    "buffer_load_dwordx4 v[152:155], v95, s[4:7], s57 offen sc1\n"
    "s_mov_b64 exec, s[64:65]\n"
    "buffer_load_dwordx4 v[156:159], v95, s[4:7], s58 offen sc1\n"
    "s_mov_b64 exec, s[66:67]\n"
    "buffer_load_dwordx4 v[160:163], v95, s[4:7], s59 offen sc1\n"
    "s_mov_b64 exec, -1\n"
    "s_waitcnt vmcnt(3)\n"
    "v_cmp_ne_u64 s[60:61], v[148:149], v[242:243]\n"
    "v_cmp_ne_u32 vcc, v150, v246\n"
    "s_or_b64 s[60:61], s[60:61], vcc\n"
    "s_waitcnt vmcnt(2)\n"
    "v_cmp_ne_u64 s[62:63], v[152:153], v[242:243]\n"
    "v_cmp_ne_u32 vcc, v154, v246\n"
    "s_or_b64 s[62:63], s[62:63], vcc\n"
    "s_waitcnt vmcnt(1)\n"
    "v_cmp_ne_u64 s[64:65], v[156:157], v[242:243]\n"
    "v_cmp_ne_u32 vcc, v158, v246\n"
    "s_or_b64 s[64:65], s[64:65], vcc\n"
    "s_waitcnt vmcnt(0)\n"
    "v_cmp_ne_u64 s[66:67], v[160:161], v[242:243]\n"
    "v_cmp_ne_u32 vcc, v162, v246\n"
    "s_or_b64 s[66:67], s[66:67], vcc\n"
    "v_max_u32_e64 v164, v151, v155\n"
    "v_max3_u32 v164, v164, v159, v163\n"
    "s_or_b64 vcc, s[60:61], s[62:63]\n"
    "s_or_b64 s[68:69], s[64:65], s[66:67]\n"
    "s_or_b64 vcc, vcc, s[68:69]\n"
    "s_cbranch_scc0 .Lk_pad33\n"
    "s_or_b32 s60, s60, 1\n"
    "s_or_b32 s62, s62, 1\n"
    "s_or_b32 s64, s64, 1\n"
    "s_or_b32 s66, s66, 1\n"
    "s_sub_u32 s54, s54, 1\n"
    "s_cbranch_scc0 .Lk_pa32\n"
    "s_mov_b32 s54, 0\n"
    ".Lk_pad33:\n"
    "s_mov_b32 s66, 196608\n"
    "s_mov_b32 s67, 102400\n"
    "s_mov_b32 m0, s67\n"
    "s_add_u32 s68, s66, 0\n"
    "s_nop 0\n"
    "buffer_load_dwordx4 v255, s[4:7], s68 offen sc1 lds\n"
    "buffer_load_dwordx4 v255, s[4:7], s68 offen offset:1024 sc1 lds\n"
    "buffer_load_dwordx4 v255, s[4:7], s68 offen offset:2048 sc1 lds\n"
    "buffer_load_dwordx4 v255, s[4:7], s68 offen offset:3072 sc1 lds\n"
    "s_add_u32 m0, s67, 4096\n"
    "s_add_u32 s68, s66, 4096\n"
    "buffer_load_dwordx4 v255, s[4:7], s68 offen sc1 lds\n"
    "buffer_load_dwordx4 v255, s[4:7], s68 offen offset:1024 sc1 lds\n"
    "buffer_load_dwordx4 v255, s[4:7], s68 offen offset:2048 sc1 lds\n"
    "buffer_load_dwordx4 v255, s[4:7], s68 offen offset:3072 sc1 lds\n"
    "s_add_u32 m0, s67, 8192\n"
    "s_add_u32 s68, s66, 8192\n"
    "buffer_load_dwordx4 v255, s[4:7], s68 offen sc1 lds\n"
    "buffer_load_dwordx4 v255, s[4:7], s68 offen offset:1024 sc1 lds\n"
    "buffer_load_dwordx4 v255, s[4:7], s68 offen offset:2048 sc1 lds\n"
    "buffer_load_dwordx4 v255, s[4:7], s68 offen offset:3072 sc1 lds\n"
    "s_add_u32 m0, s67, 12288\n"
    "s_add_u32 s68, s66, 12288\n"
    "buffer_load_dwordx4 v255, s[4:7], s68 offen sc1 lds\n"
    "buffer_load_dwordx4 v255, s[4:7], s68 offen offset:1024 sc1 lds\n"
    "buffer_load_dwordx4 v255, s[4:7], s68 offen offset:2048 sc1 lds\n"
    "buffer_load_dwordx4 v255, s[4:7], s68 offen offset:3072 sc1 lds\n"
    "s_add_u32 m0, s67, 16384\n"
    "s_add_u32 s68, s66, 16384\n"
    "buffer_load_dwordx4 v255, s[4:7], s68 offen sc1 lds\n"
    "buffer_load_dwordx4 v255, s[4:7], s68 offen offset:1024 sc1 lds\n"
    "buffer_load_dwordx4 v255, s[4:7], s68 offen offset:2048 sc1 lds\n"
    "buffer_load_dwordx4 v255, s[4:7], s68 offen offset:3072 sc1 lds\n"
    "s_add_u32 m0, s67, 20480\n"
    "s_add_u32 s68, s66, 20480\n"
    "buffer_load_dwordx4 v255, s[4:7], s68 offen sc1 lds\n"
    "buffer_load_dwordx4 v255, s[4:7], s68 offen offset:1024 sc1 lds\n"
    "ds_add_u32 v254, v253 offset:32\n"
    "v_mov_b32 v151, v164\n"
    "s_nop 1\n"
    "v_max_u32_dpp v151, v151, v151 quad_perm:[1,0,3,2] row_mask:0xf bank_mask:0xf\n"
    "s_nop 1\n"
    "v_max_u32_dpp v151, v151, v151 quad_perm:[2,3,0,1] row_mask:0xf bank_mask:0xf\n"
    "s_nop 1\n"
    "v_max_u32_dpp v151, v151, v151 row_half_mirror row_mask:0xf bank_mask:0xf\n"
    "s_nop 1\n"
    "v_max_u32_dpp v151, v151, v151 row_mirror row_mask:0xf bank_mask:0xf\n"
    "v_mov_b32 v152, v151\n"
    "s_nop 1\n"
    "v_permlane16_swap_b32 v151, v152\n"
    "v_max_u32 v151, v151, v152\n"
    "v_mov_b32 v152, v151\n"
    "s_nop 1\n"
    "v_permlane32_swap_b32 v151, v152\n"
    "v_max_u32_e64 v151, v151, v152\n"
    "ds_write_b32 v125, v151 offset:8\n"
    "s_waitcnt vmcnt(11)\n"
    "v_mov_b32 v252, 1\n"
    "ds_add_u32 v254, v252 offset:24\n"
    "s_waitcnt vmcnt(0)\n"
    "v_mov_b32 v252, 512\n"
    "ds_add_u32 v254, v252 offset:24\n"
    "s_cmp_lg_u32 s2, 0\n"
    "s_cbranch_scc1 .Lk_k29\n"
    "buffer_store_dwordx2 v[242:243], v240, s[4:7], 0 offen sc1\n"
    ".Lk_k29:\n"
    "s_cmp_lg_u32 s52, 1\n"
    "s_cbranch_scc1 .Lk_p134\n"
    "s_waitcnt lgkmcnt(0)\n"
    ".Lk_w35:\n"
    "ds_read_b32 v252, v254 offset:32\n"
    "s_waitcnt lgkmcnt(0)\n"
    "v_readfirstlane_b32 s71, v252\n"
    "s_cmp_ge_u32 s71, 1\n"
    "s_cbranch_scc1 .Lk_d36\n"
    "s_sleep 2\n"
    "s_sub_u32 s54, s54, 1\n"
    "s_cbranch_scc0 .Lk_w35\n"
    "s_mov_b32 s54, 0\n"
    ".Lk_d36:\n"
    ".Lk_w37:\n"
    "ds_read_b32 v252, v254 offset:100\n"
    "s_waitcnt lgkmcnt(0)\n"
    "v_readfirstlane_b32 s71, v252\n"
    "s_cmp_ge_u32 s71, 4\n"
    "s_cbranch_scc1 .Lk_d38\n"
    "s_sleep 1\n"
    "s_sub_u32 s54, s54, 1\n"
    "s_cbranch_scc0 .Lk_w37\n"
    "s_mov_b32 s54, 0\n"
    ".Lk_d38:\n"
    "s_mov_b32 s66, 219136\n"
    ".Lk_pad2:\n"
    "s_cbranch_execz .Lk_tramp\n"
    "s_mov_b32 s67, 124928\n"
    "s_mov_b32 m0, s67\n"
    "s_add_u32 s68, s66, 0\n"
    "s_nop 0\n"
    "buffer_load_dwordx4 v255, s[4:7], s68 offen sc1 lds\n"
    "buffer_load_dwordx4 v255, s[4:7], s68 offen offset:1024 sc1 lds\n"
    "buffer_load_dwordx4 v255, s[4:7], s68 offen offset:2048 sc1 lds\n"
    "buffer_load_dwordx4 v255, s[4:7], s68 offen offset:3072 sc1 lds\n"
    "s_add_u32 m0, s67, 4096\n"
    "s_add_u32 s68, s66, 4096\n"
    "buffer_load_dwordx4 v255, s[4:7], s68 offen sc1 lds\n"
    "buffer_load_dwordx4 v255, s[4:7], s68 offen offset:1024 sc1 lds\n"
    "buffer_load_dwordx4 v255, s[4:7], s68 offen offset:2048 sc1 lds\n"
    "buffer_load_dwordx4 v255, s[4:7], s68 offen offset:3072 sc1 lds\n"
    "s_add_u32 m0, s67, 8192\n"
    "s_add_u32 s68, s66, 8192\n"
    "buffer_load_dwordx4 v255, s[4:7], s68 offen sc1 lds\n"
    "buffer_load_dwordx4 v255, s[4:7], s68 offen offset:1024 sc1 lds\n"
    "buffer_load_dwordx4 v255, s[4:7], s68 offen offset:2048 sc1 lds\n"
    "buffer_load_dwordx4 v255, s[4:7], s68 offen offset:3072 sc1 lds\n"
    "s_add_u32 m0, s67, 12288\n"
    "s_add_u32 s68, s66, 12288\n"
    "buffer_load_dwordx4 v255, s[4:7], s68 offen sc1 lds\n"
    "buffer_load_dwordx4 v255, s[4:7], s68 offen offset:1024 sc1 lds\n"
    "buffer_load_dwordx4 v255, s[4:7], s68 offen offset:2048 sc1 lds\n"
    "buffer_load_dwordx4 v255, s[4:7], s68 offen offset:3072 sc1 lds\n"
    "s_add_u32 m0, s67, 16384\n"
    "s_add_u32 s68, s66, 16384\n"
    "buffer_load_dwordx4 v255, s[4:7], s68 offen sc1 lds\n"
    "buffer_load_dwordx4 v255, s[4:7], s68 offen offset:1024 sc1 lds\n"
    "buffer_load_dwordx4 v255, s[4:7], s68 offen offset:2048 sc1 lds\n"
    "buffer_load_dwordx4 v255, s[4:7], s68 offen offset:3072 sc1 lds\n"
    "s_add_u32 m0, s67, 20480\n"
    "s_add_u32 s68, s66, 20480\n"
    "buffer_load_dwordx4 v255, s[4:7], s68 offen sc1 lds\n"
    "buffer_load_dwordx4 v255, s[4:7], s68 offen offset:1024 sc1 lds\n"
    "s_waitcnt vmcnt(0)\n"
    "v_mov_b32 v252, 2\n"
    "ds_add_u32 v254, v252 offset:24\n"
    ".Lk_p134:\n"
    "s_waitcnt lgkmcnt(0)\n"
    ".Lk_w40:\n"
    "ds_read_b32 v252, v254 offset:44\n"
    "s_waitcnt lgkmcnt(0)\n"
    "v_readfirstlane_b32 s71, v252\n"
    "s_cmp_ge_u32 s71, 4\n"
    "s_cbranch_scc1 .Lk_d41\n"
    "s_sleep 4\n"
    "s_sub_u32 s54, s54, 1\n"
    "s_cbranch_scc0 .Lk_w40\n"
    "s_mov_b32 s54, 0\n"
    ".Lk_d41:\n"
    "s_mul_i32 s67, s52, 0x5800\n"
    "s_add_u32 s66, s67, 0x3b000\n"
    "s_lshl_b32 s65, 4, s52\n"
    "s_mov_b32 m0, s67\n"
    "s_add_u32 s68, s66, 0\n"
    "buffer_load_dwordx4 v255, s[4:7], s68 offen sc1 lds\n"
    "buffer_load_dwordx4 v255, s[4:7], s68 offen offset:1024 sc1 lds\n"
    "buffer_load_dwordx4 v255, s[4:7], s68 offen offset:2048 sc1 lds\n"
    "buffer_load_dwordx4 v255, s[4:7], s68 offen offset:3072 sc1 lds\n"
    "s_add_u32 m0, s67, 4096\n"
    "s_add_u32 s68, s66, 4096\n"
    "buffer_load_dwordx4 v255, s[4:7], s68 offen sc1 lds\n"
    "buffer_load_dwordx4 v255, s[4:7], s68 offen offset:1024 sc1 lds\n"
    "buffer_load_dwordx4 v255, s[4:7], s68 offen offset:2048 sc1 lds\n"
    "buffer_load_dwordx4 v255, s[4:7], s68 offen offset:3072 sc1 lds\n"
    "s_add_u32 m0, s67, 8192\n"
    "s_add_u32 s68, s66, 8192\n"
    "buffer_load_dwordx4 v255, s[4:7], s68 offen sc1 lds\n"
    "buffer_load_dwordx4 v255, s[4:7], s68 offen offset:1024 sc1 lds\n"
    "buffer_load_dwordx4 v255, s[4:7], s68 offen offset:2048 sc1 lds\n"
    "buffer_load_dwordx4 v255, s[4:7], s68 offen offset:3072 sc1 lds\n"
    "s_add_u32 m0, s67, 12288\n"
    "s_add_u32 s68, s66, 12288\n"
    "buffer_load_dwordx4 v255, s[4:7], s68 offen sc1 lds\n"
    "buffer_load_dwordx4 v255, s[4:7], s68 offen offset:1024 sc1 lds\n"
    "buffer_load_dwordx4 v255, s[4:7], s68 offen offset:2048 sc1 lds\n"
    "buffer_load_dwordx4 v255, s[4:7], s68 offen offset:3072 sc1 lds\n"
    "s_add_u32 m0, s67, 16384\n"
    "s_add_u32 s68, s66, 16384\n"
    "buffer_load_dwordx4 v255, s[4:7], s68 offen sc1 lds\n"
    "buffer_load_dwordx4 v255, s[4:7], s68 offen offset:1024 sc1 lds\n"
    "buffer_load_dwordx4 v255, s[4:7], s68 offen offset:2048 sc1 lds\n"
    "buffer_load_dwordx4 v255, s[4:7], s68 offen offset:3072 sc1 lds\n"
    "s_add_u32 m0, s67, 20480\n"
    "s_add_u32 s68, s66, 20480\n"
    "buffer_load_dwordx4 v255, s[4:7], s68 offen sc1 lds\n"
    "buffer_load_dwordx4 v255, s[4:7], s68 offen offset:1024 sc1 lds\n"
    "ds_add_u32 v254, v253 offset:28\n"
    "s_waitcnt lgkmcnt(0)\n"
    ".Lk_w42:\n"
    "ds_read_b32 v252, v254 offset:28\n"
    "s_waitcnt lgkmcnt(0)\n"
    "v_readfirstlane_b32 s71, v252\n"
    "s_cmp_ge_u32 s71, 4\n"
    "s_cbranch_scc1 .Lk_d43\n"
    "s_sleep 1\n"
    "s_sub_u32 s54, s54, 1\n"
    "s_cbranch_scc0 .Lk_w42\n"
    "s_mov_b32 s54, 0\n"
    ".Lk_d43:\n"
    "s_mov_b32 m0, s77\n"
    "s_nop 0\n"
    "buffer_load_dwordx4 v130, s[12:15], s73 offen lds\n"
    "s_add_u32 s73, s73, s74\n"
    "s_add_u32 m0, s77, 1024\n"
    "s_nop 0\n"
    "buffer_load_dwordx4 v130, s[12:15], s73 offen lds\n"
    "s_add_u32 s73, s73, s74\n"
    "s_add_u32 m0, s77, 2048\n"
    "s_nop 0\n"
    "buffer_load_dwordx4 v130, s[12:15], s73 offen lds\n"
    "s_add_u32 s73, s73, s74\n"
    "s_add_u32 m0, s77, 3072\n"
    "s_nop 0\n"
    "buffer_load_dwordx4 v130, s[12:15], s73 offen lds\n"
    "s_add_u32 s73, s73, s74\n"
    "s_add_u32 m0, s77, 4096\n"
    "s_nop 0\n"
    "buffer_load_dwordx4 v130, s[12:15], s73 offen lds\n"
    "s_waitcnt vmcnt(5)\n"
    "v_mov_b32_e64 v252, s65\n"
    "ds_add_u32 v254, v252 offset:24\n"
    "s_waitcnt vmcnt(0)\n"
    "s_cmp_ge_u32 s72, 108\n"
    "s_cbranch_scc1 .Lk_t244\n"
    "ds_read_b64_tr_b16 v[0:1], v132\n"
    "ds_read_b64_tr_b16 v[2:3], v132 offset:128\n"
    "ds_read_b64_tr_b16 v[4:5], v132 offset:1024\n"
    "ds_read_b64_tr_b16 v[6:7], v132 offset:1152\n"
    "ds_read_b64_tr_b16 v[8:9], v132 offset:2560\n"
    "ds_read_b64_tr_b16 v[10:11], v132 offset:2688\n"
    "ds_read_b64_tr_b16 v[12:13], v132 offset:3584\n"
    "ds_read_b64_tr_b16 v[14:15], v132 offset:3712\n"
    "ds_read_b64_tr_b16 v[16:17], v133 offset:2048\n"
    "ds_read_b64_tr_b16 v[18:19], v133 offset:2176\n"
    "s_waitcnt lgkmcnt(8)\n"
    "buffer_store_dwordx4 v[0:3], v139, s[4:7], 0 offen sc1\n"
    "s_waitcnt lgkmcnt(6)\n"
    "buffer_store_dwordx4 v[4:7], v139, s[4:7], 0 offen offset:1024 sc1\n"
    "s_waitcnt lgkmcnt(4)\n"
    "buffer_store_dwordx4 v[8:11], v139, s[4:7], 0 offen offset:2048 sc1\n"
    "s_waitcnt lgkmcnt(2)\n"
    "buffer_store_dwordx4 v[12:15], v139, s[4:7], 0 offen offset:3072 sc1\n"
    "v_add_u32 v139, 0x1000, v139\n"
    "s_waitcnt lgkmcnt(0)\n"
    "buffer_store_dwordx4 v[16:19], v139, s[4:7], 0 offen sc1\n"
    "v_mfma_f32_16x16x32_bf16 v[20:23], v[0:3], v[84:87], 0\n"
    "v_mfma_f32_16x16x32_bf16 v[20:23], v[4:7], v[84:87], v[20:23]\n"
    "v_mfma_f32_16x16x32_bf16 v[20:23], v[8:11], v[84:87], v[20:23]\n"
    "v_mfma_f32_16x16x32_bf16 v[20:23], v[12:15], v[84:87], v[20:23]\n"
    "v_mfma_f32_16x16x32_bf16 v[20:23], v[16:19], v[84:87], v[20:23]\n"
    "v_lshl_add_u32 v24, v94, 4, s76\n"
    "s_mov_b32 exec_lo, 0x10001\n"
    "s_mov_b32 exec_hi, 0x10001\n"
    "s_nop 4\n"
    "buffer_store_dwordx4 v[20:23], v24, s[4:7], 0 offen sc1\n"
    "s_mov_b64 exec, -1\n"
    "s_branch .Lk_j45\n"
    ".Lk_t244:\n"
    "ds_read_b64_tr_b16 v[0:1], v131\n"
    "ds_read_b64_tr_b16 v[2:3], v131 offset:128\n"
    "ds_read_b64_tr_b16 v[4:5], v131 offset:8\n"
    "ds_read_b64_tr_b16 v[6:7], v131 offset:136\n"
    "ds_read_b64_tr_b16 v[8:9], v131 offset:2048\n"
    "ds_read_b64_tr_b16 v[10:11], v131 offset:2176\n"
    "ds_read_b64_tr_b16 v[12:13], v131 offset:2056\n"
    "ds_read_b64_tr_b16 v[14:15], v131 offset:2184\n"
    "ds_read_b64_tr_b16 v[16:17], v132 offset:4096\n"
    "ds_read_b64_tr_b16 v[18:19], v132 offset:4224\n"
    "s_waitcnt lgkmcnt(8)\n"
    "buffer_store_dwordx4 v[0:3], v139, s[4:7], 0 offen sc1\n"
    "s_waitcnt lgkmcnt(6)\n"
    "buffer_store_dwordx4 v[4:7], v139, s[4:7], 0 offen offset:1024 sc1\n"
    "s_waitcnt lgkmcnt(4)\n"
    "buffer_store_dwordx4 v[8:11], v139, s[4:7], 0 offen offset:2048 sc1\n"
    "s_waitcnt lgkmcnt(2)\n"
    "buffer_store_dwordx4 v[12:15], v139, s[4:7], 0 offen offset:3072 sc1\n"
    "v_add_u32 v139, 0x1000, v139\n"
    "s_waitcnt lgkmcnt(0)\n"
    "buffer_store_dwordx4 v[16:19], v139, s[4:7], 0 offen sc1\n"
    ".Lk_j45:\n"
    "s_waitcnt vmcnt(0)\n"
    "ds_add_u32 v254, v253 offset:48\n"
    "s_cmp_lg_u32 s52, 0\n"
    "s_cbranch_scc1 .Lk_n46\n"
    "s_mul_i32 s56, s49, 0x2000\n"
    "s_add_u32 s56, s56, 0x568000\n"
    "s_mul_i32 s57, s72, 0x20\n"
    "s_add_u32 s57, s57, s56\n"
    "s_waitcnt lgkmcnt(0)\n"
    ".Lk_w48:\n"
    "ds_read_b32 v252, v254 offset:48\n"
    "s_waitcnt lgkmcnt(0)\n"
    "v_readfirstlane_b32 s71, v252\n"
    "s_cmp_ge_u32 s71, 4\n"
    "s_cbranch_scc1 .Lk_d49\n"
    "s_sleep 2\n"
    "s_sub_u32 s54, s54, 1\n"
    "s_cbranch_scc0 .Lk_w48\n"
    "s_mov_b32 s54, 0\n"
    ".Lk_d49:\n"
    "s_mov_b64 exec, 1\n"
    "buffer_store_dwordx2 v[242:243], v255, s[4:7], s57 offen sc1\n"
    "s_mov_b64 exec, -1\n"
    "s_sleep 16\n"
    ".Lk_w50:\n"
    "ds_read_b32 v252, v254 offset:160\n"
    "s_waitcnt lgkmcnt(0)\n"
    "v_readfirstlane_b32 s71, v252\n"
    "s_cmp_ge_u32 s71, 1\n"
    "s_cbranch_scc1 .Lk_d51\n"
    "s_sleep 4\n"
    "s_sub_u32 s54, s54, 1\n"
    "s_cbranch_scc0 .Lk_w50\n"
    "s_mov_b32 s54, 0\n"
    ".Lk_d51:\n"
    ".Lk_pw52:\n"
    "buffer_load_dwordx2 v[244:245], v95, s[4:7], s56 offen sc1\n"
    "s_waitcnt vmcnt(0)\n"
    "v_cmp_eq_u64 vcc, v[244:245], v[242:243]\n"
    "s_cmp_eq_u64 vcc, exec\n"
    "s_cbranch_scc1 .Lk_pwd53\n"
    "s_sleep 10\n"
    "s_sub_u32 s54, s54, 1\n"
    "s_cbranch_scc0 .Lk_pw52\n"
    "s_mov_b32 s54, 0\n"
    ".Lk_pwd53:\n"
    "ds_add_u32 v254, v253 offset:52\n"
    "s_branch .Lk_j47\n"
    ".Lk_n46:\n"
    "s_cmp_lg_u32 s52, 1\n"
    "s_cbranch_scc1 .Lk_b654\n"
    "s_waitcnt lgkmcnt(0)\n"
    ".Lk_w56:\n"
    "ds_read_b32 v252, v250\n"
    "s_waitcnt lgkmcnt(0)\n"
    "v_cmp_le_u32 vcc, 1, v252\n"
    "s_cmp_eq_u64 vcc, exec\n"
    "s_cbranch_scc1 .Lk_d57\n"
    "s_sleep 8\n"
    "s_sub_u32 s54, s54, 1\n"
    "s_cbranch_scc0 .Lk_w56\n"
    "s_mov_b32 s54, 0\n"
    ".Lk_d57:\n"
    ".Lk_w58:\n"
    "ds_read_b32 v252, v254 offset:160\n"
    "s_waitcnt lgkmcnt(0)\n"
    "v_readfirstlane_b32 s71, v252\n"
    "s_cmp_ge_u32 s71, 1\n"
    "s_cbranch_scc1 .Lk_d59\n"
    "s_sleep 4\n"
    "s_sub_u32 s54, s54, 1\n"
    "s_cbranch_scc0 .Lk_w58\n"
    "s_mov_b32 s54, 0\n"
    ".Lk_d59:\n"
    "s_mov_b32 s66, 331776\n"
    "s_mov_b32 s67, 102400\n"
    "s_mov_b32 m0, s67\n"
    "s_add_u32 s68, s66, 0\n"
    "buffer_load_dwordx4 v255, s[4:7], s68 offen sc1 lds\n"
    "buffer_load_dwordx4 v255, s[4:7], s68 offen offset:1024 sc1 lds\n"
    "buffer_load_dwordx4 v255, s[4:7], s68 offen offset:2048 sc1 lds\n"
    "buffer_load_dwordx4 v255, s[4:7], s68 offen offset:3072 sc1 lds\n"
    "s_add_u32 m0, s67, 4096\n"
    "s_add_u32 s68, s66, 4096\n"
    "buffer_load_dwordx4 v255, s[4:7], s68 offen sc1 lds\n"
    "buffer_load_dwordx4 v255, s[4:7], s68 offen offset:1024 sc1 lds\n"
    "buffer_load_dwordx4 v255, s[4:7], s68 offen offset:2048 sc1 lds\n"
    "buffer_load_dwordx4 v255, s[4:7], s68 offen offset:3072 sc1 lds\n"
    "s_add_u32 m0, s67, 8192\n"
    "s_add_u32 s68, s66, 8192\n"
    "buffer_load_dwordx4 v255, s[4:7], s68 offen sc1 lds\n"
    "buffer_load_dwordx4 v255, s[4:7], s68 offen offset:1024 sc1 lds\n"
    "buffer_load_dwordx4 v255, s[4:7], s68 offen offset:2048 sc1 lds\n"
    "buffer_load_dwordx4 v255, s[4:7], s68 offen offset:3072 sc1 lds\n"
    "s_add_u32 m0, s67, 12288\n"
    "s_add_u32 s68, s66, 12288\n"
    "buffer_load_dwordx4 v255, s[4:7], s68 offen sc1 lds\n"
    "buffer_load_dwordx4 v255, s[4:7], s68 offen offset:1024 sc1 lds\n"
    "buffer_load_dwordx4 v255, s[4:7], s68 offen offset:2048 sc1 lds\n"
    "buffer_load_dwordx4 v255, s[4:7], s68 offen offset:3072 sc1 lds\n"
    "s_add_u32 m0, s67, 16384\n"
    "s_add_u32 s68, s66, 16384\n"
    "buffer_load_dwordx4 v255, s[4:7], s68 offen sc1 lds\n"
    "buffer_load_dwordx4 v255, s[4:7], s68 offen offset:1024 sc1 lds\n"
    "buffer_load_dwordx4 v255, s[4:7], s68 offen offset:2048 sc1 lds\n"
    "buffer_load_dwordx4 v255, s[4:7], s68 offen offset:3072 sc1 lds\n"
    "s_add_u32 m0, s67, 20480\n"
    "s_add_u32 s68, s66, 20480\n"
    "buffer_load_dwordx4 v255, s[4:7], s68 offen sc1 lds\n"
    "buffer_load_dwordx4 v255, s[4:7], s68 offen offset:1024 sc1 lds\n"
    "s_waitcnt vmcnt(0)\n"
    "v_mov_b32 v252, 64\n"
    "ds_add_u32 v254, v252 offset:24\n"
    ".Lk_b654:\n"
    "s_cmp_lg_u32 s52, 2\n"
    "s_cbranch_scc1 .Lk_b755\n"
    "s_waitcnt lgkmcnt(0)\n"
    ".Lk_w60:\n"
    "ds_read_b32 v252, v250\n"
    "s_waitcnt lgkmcnt(0)\n"
    "v_cmp_le_u32 vcc, 2, v252\n"
    "s_cmp_eq_u64 vcc, exec\n"
    "s_cbranch_scc1 .Lk_d61\n"
    "s_sleep 8\n"
    "s_sub_u32 s54, s54, 1\n"
    "s_cbranch_scc0 .Lk_w60\n"
    "s_mov_b32 s54, 0\n"
    ".Lk_d61:\n"
    ".Lk_w62:\n"
    "ds_read_b32 v252, v254 offset:160\n"
    "s_waitcnt lgkmcnt(0)\n"
    "v_readfirstlane_b32 s71, v252\n"
    "s_cmp_ge_u32 s71, 1\n"
    "s_cbranch_scc1 .Lk_d63\n"
    "s_sleep 4\n"
    "s_sub_u32 s54, s54, 1\n"
    "s_cbranch_scc0 .Lk_w62\n"
    "s_mov_b32 s54, 0\n"
    ".Lk_d63:\n"
    "s_mov_b32 s66, 354304\n"
    "s_mov_b32 s67, 124928\n"
    "s_mov_b32 m0, s67\n"
    "s_add_u32 s68, s66, 0\n"
    "s_nop 0\n"
    "buffer_load_dwordx4 v255, s[4:7], s68 offen sc1 lds\n"
    "buffer_load_dwordx4 v255, s[4:7], s68 offen offset:1024 sc1 lds\n"
    "buffer_load_dwordx4 v255, s[4:7], s68 offen offset:2048 sc1 lds\n"
    "buffer_load_dwordx4 v255, s[4:7], s68 offen offset:3072 sc1 lds\n"
    "s_add_u32 m0, s67, 4096\n"
    "s_add_u32 s68, s66, 4096\n"
    "buffer_load_dwordx4 v255, s[4:7], s68 offen sc1 lds\n"
    "buffer_load_dwordx4 v255, s[4:7], s68 offen offset:1024 sc1 lds\n"
    "buffer_load_dwordx4 v255, s[4:7], s68 offen offset:2048 sc1 lds\n"
    "buffer_load_dwordx4 v255, s[4:7], s68 offen offset:3072 sc1 lds\n"
    "s_add_u32 m0, s67, 8192\n"
    "s_add_u32 s68, s66, 8192\n"
    "buffer_load_dwordx4 v255, s[4:7], s68 offen sc1 lds\n"
    "buffer_load_dwordx4 v255, s[4:7], s68 offen offset:1024 sc1 lds\n"
    "buffer_load_dwordx4 v255, s[4:7], s68 offen offset:2048 sc1 lds\n"
    "buffer_load_dwordx4 v255, s[4:7], s68 offen offset:3072 sc1 lds\n"
    "s_add_u32 m0, s67, 12288\n"
    "s_add_u32 s68, s66, 12288\n"
    "buffer_load_dwordx4 v255, s[4:7], s68 offen sc1 lds\n"
    "buffer_load_dwordx4 v255, s[4:7], s68 offen offset:1024 sc1 lds\n"
    "buffer_load_dwordx4 v255, s[4:7], s68 offen offset:2048 sc1 lds\n"
    "buffer_load_dwordx4 v255, s[4:7], s68 offen offset:3072 sc1 lds\n"
    "s_add_u32 m0, s67, 16384\n"
    "s_add_u32 s68, s66, 16384\n"
    "buffer_load_dwordx4 v255, s[4:7], s68 offen sc1 lds\n"
    "buffer_load_dwordx4 v255, s[4:7], s68 offen offset:1024 sc1 lds\n"
    "buffer_load_dwordx4 v255, s[4:7], s68 offen offset:2048 sc1 lds\n"
    "buffer_load_dwordx4 v255, s[4:7], s68 offen offset:3072 sc1 lds\n"
    "s_add_u32 m0, s67, 20480\n"
    "s_add_u32 s68, s66, 20480\n"
    "buffer_load_dwordx4 v255, s[4:7], s68 offen sc1 lds\n"
    "buffer_load_dwordx4 v255, s[4:7], s68 offen offset:1024 sc1 lds\n"
    "s_waitcnt vmcnt(0)\n"
    "v_mov_b32 v252, 128\n"
    "ds_add_u32 v254, v252 offset:24\n"
    ".Lk_b755:\n"
    "s_cmp_lg_u32 s52, 3\n"
    "s_cbranch_scc1 .Lk_iw64\n"
    "s_mul_i32 s69, s47, 0x200000\n"
    "s_add_u32 s69, s69, 0x9fffff\n"
    "s_add_u32 s8, s36, s69\n"
    "s_addc_u32 s9, s37, 0\n"
    "s_and_b32 s8, s8, 0xffe00000\n"
    "s_and_b32 s9, s9, 0xffff\n"
    "s_mov_b32 s10, 1163264\n"
    "s_mov_b32 s11, 0x20000\n"
    "s_mov_b32 m0, 158720\n"
    "s_mov_b64 exec, 1\n"
    "s_mov_b32 s56, 20480\n"
    "buffer_load_dword v255, s[8:11], s56 offen sc1 lds\n"
    "s_mov_b64 exec, -1\n"
    "s_cmp_lg_u32 s48, 0\n"
    "s_cbranch_scc1 .Lk_pr65\n"
    "s_waitcnt lgkmcnt(0)\n"
    ".Lk_w66:\n"
    "ds_read_b32 v252, v254 offset:48\n"
    "s_waitcnt lgkmcnt(0)\n"
    "v_readfirstlane_b32 s71, v252\n"
    "s_cmp_ge_u32 s71, 4\n"
    "s_cbranch_scc1 .Lk_d67\n"
    "s_sleep 2\n"
    "s_sub_u32 s54, s54, 1\n"
    "s_cbranch_scc0 .Lk_w66\n"
    "s_mov_b32 s54, 0\n"
    ".Lk_d67:\n"
    "s_mov_b32 s58, 1146880\n"
    "s_mov_b32 s59, 1155072\n"
    "s_mov_b32 s60, -1\n"
    "s_mov_b32 s61, -1\n"
    "s_mov_b64 exec, 0xff\n"
    "s_waitcnt vmcnt(0)\n"
    "s_memtime s[62:63]\n"
    "s_waitcnt lgkmcnt(0)\n"
    "buffer_store_dwordx4 v[0:3], v255, s[8:11], s58 offen sc1\n"
    "s_waitcnt vmcnt(0)\n"
    "s_memtime s[64:65]\n"
    "s_waitcnt lgkmcnt(0)\n"
    "s_sub_u32 s62, s64, s62\n"
    "s_min_u32 s60, s60, s62\n"
    "s_waitcnt vmcnt(0)\n"
    "s_memtime s[62:63]\n"
    "s_waitcnt lgkmcnt(0)\n"
    "buffer_store_dwordx4 v[0:3], v255, s[8:11], s59 offen sc1\n"
    "s_waitcnt vmcnt(0)\n"
    "s_memtime s[64:65]\n"
    "s_waitcnt lgkmcnt(0)\n"
    "s_sub_u32 s62, s64, s62\n"
    "s_min_u32 s61, s61, s62\n"
    "s_waitcnt vmcnt(0)\n"
    "s_memtime s[62:63]\n"
    "s_waitcnt lgkmcnt(0)\n"
    "buffer_store_dwordx4 v[0:3], v255, s[8:11], s58 offen sc1\n"
    "s_waitcnt vmcnt(0)\n"
    "s_memtime s[64:65]\n"
    "s_waitcnt lgkmcnt(0)\n"
    "s_sub_u32 s62, s64, s62\n"
    "s_min_u32 s60, s60, s62\n"
    "s_waitcnt vmcnt(0)\n"
    "s_memtime s[62:63]\n"
    "s_waitcnt lgkmcnt(0)\n"
    "buffer_store_dwordx4 v[0:3], v255, s[8:11], s59 offen sc1\n"
    "s_waitcnt vmcnt(0)\n"
    "s_memtime s[64:65]\n"
    "s_waitcnt lgkmcnt(0)\n"
    "s_sub_u32 s62, s64, s62\n"
    "s_min_u32 s61, s61, s62\n"
    "s_cmp_lt_u32 s61, s60\n"
    "s_cselect_b32 s60, 8192, 0\n"
    "s_mul_i32 s58, s47, 0x80\n"
    "s_add_u32 s58, s58, 0x1000\n"
    "v_not_b32 v122, s44\n"
    "v_mov_b32 v123, s60\n"
    "v_lshrrev_b32 v124, 1, v255\n"
    "s_mov_b64 exec, 1\n"
    "v_mov_b32 v122, s44\n"
    "v_mov_b32 v123, s45\n"
    "s_mov_b64 exec, 3\n"
    "buffer_store_dwordx2 v[122:123], v124, s[4:7], s58 offen sc1\n"
    "s_mov_b64 exec, -1\n"
    ".Lk_pr65:\n"
    "ds_add_u32 v254, v253 offset:160\n"
    ".Lk_iw64:\n"
    "s_waitcnt lgkmcnt(0)\n"
    ".Lk_w68:\n"
    "ds_read_b32 v252, v254 offset:52\n"
    "s_waitcnt lgkmcnt(0)\n"
    "v_readfirstlane_b32 s71, v252\n"
    "s_cmp_ge_u32 s71, 1\n"
    "s_cbranch_scc1 .Lk_d69\n"
    "s_sleep 8\n"
    "s_sub_u32 s54, s54, 1\n"
    "s_cbranch_scc0 .Lk_w68\n"
    "s_mov_b32 s54, 0\n"
    ".Lk_d69:\n"
    ".Lk_j47:\n"
    "s_mul_i32 s72, s48, 0x43800\n"
    "s_mul_i32 s70, s52, 0xa000\n"
    "s_add_u32 s72, s72, s70\n"
    "s_add_u32 s72, s72, 0x344000\n"
    "s_mul_i32 s73, s52, 0x400\n"
    "s_add_u32 s73, s73, 0x14000\n"
    "v_mov_b32 v247, v95\n"
    "v_mov_b32 v248, v93\n"
    "v_mov_b32 v249, v94\n"
    "s_and_b32 s56, s47, 3\n"
    "s_mul_i32 s56, s56, 0x4\n"
    "s_add_u32 s56, s56, s52\n"
    "s_mul_i32 s57, s49, 0x1b\n"
    "s_mul_i32 s70, s50, 0x6\n"
    "s_add_u32 s57, s57, s70\n"
    "s_add_u32 s57, s57, 0x10\n"
    "s_mul_i32 s75, s57, 0x5000\n"
    "s_mul_i32 s70, s56, 0x1e00\n"
    "s_add_u32 s75, s75, s70\n"
    "s_add_u32 s75, s75, 0x128000\n"
    "s_mul_i32 s76, s48, 0x43800\n"
    "s_mul_i32 s70, s56, 0x4380\n"
    "s_add_u32 s76, s76, s70\n"
    "s_add_u32 s76, s76, 0x344000\n"
    "s_mul_i32 s57, s49, 0x1b\n"
    "s_mul_i32 s70, s50, 0x8\n"
    "s_add_u32 s57, s57, s70\n"
    "s_mul_i32 s70, s52, 0x2\n"
    "s_add_u32 s57, s57, s70\n"
    "s_mul_i32 s58, s57, 0x5000\n"
    "s_add_u32 s58, s58, 0x128000\n"
    "buffer_load_dwordx4 v[0:3], v255, s[4:7], s58 offen sc1\n"
    "buffer_load_dwordx4 v[8:11], v255, s[4:7], s58 offen offset:1024 sc1\n"
    "buffer_load_dwordx4 v[16:19], v255, s[4:7], s58 offen offset:2048 sc1\n"
    "buffer_load_dwordx4 v[24:27], v255, s[4:7], s58 offen offset:3072 sc1\n"
    "s_add_u32 s58, s58, 4096\n"
    "s_waitcnt vmcnt(10)\n"
    "buffer_load_dwordx4 v[32:35], v255, s[4:7], s58 offen sc1\n"
    "buffer_load_dwordx4 v[40:43], v255, s[4:7], s58 offen offset:1024 sc1\n"
    "buffer_load_dwordx4 v[48:51], v255, s[4:7], s58 offen offset:2048 sc1\n"
    "buffer_load_dwordx4 v[56:59], v255, s[4:7], s58 offen offset:3072 sc1\n"
    "s_add_u32 s58, s58, 4096\n"
    "s_waitcnt vmcnt(10)\n"
    "buffer_load_dwordx4 v[64:67], v255, s[4:7], s58 offen sc1\n"
    "buffer_load_dwordx4 v[72:75], v255, s[4:7], s58 offen offset:1024 sc1\n"
    "buffer_load_dwordx4 v[80:83], v255, s[4:7], s58 offen offset:2048 sc1\n"
    "buffer_load_dwordx4 v[88:91], v255, s[4:7], s58 offen offset:3072 sc1\n"
    "s_add_u32 s58, s58, 4096\n"
    "s_waitcnt vmcnt(10)\n"
    "buffer_load_dwordx4 v[96:99], v255, s[4:7], s58 offen sc1\n"
    "buffer_load_dwordx4 v[104:107], v255, s[4:7], s58 offen offset:1024 sc1\n"
    "buffer_load_dwordx4 v[112:115], v255, s[4:7], s58 offen offset:2048 sc1\n"
    "buffer_load_dwordx4 v[120:123], v255, s[4:7], s58 offen offset:3072 sc1\n"
    "s_add_u32 s58, s58, 4096\n"
    "s_waitcnt vmcnt(10)\n"
    "buffer_load_dwordx4 v[128:131], v255, s[4:7], s58 offen sc1\n"
    "buffer_load_dwordx4 v[136:139], v255, s[4:7], s58 offen offset:1024 sc1\n"
    "buffer_load_dwordx4 v[144:147], v255, s[4:7], s58 offen offset:2048 sc1\n"
    "buffer_load_dwordx4 v[152:155], v255, s[4:7], s58 offen offset:3072 sc1\n"
    "s_add_u32 s58, s58, 4096\n"
    "s_waitcnt vmcnt(10)\n"
    "buffer_load_dwordx4 v[4:7], v255, s[4:7], s58 offen sc1\n"
    "buffer_load_dwordx4 v[12:15], v255, s[4:7], s58 offen offset:1024 sc1\n"
    "buffer_load_dwordx4 v[20:23], v255, s[4:7], s58 offen offset:2048 sc1\n"
    "buffer_load_dwordx4 v[28:31], v255, s[4:7], s58 offen offset:3072 sc1\n"
    "s_add_u32 s58, s58, 4096\n"
    "s_waitcnt vmcnt(10)\n"
    "buffer_load_dwordx4 v[36:39], v255, s[4:7], s58 offen sc1\n"
    "buffer_load_dwordx4 v[44:47], v255, s[4:7], s58 offen offset:1024 sc1\n"
    "buffer_load_dwordx4 v[52:55], v255, s[4:7], s58 offen offset:2048 sc1\n"
    "buffer_load_dwordx4 v[60:63], v255, s[4:7], s58 offen offset:3072 sc1\n"
    "s_add_u32 s58, s58, 4096\n"
    "s_waitcnt vmcnt(10)\n"
    "buffer_load_dwordx4 v[68:71], v255, s[4:7], s58 offen sc1\n"
    "buffer_load_dwordx4 v[76:79], v255, s[4:7], s58 offen offset:1024 sc1\n"
    "buffer_load_dwordx4 v[84:87], v255, s[4:7], s58 offen offset:2048 sc1\n"
    "buffer_load_dwordx4 v[92:95], v255, s[4:7], s58 offen offset:3072 sc1\n"
    "s_add_u32 s58, s58, 4096\n"
    "s_waitcnt vmcnt(10)\n"
    "buffer_load_dwordx4 v[100:103], v255, s[4:7], s58 offen sc1\n"
    "buffer_load_dwordx4 v[108:111], v255, s[4:7], s58 offen offset:1024 sc1\n"
    "buffer_load_dwordx4 v[116:119], v255, s[4:7], s58 offen offset:2048 sc1\n"
    "buffer_load_dwordx4 v[124:127], v255, s[4:7], s58 offen offset:3072 sc1\n"
    "s_add_u32 s58, s58, 4096\n"
    "s_waitcnt vmcnt(10)\n"
    "buffer_load_dwordx4 v[132:135], v255, s[4:7], s58 offen sc1\n"
    "buffer_load_dwordx4 v[140:143], v255, s[4:7], s58 offen offset:1024 sc1\n"
    "buffer_load_dwordx4 v[148:151], v255, s[4:7], s58 offen offset:2048 sc1\n"
    "buffer_load_dwordx4 v[156:159], v255, s[4:7], s58 offen offset:3072 sc1\n"
    "s_add_u32 s58, s58, 4096\n"
    "s_waitcnt vmcnt(10)\n"
    "buffer_load_dwordx4 v[224:227], v255, s[4:7], s72 offen sc1\n"
    "buffer_load_dwordx4 v[228:231], v255, s[4:7], s72 offen offset:1024 sc1\n"
    "buffer_load_dwordx4 v[232:235], v255, s[4:7], s72 offen offset:2048 sc1\n"
    "buffer_load_dwordx4 v[236:239], v255, s[4:7], s72 offen offset:3072 sc1\n"
    "s_waitcnt vmcnt(10)\n"
    "s_add_u32 s58, s72, 22528\n"
    "buffer_load_dwordx4 v[176:179], v255, s[4:7], s58 offen sc1\n"
    "s_add_u32 s58, s72, 23552\n"
    "buffer_load_dwordx4 v[180:183], v255, s[4:7], s58 offen sc1\n"
    "s_add_u32 s58, s72, 24576\n"
    "buffer_load_dwordx4 v[184:187], v255, s[4:7], s58 offen sc1\n"
    "s_add_u32 s58, s72, 25600\n"
    "buffer_load_dwordx4 v[188:191], v255, s[4:7], s58 offen sc1\n"
    "s_waitcnt vmcnt(10)\n"
    "s_add_u32 s58, s72, 26624\n"
    "buffer_load_dwordx4 v[192:195], v255, s[4:7], s58 offen sc1\n"
    "s_add_u32 s58, s72, 32768\n"
    "buffer_load_dwordx4 v[196:199], v255, s[4:7], s58 offen sc1\n"
    "s_add_u32 s58, s72, 33792\n"
    "buffer_load_dwordx4 v[200:203], v255, s[4:7], s58 offen sc1\n"
    "s_add_u32 s58, s72, 34816\n"
    "buffer_load_dwordx4 v[204:207], v255, s[4:7], s58 offen sc1\n"
    "s_waitcnt vmcnt(10)\n"
    "s_nop 0\n"
    "s_add_u32 s58, s72, 35840\n"
    "buffer_load_dwordx4 v[208:211], v255, s[4:7], s58 offen sc1\n"
    "s_add_u32 s58, s72, 36864\n"
    "buffer_load_dwordx4 v[212:215], v255, s[4:7], s58 offen sc1\n"
    "s_add_u32 s58, s72, 37888\n"
    "buffer_load_dwordx4 v[216:219], v255, s[4:7], s58 offen sc1\n"
    "s_add_u32 m0, s73, 65536\n"
    "s_add_u32 s58, s72, 38912\n"
    "buffer_load_dwordx4 v255, s[4:7], s58 offen sc1 lds\n"
    "s_add_u32 m0, s73, 69632\n"
    "s_add_u32 s58, s72, 39936\n"
    "buffer_load_dwordx4 v255, s[4:7], s58 offen sc1 lds\n"
    "s_lshl_b32 s57, s50, 3\n"
    "s_mul_i32 s58, s50, 6\n"
    "s_add_u32 s58, s58, 8\n"
    "s_cmp_lt_u32 s52, 2\n"
    "s_cselect_b32 s57, s57, s58\n"
    "s_mul_i32 s57, s57, 0x10\n"
    "s_mul_i32 s70, s49, 0x1b0\n"
    "s_add_u32 s57, s57, s70\n"
    "s_mul_i32 s70, s52, 0x40\n"
    "s_add_u32 s57, s57, s70\n"
    "s_mul_i32 s58, s49, 0x1b0\n"
    "s_add_u32 s58, s58, 0x1af\n"
    "v_lshrrev_b32 v160, 4, v255\n"
    "v_add_u32 v161, s57, v160\n"
    "v_min_u32 v161, s58, v161\n"
    "v_lshlrev_b32 v162, 2, v161\n"
    "v_lshlrev_b32 v161, 1, v161\n"
    "s_mov_b32 s59, 36864\n"
    "buffer_load_dword v164, v162, s[4:7], s59 offen sc1\n"
    "s_mov_b32 s59, 43776\n"
    "buffer_load_dword v165, v162, s[4:7], s59 offen sc1\n"
    "s_mov_b32 s59, 50688\n"
    "buffer_load_dword v166, v162, s[4:7], s59 offen sc1\n"
    "s_mov_b32 s59, 57600\n"
    "buffer_load_dword v167, v162, s[4:7], s59 offen sc1\n"
    "s_mov_b32 s12, s28\n"
    "s_and_b32 s13, s29, 0xffff\n"
    "s_mov_b32 s14, 3456\n"
    "s_mov_b32 s15, 0x20000\n"
    "s_mov_b32 s40, s32\n"
    "s_and_b32 s41, s33, 0xffff\n"
    "s_mov_b32 s42, 1280\n"
    "s_mov_b32 s43, 0x20000\n"
    "buffer_load_ushort v168, v161, s[12:15], 0 offen\n"
    "s_mul_i32 s57, s48, 0xa0\n"
    "s_mul_i32 s70, s52, 0x28\n"
    "s_add_u32 s57, s57, s70\n"
    "v_lshlrev_b32_e64 v163, 1, v160\n"
    "buffer_load_ushort v169, v163, s[40:43], s57 offen\n"
    "s_mul_i32 s57, s52, 0x100\n"
    "s_add_u32 s57, s57, 0x27680\n"
    "s_mul_i32 s58, s52, 0x50\n"
    "s_add_u32 s58, s58, 0x27100\n"
    "v_lshl_add_u32 v161, v160, 2, s57\n"
    "v_lshl_add_u32 v162, v160, 2, s58\n"
    "s_waitcnt vmcnt(4)\n"
    "v_add_f32 v164, v164, v165\n"
    "s_waitcnt vmcnt(2)\n"
    "v_add_f32 v166, v166, v167\n"
    "v_add_f32 v164, v164, v166\n"
    "s_waitcnt vmcnt(1)\n"
    "v_lshlrev_b32 v168, 16, v168\n"
    "s_waitcnt vmcnt(0)\n"
    "v_lshlrev_b32_e64 v169, 16, v169\n"
    "ds_write_b32 v161, v164\n"
    "ds_write_b32 v161, v168 offset:1024\n"
    "s_mov_b64 exec, 0xfffff\n"
    "ds_write_b32 v162, v169\n"
    "s_mov_b64 exec, -1\n"
    "s_waitcnt vmcnt(0)\n"
    "s_mov_b32 m0, 158720\n"
    "s_mov_b32 exec_hi, 0xfffffff\n"
    "buffer_load_dword v247, s[4:7], s75 offen sc1 lds\n"
    "s_mov_b32 exec_hi, -1\n"
    "s_waitcnt vmcnt(4)\n"
    "s_mov_b32 m0, 158720\n"
    "s_mov_b32 exec_hi, 0x1fff\n"
    "buffer_load_dword v247, s[4:7], s76 offen sc1 lds\n"
    "s_mov_b32 exec_hi, -1\n"
    "s_add_u32 s76, s76, 5760\n"
    "s_waitcnt vmcnt(4)\n"
    "s_mov_b32 m0, 158720\n"
    "s_mov_b32 exec_hi, 0x1fff\n"
    "buffer_load_dword v247, s[4:7], s76 offen sc1 lds\n"
    "s_mov_b32 exec_hi, -1\n"
    "s_add_u32 s76, s76, 5760\n"
    "s_waitcnt vmcnt(4)\n"
    "s_waitcnt vmcnt(0)\n"
    "s_cmp_lg_u32 s52, 0\n"
    "s_cbranch_scc1 .Lk_sd70\n"
    "s_mul_i32 s58, s47, 0x80\n"
    "s_add_u32 s58, s58, 0x1000\n"
    "v_mov_b32 v160, 0\n"
    "s_not_b32 s59, s44\n"
    ".Lk_ps71:\n"
    "buffer_load_dwordx4 v[164:167], v160, s[4:7], s58 offen sc1\n"
    "s_waitcnt vmcnt(0)\n"
    ".Lk_pad3:\n"
    "s_cbranch_execz .Lk_tramp\n"
    "v_cmp_eq_u64 s[60:61], v[164:165], v[242:243]\n"
    "v_cmp_eq_u32 vcc, s59, v166\n"
    "s_and_b64 vcc, vcc, s[60:61]\n"
    "s_cbranch_scc1 .Lk_psd72\n"
    "s_sleep 4\n"
    "s_sub_u32 s54, s54, 1\n"
    "s_cbranch_scc0 .Lk_ps71\n"
    "s_mov_b32 s54, 0\n"
    ".Lk_psd72:\n"
    "v_and_b32 v167, 8192, v167\n"
    "v_or_b32 v167, 256, v167\n"
    "ds_add_u32 v254, v167 offset:24\n"
    ".Lk_sd70:\n"
    "s_cmp_lg_u32 s52, 1\n"
    "s_cbranch_scc1 .Lk_iw73\n"
    "s_mov_b64 exec, 0\n"
    "s_call_b64 s[100:101], .Lk_pad8\n"
    "s_call_b64 s[100:101], .Lk_pad9\n"
    "s_call_b64 s[100:101], .Lk_pad10\n"
    "s_call_b64 s[100:101], .Lk_pad11\n"
    "s_call_b64 s[100:101], .Lk_pad4\n"
    "s_call_b64 s[100:101], .Lk_pad5\n"
    "s_call_b64 s[100:101], .Lk_pad3\n"
    "s_nop 0\n"
    "s_nop 0\n"
    "s_nop 0\n"
    "s_mov_b64 exec, -1\n"
    ".Lk_iw73:\n"
    "s_add_u32 s65, s52, 4\n"
    "s_min_u32 s65, s65, 6\n"
    "s_waitcnt lgkmcnt(0)\n"
    ".Lk_w74:\n"
    "ds_read_b32 v252, v250\n"
    "s_waitcnt lgkmcnt(0)\n"
    "v_cmp_le_u32 vcc, s65, v252\n"
    "s_cmp_eq_u64 vcc, exec\n"
    "s_cbranch_scc1 .Lk_d75\n"
    "s_sleep 8\n"
    "s_sub_u32 s54, s54, 1\n"
    "s_cbranch_scc0 .Lk_w74\n"
    "s_mov_b32 s54, 0\n"
    ".Lk_d75:\n"
    "s_mul_i32 s58, s48, 0x19000\n"
    "s_mul_i32 s70, s52, 0x6400\n"
    "s_add_u32 s58, s58, s70\n"
    "s_add_u32 s58, s58, 0x60000\n"
    "s_mul_i32 s59, s52, 0x6400\n"
    "s_mov_b32 m0, s59\n"
    "s_nop 0\n"
    "buffer_load_dwordx4 v255, s[4:7], s58 offen sc1 lds\n"
    "buffer_load_dwordx4 v255, s[4:7], s58 offen offset:1024 sc1 lds\n"
    "buffer_load_dwordx4 v255, s[4:7], s58 offen offset:2048 sc1 lds\n"
    "buffer_load_dwordx4 v255, s[4:7], s58 offen offset:3072 sc1 lds\n"
    "s_add_u32 s58, s58, 4096\n"
    "s_waitcnt vmcnt(10)\n"
    "s_add_u32 m0, s59, 4096\n"
    "s_nop 0\n"
    "buffer_load_dwordx4 v255, s[4:7], s58 offen sc1 lds\n"
    "buffer_load_dwordx4 v255, s[4:7], s58 offen offset:1024 sc1 lds\n"
    "buffer_load_dwordx4 v255, s[4:7], s58 offen offset:2048 sc1 lds\n"
    "buffer_load_dwordx4 v255, s[4:7], s58 offen offset:3072 sc1 lds\n"
    "s_add_u32 s58, s58, 4096\n"
    "s_waitcnt vmcnt(10)\n"
    "s_add_u32 m0, s59, 8192\n"
    "s_nop 0\n"
    "buffer_load_dwordx4 v255, s[4:7], s58 offen sc1 lds\n"
    "buffer_load_dwordx4 v255, s[4:7], s58 offen offset:1024 sc1 lds\n"
    "buffer_load_dwordx4 v255, s[4:7], s58 offen offset:2048 sc1 lds\n"
    "buffer_load_dwordx4 v255, s[4:7], s58 offen offset:3072 sc1 lds\n"
    "s_add_u32 s58, s58, 4096\n"
    "s_waitcnt vmcnt(10)\n"
    "s_add_u32 m0, s59, 12288\n"
    "s_nop 0\n"
    "buffer_load_dwordx4 v255, s[4:7], s58 offen sc1 lds\n"
    "buffer_load_dwordx4 v255, s[4:7], s58 offen offset:1024 sc1 lds\n"
    "buffer_load_dwordx4 v255, s[4:7], s58 offen offset:2048 sc1 lds\n"
    "buffer_load_dwordx4 v255, s[4:7], s58 offen offset:3072 sc1 lds\n"
    "s_add_u32 s58, s58, 4096\n"
    "s_waitcnt vmcnt(10)\n"
    "s_add_u32 m0, s59, 16384\n"
    "s_nop 0\n"
    "buffer_load_dwordx4 v255, s[4:7], s58 offen sc1 lds\n"
    "buffer_load_dwordx4 v255, s[4:7], s58 offen offset:1024 sc1 lds\n"
    "buffer_load_dwordx4 v255, s[4:7], s58 offen offset:2048 sc1 lds\n"
    "buffer_load_dwordx4 v255, s[4:7], s58 offen offset:3072 sc1 lds\n"
    "s_add_u32 s58, s58, 4096\n"
    "s_waitcnt vmcnt(10)\n"
    "s_add_u32 m0, s59, 20480\n"
    "s_nop 0\n"
    "buffer_load_dwordx4 v255, s[4:7], s58 offen sc1 lds\n"
    "buffer_load_dwordx4 v255, s[4:7], s58 offen offset:1024 sc1 lds\n"
    "buffer_load_dwordx4 v255, s[4:7], s58 offen offset:2048 sc1 lds\n"
    "buffer_load_dwordx4 v255, s[4:7], s58 offen offset:3072 sc1 lds\n"
    "s_add_u32 s58, s58, 4096\n"
    "s_waitcnt vmcnt(10)\n"
    "s_add_u32 m0, s59, 24576\n"
    "s_nop 0\n"
    "buffer_load_dwordx4 v255, s[4:7], s58 offen sc1 lds\n"
    "s_mov_b32 m0, 158720\n"
    "s_mov_b32 exec_hi, 0x1fff\n"
    "buffer_load_dword v247, s[4:7], s76 offen sc1 lds\n"
    "s_mov_b32 exec_hi, -1\n"
    "s_add_u32 s76, s76, 5760\n"
    "s_waitcnt vmcnt(4)\n"
    ".Lk_w76:\n"
    "ds_read_b32 v252, v250\n"
    "s_waitcnt lgkmcnt(0)\n"
    "v_cmp_le_u32 vcc, 7, v252\n"
    "s_cmp_eq_u64 vcc, exec\n"
    "s_cbranch_scc1 .Lk_d77\n"
    "s_sleep 8\n"
    "s_sub_u32 s54, s54, 1\n"
    "s_cbranch_scc0 .Lk_w76\n"
    "s_mov_b32 s54, 0\n"
    ".Lk_d77:\n"
    "s_add_u32 m0, s73, 20480\n"
    "s_add_u32 s58, s72, 27648\n"
    "buffer_load_dwordx4 v255, s[4:7], s58 offen sc1 lds\n"
    "s_add_u32 m0, s73, 24576\n"
    "s_add_u32 s58, s72, 28672\n"
    "buffer_load_dwordx4 v255, s[4:7], s58 offen sc1 lds\n"
    "s_add_u32 m0, s73, 28672\n"
    "s_add_u32 s58, s72, 29696\n"
    "buffer_load_dwordx4 v255, s[4:7], s58 offen sc1 lds\n"
    "s_add_u32 m0, s73, 32768\n"
    "s_add_u32 s58, s72, 30720\n"
    "buffer_load_dwordx4 v255, s[4:7], s58 offen sc1 lds\n"
    "s_add_u32 m0, s73, 36864\n"
    "s_add_u32 s58, s72, 31744\n"
    "buffer_load_dwordx4 v255, s[4:7], s58 offen sc1 lds\n"
    ".Lk_nobg39:\n"
    "s_mul_i32 s75, s49, 0x4000\n"
    "s_mul_i32 s70, s50, 0x1000\n"
    "s_add_u32 s75, s75, s70\n"
    "s_mul_i32 s70, s52, 0x400\n"
    "s_add_u32 s75, s75, s70\n"
    "s_add_u32 s75, s75, 0x88000\n"
    "s_add_u32 s56, s52, 0\n"
    "s_and_b32 s56, s56, 3\n"
    "s_mul_i32 s76, s56, 0x20000\n"
    "s_mul_i32 s70, s52, 0x4000\n"
    "s_add_u32 s76, s76, s70\n"
    "s_add_u32 s76, s76, 0x88000\n"
    "s_add_u32 s56, s52, 1\n"
    "s_and_b32 s56, s56, 3\n"
    "s_mul_i32 s77, s56, 0x20000\n"
    "s_mul_i32 s70, s52, 0x4000\n"
    "s_add_u32 s77, s77, s70\n"
    "s_add_u32 s77, s77, 0x88000\n"
    "s_add_u32 s56, s52, 2\n"
    "s_and_b32 s56, s56, 3\n"
    "s_mul_i32 s78, s56, 0x20000\n"
    "s_mul_i32 s70, s52, 0x4000\n"
    "s_add_u32 s78, s78, s70\n"
    "s_add_u32 s78, s78, 0x88000\n"
    "s_add_u32 s56, s52, 3\n"
    "s_and_b32 s56, s56, 3\n"
    "s_mul_i32 s79, s56, 0x20000\n"
    "s_mul_i32 s70, s52, 0x4000\n"
    "s_add_u32 s79, s79, s70\n"
    "s_add_u32 s79, s79, 0x88000\n"
    "s_mul_i32 s80, s47, 0x14000\n"
    "s_mul_i32 s70, s52, 0x5000\n"
    "s_add_u32 s80, s80, s70\n"
    "s_mul_i32 s70, s48, 0xa0\n"
    "s_add_u32 s80, s80, s70\n"
    "v_add_u32 v246, 40960, v255\n"
    "v_and_b32 v160, 1, v249\n"
    "v_lshlrev_b32 v160, 17, v160\n"
    "v_lshrrev_b32_e64 v161, 1, v249\n"
    "v_lshl_add_u32 v160, v161, 8, v160\n"
    "v_lshl_add_u32 v160, v248, 4, v160\n"
    "v_add_u32 v250, s75, v160\n"
    "s_mov_b32 s12, s34\n"
    "s_and_b32 s13, s35, 0xffff\n"
    "s_mov_b32 s14, 2621440\n"
    "s_mov_b32 s15, 0x20000\n"
    "s_waitcnt vmcnt(0)\n"
    "s_setprio 2\n"
    "s_mul_i32 s69, s47, 0x200000\n"
    "s_add_u32 s69, s69, 0x9fffff\n"
    "s_add_u32 s8, s36, s69\n"
    "s_addc_u32 s9, s37, 0\n"
    "s_and_b32 s8, s8, 0xffe00000\n"
    "s_and_b32 s9, s9, 0xffff\n"
    "s_mov_b32 s10, 1081344\n"
    "s_mov_b32 s11, 0x20000\n"
    "s_barrier\n"
    "ds_read_b32 v160, v254 offset:24\n"
    "s_waitcnt lgkmcnt(0)\n"
    "v_readfirstlane_b32 s71, v160\n"
    "s_and_b32 s71, s71, 8192\n"
    "s_or_b32 s8, s8, s71\n"
    "v_add_u32_e64 v160, s73, v255\n"
    "ds_write_b128 v160, v[196:199] offset:40960\n"
    "ds_write_b128 v160, v[200:203] offset:45056\n"
    "ds_write_b128 v160, v[204:207] offset:49152\n"
    "ds_write_b128 v160, v[208:211] offset:53248\n"
    "ds_write_b128 v160, v[212:215] offset:57344\n"
    "ds_write_b128 v160, v[216:219] offset:61440\n"
    "s_waitcnt lgkmcnt(0)\n"
    "s_barrier\n"
    "s_cmp_lg_u32 s52, 0\n"
    "s_cbranch_scc1 .Lk_x78\n"
    "buffer_store_dwordx2 v[242:243], v240, s[8:11], 0 offen sc1\n"
    "s_sleep 11\n"
    ".Lk_pg79:\n"
    "buffer_load_dwordx2 v[244:245], v241, s[8:11], 0 offen sc1\n"
    "s_waitcnt vmcnt(0)\n"
    "v_cmp_eq_u64 vcc, v[244:245], v[242:243]\n"
    "s_cmp_eq_u64 vcc, exec\n"
    "s_cbranch_scc1 .Lk_pgd80\n"
    "s_sub_u32 s54, s54, 1\n"
    "s_cbranch_scc0 .Lk_pg79\n"
    "s_mov_b32 s54, 0\n"
    ".Lk_pgd80:\n"
    ".Lk_x78:\n"
    "s_barrier\n"
    "s_barrier\n"
    "v_add_u32_e64 v160, s73, v255\n"
    "ds_write_b128 v160, v[176:179]\n"
    "ds_write_b128 v160, v[180:183] offset:4096\n"
    "ds_write_b128 v160, v[184:187] offset:8192\n"
    "ds_write_b128 v160, v[188:191] offset:12288\n"
    "ds_write_b128 v160, v[192:195] offset:16384\n"
    "s_waitcnt lgkmcnt(0)\n"
    "s_barrier\n"
    "s_cmp_lg_u32 s52, 0\n"
    "s_cbranch_scc1 .Lk_x81\n"
    "buffer_store_dwordx2 v[242:243], v240, s[8:11], 0 offen offset:1024 sc1\n"
    "s_sleep 10\n"
    ".Lk_pg82:\n"
    "buffer_load_dwordx2 v[244:245], v241, s[8:11], 0 offen offset:1024 sc1\n"
    "s_waitcnt vmcnt(0)\n"
    "v_cmp_eq_u64 vcc, v[244:245], v[242:243]\n"
    "s_cmp_eq_u64 vcc, exec\n"
    "s_cbranch_scc1 .Lk_pgd83\n"
    "s_sub_u32 s54, s54, 1\n"
    "s_cbranch_scc0 .Lk_pg82\n"
    "s_mov_b32 s54, 0\n"
    ".Lk_pgd83:\n"
    ".Lk_x81:\n"
    "s_barrier\n"
    "s_add_u32 s58, s72, 4096\n"
    "s_mul_i32 s56, s52, 0x80\n"
    "s_add_u32 s56, s56, 0x27680\n"
    "s_mul_i32 s59, s52, 0x5000\n"
    "s_mul_i32 s70, s49, 0x1400\n"
    "s_add_u32 s59, s59, s70\n"
    "s_mul_i32 s70, s50, 0x800\n"
    "s_add_u32 s59, s59, s70\n"
    "s_mul_i32 s60, s52, 0x5000\n"
    "s_mul_i32 s70, s49, 0x1400\n"
    "s_add_u32 s60, s60, s70\n"
    "s_mul_i32 s70, s50, 0x200\n"
    "s_add_u32 s60, s60, s70\n"
    "s_add_u32 s60, s60, 0x1000\n"
    "s_barrier\n"
    "ds_read_b128 v[160:163], v255\n"
    "ds_read_b128 v[164:167], v255 offset:20480\n"
    "ds_read_b128 v[168:171], v246\n"
    "ds_read_b128 v[172:175], v246 offset:20480\n"
    "ds_read_b128 v[176:179], v255 offset:1024\n"
    "ds_read_b128 v[180:183], v255 offset:21504\n"
    "ds_read_b128 v[184:187], v246 offset:1024\n"
    "ds_read_b128 v[188:191], v246 offset:21504\n"
    "s_waitcnt lgkmcnt(7)\n"
    "v_mfma_f32_16x16x32_bf16 v[192:195], v[0:3], v[160:163], 0\n"
    "v_mfma_f32_16x16x32_bf16 v[208:211], v[4:7], v[160:163], 0\n"
    "ds_read_b128 v[160:163], v255 offset:2048\n"
    "s_waitcnt lgkmcnt(7)\n"
    "v_mfma_f32_16x16x32_bf16 v[196:199], v[0:3], v[164:167], 0\n"
    "v_mfma_f32_16x16x32_bf16 v[212:215], v[4:7], v[164:167], 0\n"
    "ds_read_b128 v[164:167], v255 offset:22528\n"
    "s_waitcnt lgkmcnt(7)\n"
    "v_mfma_f32_16x16x32_bf16 v[200:203], v[0:3], v[168:171], 0\n"
    "v_mfma_f32_16x16x32_bf16 v[216:219], v[4:7], v[168:171], 0\n"
    "ds_read_b128 v[168:171], v246 offset:2048\n"
    "s_waitcnt lgkmcnt(7)\n"
    "v_mfma_f32_16x16x32_bf16 v[204:207], v[0:3], v[172:175], 0\n"
    "v_mfma_f32_16x16x32_bf16 v[220:223], v[4:7], v[172:175], 0\n"
    "ds_read_b128 v[172:175], v246 offset:22528\n"
    "s_barrier\n"
    "s_waitcnt lgkmcnt(7)\n"
    "v_mfma_f32_16x16x32_bf16 v[192:195], v[8:11], v[176:179], v[192:195]\n"
    "v_mfma_f32_16x16x32_bf16 v[208:211], v[12:15], v[176:179], v[208:211]\n"
    "ds_read_b128 v[176:179], v255 offset:3072\n"
    "s_waitcnt lgkmcnt(7)\n"
    "v_mfma_f32_16x16x32_bf16 v[196:199], v[8:11], v[180:183], v[196:199]\n"
    "v_mfma_f32_16x16x32_bf16 v[212:215], v[12:15], v[180:183], v[212:215]\n"
    "ds_read_b128 v[180:183], v255 offset:23552\n"
    "s_waitcnt lgkmcnt(7)\n"
    "v_mfma_f32_16x16x32_bf16 v[200:203], v[8:11], v[184:187], v[200:203]\n"
    "v_mfma_f32_16x16x32_bf16 v[216:219], v[12:15], v[184:187], v[216:219]\n"
    "ds_read_b128 v[184:187], v246 offset:3072\n"
    "s_waitcnt lgkmcnt(7)\n"
    "v_mfma_f32_16x16x32_bf16 v[204:207], v[8:11], v[188:191], v[204:207]\n"
    "v_mfma_f32_16x16x32_bf16 v[220:223], v[12:15], v[188:191], v[220:223]\n"
    "ds_read_b128 v[188:191], v246 offset:23552\n"
    "s_waitcnt lgkmcnt(7)\n"
    "v_mfma_f32_16x16x32_bf16 v[192:195], v[16:19], v[160:163], v[192:195]\n"
    "v_mfma_f32_16x16x32_bf16 v[208:211], v[20:23], v[160:163], v[208:211]\n"
    "ds_read_b128 v[160:163], v255 offset:4096\n"
    "s_waitcnt lgkmcnt(7)\n"
    "v_mfma_f32_16x16x32_bf16 v[196:199], v[16:19], v[164:167], v[196:199]\n"
    "v_mfma_f32_16x16x32_bf16 v[212:215], v[20:23], v[164:167], v[212:215]\n"
    "ds_read_b128 v[164:167], v255 offset:24576\n"
    "s_waitcnt lgkmcnt(7)\n"
    "v_mfma_f32_16x16x32_bf16 v[200:203], v[16:19], v[168:171], v[200:203]\n"
    "v_mfma_f32_16x16x32_bf16 v[216:219], v[20:23], v[168:171], v[216:219]\n"
    "ds_read_b128 v[168:171], v246 offset:4096\n"
    "s_waitcnt lgkmcnt(7)\n"
    "v_mfma_f32_16x16x32_bf16 v[204:207], v[16:19], v[172:175], v[204:207]\n"
    "v_mfma_f32_16x16x32_bf16 v[220:223], v[20:23], v[172:175], v[220:223]\n"
    "ds_read_b128 v[172:175], v246 offset:24576\n"
    "s_waitcnt lgkmcnt(7)\n"
    "v_mfma_f32_16x16x32_bf16 v[192:195], v[24:27], v[176:179], v[192:195]\n"
    "v_mfma_f32_16x16x32_bf16 v[208:211], v[28:31], v[176:179], v[208:211]\n"
    "ds_read_b128 v[176:179], v255 offset:5120\n"
    "s_waitcnt lgkmcnt(7)\n"
    "v_mfma_f32_16x16x32_bf16 v[196:199], v[24:27], v[180:183], v[196:199]\n"
    "v_mfma_f32_16x16x32_bf16 v[212:215], v[28:31], v[180:183], v[212:215]\n"
    "ds_read_b128 v[180:183], v255 offset:25600\n"
    "s_waitcnt lgkmcnt(7)\n"
    "v_mfma_f32_16x16x32_bf16 v[200:203], v[24:27], v[184:187], v[200:203]\n"
    "v_mfma_f32_16x16x32_bf16 v[216:219], v[28:31], v[184:187], v[216:219]\n"
    "ds_read_b128 v[184:187], v246 offset:5120\n"
    "s_waitcnt lgkmcnt(7)\n"
    "v_mfma_f32_16x16x32_bf16 v[204:207], v[24:27], v[188:191], v[204:207]\n"
    "v_mfma_f32_16x16x32_bf16 v[220:223], v[28:31], v[188:191], v[220:223]\n"
    "ds_read_b128 v[188:191], v246 offset:25600\n"
    "s_barrier\n"
    "s_waitcnt lgkmcnt(7)\n"
    "v_mfma_f32_16x16x32_bf16 v[192:195], v[32:35], v[160:163], v[192:195]\n"
    "v_mfma_f32_16x16x32_bf16 v[208:211], v[36:39], v[160:163], v[208:211]\n"
    "ds_read_b128 v[160:163], v255 offset:6144\n"
    "s_waitcnt lgkmcnt(7)\n"
    "v_mfma_f32_16x16x32_bf16 v[196:199], v[32:35], v[164:167], v[196:199]\n"
    "v_mfma_f32_16x16x32_bf16 v[212:215], v[36:39], v[164:167], v[212:215]\n"
    "ds_read_b128 v[164:167], v255 offset:26624\n"
    "s_waitcnt lgkmcnt(7)\n"
    "v_mfma_f32_16x16x32_bf16 v[200:203], v[32:35], v[168:171], v[200:203]\n"
    "v_mfma_f32_16x16x32_bf16 v[216:219], v[36:39], v[168:171], v[216:219]\n"
    "ds_read_b128 v[168:171], v246 offset:6144\n"
    "s_waitcnt lgkmcnt(7)\n"
    "v_mfma_f32_16x16x32_bf16 v[204:207], v[32:35], v[172:175], v[204:207]\n"
    "v_mfma_f32_16x16x32_bf16 v[220:223], v[36:39], v[172:175], v[220:223]\n"
    "ds_read_b128 v[172:175], v246 offset:26624\n"
    "s_waitcnt lgkmcnt(7)\n"
    "v_mfma_f32_16x16x32_bf16 v[192:195], v[40:43], v[176:179], v[192:195]\n"
    "v_mfma_f32_16x16x32_bf16 v[208:211], v[44:47], v[176:179], v[208:211]\n"
    "ds_read_b128 v[176:179], v255 offset:7168\n"
    "s_waitcnt lgkmcnt(7)\n"
    "v_mfma_f32_16x16x32_bf16 v[196:199], v[40:43], v[180:183], v[196:199]\n"
    "v_mfma_f32_16x16x32_bf16 v[212:215], v[44:47], v[180:183], v[212:215]\n"
    "ds_read_b128 v[180:183], v255 offset:27648\n"
    "s_waitcnt lgkmcnt(7)\n"
    "v_mfma_f32_16x16x32_bf16 v[200:203], v[40:43], v[184:187], v[200:203]\n"
    "v_mfma_f32_16x16x32_bf16 v[216:219], v[44:47], v[184:187], v[216:219]\n"
    "ds_read_b128 v[184:187], v246 offset:7168\n"
    "s_waitcnt lgkmcnt(7)\n"
    "v_mfma_f32_16x16x32_bf16 v[204:207], v[40:43], v[188:191], v[204:207]\n"
    "v_mfma_f32_16x16x32_bf16 v[220:223], v[44:47], v[188:191], v[220:223]\n"
    "ds_read_b128 v[188:191], v246 offset:27648\n"
    "s_waitcnt lgkmcnt(7)\n"
    "v_mfma_f32_16x16x32_bf16 v[192:195], v[48:51], v[160:163], v[192:195]\n"
    "v_mfma_f32_16x16x32_bf16 v[208:211], v[52:55], v[160:163], v[208:211]\n"
    "ds_read_b128 v[160:163], v255 offset:8192\n"
    "s_waitcnt lgkmcnt(7)\n"
    "v_mfma_f32_16x16x32_bf16 v[196:199], v[48:51], v[164:167], v[196:199]\n"
    "v_mfma_f32_16x16x32_bf16 v[212:215], v[52:55], v[164:167], v[212:215]\n"
    "ds_read_b128 v[164:167], v255 offset:28672\n"
    "s_waitcnt lgkmcnt(7)\n"
    "v_mfma_f32_16x16x32_bf16 v[200:203], v[48:51], v[168:171], v[200:203]\n"
    "v_mfma_f32_16x16x32_bf16 v[216:219], v[52:55], v[168:171], v[216:219]\n"
    "ds_read_b128 v[168:171], v246 offset:8192\n"
    "s_waitcnt lgkmcnt(7)\n"
    "v_mfma_f32_16x16x32_bf16 v[204:207], v[48:51], v[172:175], v[204:207]\n"
    "v_mfma_f32_16x16x32_bf16 v[220:223], v[52:55], v[172:175], v[220:223]\n"
    "ds_read_b128 v[172:175], v246 offset:28672\n"
    "s_barrier\n"
    "s_waitcnt lgkmcnt(7)\n"
    "v_mfma_f32_16x16x32_bf16 v[192:195], v[56:59], v[176:179], v[192:195]\n"
    "v_mfma_f32_16x16x32_bf16 v[208:211], v[60:63], v[176:179], v[208:211]\n"
    "ds_read_b128 v[176:179], v255 offset:9216\n"
    "s_waitcnt lgkmcnt(7)\n"
    "v_mfma_f32_16x16x32_bf16 v[196:199], v[56:59], v[180:183], v[196:199]\n"
    "v_mfma_f32_16x16x32_bf16 v[212:215], v[60:63], v[180:183], v[212:215]\n"
    "ds_read_b128 v[180:183], v255 offset:29696\n"
    "s_waitcnt lgkmcnt(7)\n"
    "v_mfma_f32_16x16x32_bf16 v[200:203], v[56:59], v[184:187], v[200:203]\n"
    "v_mfma_f32_16x16x32_bf16 v[216:219], v[60:63], v[184:187], v[216:219]\n"
    "ds_read_b128 v[184:187], v246 offset:9216\n"
    "s_waitcnt lgkmcnt(7)\n"
    "v_mfma_f32_16x16x32_bf16 v[204:207], v[56:59], v[188:191], v[204:207]\n"
    "v_mfma_f32_16x16x32_bf16 v[220:223], v[60:63], v[188:191], v[220:223]\n"
    "ds_read_b128 v[188:191], v246 offset:29696\n"
    "s_waitcnt lgkmcnt(7)\n"
    "v_mfma_f32_16x16x32_bf16 v[192:195], v[64:67], v[160:163], v[192:195]\n"
    "v_mfma_f32_16x16x32_bf16 v[208:211], v[68:71], v[160:163], v[208:211]\n"
    "ds_read_b128 v[160:163], v255 offset:10240\n"
    "s_waitcnt lgkmcnt(7)\n"
    "v_mfma_f32_16x16x32_bf16 v[196:199], v[64:67], v[164:167], v[196:199]\n"
    "v_mfma_f32_16x16x32_bf16 v[212:215], v[68:71], v[164:167], v[212:215]\n"
    "ds_read_b128 v[164:167], v255 offset:30720\n"
    "buffer_load_dwordx4 v[0:3], v255, s[4:7], s58 offen sc1\n"
    "s_waitcnt lgkmcnt(7)\n"
    "v_mfma_f32_16x16x32_bf16 v[200:203], v[64:67], v[168:171], v[200:203]\n"
    "v_mfma_f32_16x16x32_bf16 v[216:219], v[68:71], v[168:171], v[216:219]\n"
    "ds_read_b128 v[168:171], v246 offset:10240\n"
    "s_waitcnt lgkmcnt(7)\n"
    "v_mfma_f32_16x16x32_bf16 v[204:207], v[64:67], v[172:175], v[204:207]\n"
    "v_mfma_f32_16x16x32_bf16 v[220:223], v[68:71], v[172:175], v[220:223]\n"
    "ds_read_b128 v[172:175], v246 offset:30720\n"
    "buffer_load_dwordx4 v[4:7], v255, s[4:7], s58 offen offset:1024 sc1\n"
    "s_waitcnt lgkmcnt(7)\n"
    "v_mfma_f32_16x16x32_bf16 v[192:195], v[72:75], v[176:179], v[192:195]\n"
    "v_mfma_f32_16x16x32_bf16 v[208:211], v[76:79], v[176:179], v[208:211]\n"
    "ds_read_b128 v[176:179], v255 offset:11264\n"
    "s_waitcnt lgkmcnt(7)\n"
    "v_mfma_f32_16x16x32_bf16 v[196:199], v[72:75], v[180:183], v[196:199]\n"
    "v_mfma_f32_16x16x32_bf16 v[212:215], v[76:79], v[180:183], v[212:215]\n"
    "ds_read_b128 v[180:183], v255 offset:31744\n"
    "buffer_load_dwordx4 v[8:11], v255, s[4:7], s58 offen offset:2048 sc1\n"
    "s_waitcnt lgkmcnt(7)\n"
    "v_mfma_f32_16x16x32_bf16 v[200:203], v[72:75], v[184:187], v[200:203]\n"
    "v_mfma_f32_16x16x32_bf16 v[216:219], v[76:79], v[184:187], v[216:219]\n"
    "ds_read_b128 v[184:187], v246 offset:11264\n"
    "s_waitcnt lgkmcnt(7)\n"
    "v_mfma_f32_16x16x32_bf16 v[204:207], v[72:75], v[188:191], v[204:207]\n"
    "v_mfma_f32_16x16x32_bf16 v[220:223], v[76:79], v[188:191], v[220:223]\n"
    "ds_read_b128 v[188:191], v246 offset:31744\n"
    "s_barrier\n"
    "s_waitcnt lgkmcnt(7)\n"
    "v_mfma_f32_16x16x32_bf16 v[192:195], v[80:83], v[160:163], v[192:195]\n"
    "v_mfma_f32_16x16x32_bf16 v[208:211], v[84:87], v[160:163], v[208:211]\n"
    "ds_read_b128 v[160:163], v255 offset:12288\n"
    "s_waitcnt lgkmcnt(7)\n"
    "v_mfma_f32_16x16x32_bf16 v[196:199], v[80:83], v[164:167], v[196:199]\n"
    "v_mfma_f32_16x16x32_bf16 v[212:215], v[84:87], v[164:167], v[212:215]\n"
    "ds_read_b128 v[164:167], v255 offset:32768\n"
    "buffer_load_dwordx4 v[12:15], v255, s[4:7], s58 offen offset:3072 sc1\n"
    "s_add_u32 s58, s58, 4096\n"
    "s_waitcnt lgkmcnt(7)\n"
    "v_mfma_f32_16x16x32_bf16 v[200:203], v[80:83], v[168:171], v[200:203]\n"
    "v_mfma_f32_16x16x32_bf16 v[216:219], v[84:87], v[168:171], v[216:219]\n"
    "ds_read_b128 v[168:171], v246 offset:12288\n"
    "s_waitcnt lgkmcnt(7)\n"
    "v_mfma_f32_16x16x32_bf16 v[204:207], v[80:83], v[172:175], v[204:207]\n"
    "v_mfma_f32_16x16x32_bf16 v[220:223], v[84:87], v[172:175], v[220:223]\n"
    "ds_read_b128 v[172:175], v246 offset:32768\n"
    "buffer_load_dwordx4 v[16:19], v255, s[4:7], s58 offen sc1\n"
    "s_waitcnt lgkmcnt(7)\n"
    "v_mfma_f32_16x16x32_bf16 v[192:195], v[88:91], v[176:179], v[192:195]\n"
    "v_mfma_f32_16x16x32_bf16 v[208:211], v[92:95], v[176:179], v[208:211]\n"
    "ds_read_b128 v[176:179], v255 offset:13312\n"
    "s_waitcnt lgkmcnt(7)\n"
    "v_mfma_f32_16x16x32_bf16 v[196:199], v[88:91], v[180:183], v[196:199]\n"
    "v_mfma_f32_16x16x32_bf16 v[212:215], v[92:95], v[180:183], v[212:215]\n"
    "ds_read_b128 v[180:183], v255 offset:33792\n"
    "buffer_load_dwordx4 v[20:23], v255, s[4:7], s58 offen offset:1024 sc1\n"
    "s_waitcnt lgkmcnt(7)\n"
    "v_mfma_f32_16x16x32_bf16 v[200:203], v[88:91], v[184:187], v[200:203]\n"
    "v_mfma_f32_16x16x32_bf16 v[216:219], v[92:95], v[184:187], v[216:219]\n"
    "ds_read_b128 v[184:187], v246 offset:13312\n"
    "s_waitcnt lgkmcnt(7)\n"
    "v_mfma_f32_16x16x32_bf16 v[204:207], v[88:91], v[188:191], v[204:207]\n"
    "v_mfma_f32_16x16x32_bf16 v[220:223], v[92:95], v[188:191], v[220:223]\n"
    "ds_read_b128 v[188:191], v246 offset:33792\n"
    "s_waitcnt lgkmcnt(7)\n"
    "v_mfma_f32_16x16x32_bf16 v[192:195], v[96:99], v[160:163], v[192:195]\n"
    "v_mfma_f32_16x16x32_bf16 v[208:211], v[100:103], v[160:163], v[208:211]\n"
    "ds_read_b128 v[160:163], v255 offset:14336\n"
    "s_waitcnt lgkmcnt(7)\n"
    "v_mfma_f32_16x16x32_bf16 v[196:199], v[96:99], v[164:167], v[196:199]\n"
    "v_mfma_f32_16x16x32_bf16 v[212:215], v[100:103], v[164:167], v[212:215]\n"
    "ds_read_b128 v[164:167], v255 offset:34816\n"
    "buffer_load_dwordx4 v[24:27], v255, s[4:7], s58 offen offset:2048 sc1\n"
    "s_waitcnt lgkmcnt(7)\n"
    "v_mfma_f32_16x16x32_bf16 v[200:203], v[96:99], v[168:171], v[200:203]\n"
    "v_mfma_f32_16x16x32_bf16 v[216:219], v[100:103], v[168:171], v[216:219]\n"
    "ds_read_b128 v[168:171], v246 offset:14336\n"
    "s_waitcnt lgkmcnt(7)\n"
    "v_mfma_f32_16x16x32_bf16 v[204:207], v[96:99], v[172:175], v[204:207]\n"
    "v_mfma_f32_16x16x32_bf16 v[220:223], v[100:103], v[172:175], v[220:223]\n"
    "ds_read_b128 v[172:175], v246 offset:34816\n"
    "buffer_load_dwordx4 v[28:31], v255, s[4:7], s58 offen offset:3072 sc1\n"
    "s_add_u32 s58, s58, 4096\n"
    "s_waitcnt lgkmcnt(7)\n"
    "v_mfma_f32_16x16x32_bf16 v[192:195], v[104:107], v[176:179], v[192:195]\n"
    "v_lshl_add_u32 v72, v249, 4, s56\n"
    "v_mfma_f32_16x16x32_bf16 v[208:211], v[108:111], v[176:179], v[208:211]\n"
    "v_mov_b32 v73, 160384\n"
    "ds_read_b128 v[176:179], v255 offset:15360\n"
    "s_waitcnt lgkmcnt(7)\n"
    "v_mfma_f32_16x16x32_bf16 v[196:199], v[104:107], v[180:183], v[196:199]\n"
    "v_lshl_add_u32 v73, v248, 4, v73\n"
    "v_mfma_f32_16x16x32_bf16 v[212:215], v[108:111], v[180:183], v[212:215]\n"
    "v_mov_b32 v74, 0xc0135761\n"
    "ds_read_b128 v[180:183], v255 offset:35840\n"
    "buffer_load_dwordx4 v[32:35], v255, s[4:7], s58 offen sc1\n"
    "s_waitcnt lgkmcnt(7)\n"
    "v_mfma_f32_16x16x32_bf16 v[200:203], v[104:107], v[184:187], v[200:203]\n"
    "v_mov_b32 v75, 0xc0135761\n"
    "v_mfma_f32_16x16x32_bf16 v[216:219], v[108:111], v[184:187], v[216:219]\n"
    "v_mov_b32 v76, 0xbdd2d3e8\n"
    "ds_read_b128 v[184:187], v246 offset:15360\n"
    "s_waitcnt lgkmcnt(7)\n"
    "v_mfma_f32_16x16x32_bf16 v[204:207], v[104:107], v[188:191], v[204:207]\n"
    "v_mov_b32 v77, 0xbdd2d3e8\n"
    "v_mfma_f32_16x16x32_bf16 v[220:223], v[108:111], v[188:191], v[220:223]\n"
    "v_mov_b32_e64 v78, 0x3f800000\n"
    "ds_read_b128 v[188:191], v246 offset:35840\n"
    "s_barrier\n"
    "s_waitcnt lgkmcnt(7)\n"
    "v_mfma_f32_16x16x32_bf16 v[192:195], v[112:115], v[160:163], v[192:195]\n"
    "v_mov_b32_e64 v79, 0x3f800000\n"
    "v_mfma_f32_16x16x32_bf16 v[208:211], v[116:119], v[160:163], v[208:211]\n"
    "ds_read_b128 v[160:163], v255 offset:16384\n"
    "s_waitcnt lgkmcnt(7)\n"
    "v_mfma_f32_16x16x32_bf16 v[196:199], v[112:115], v[164:167], v[196:199]\n"
    "v_mfma_f32_16x16x32_bf16 v[212:215], v[116:119], v[164:167], v[212:215]\n"
    "ds_read_b128 v[164:167], v255 offset:36864\n"
    "buffer_load_dwordx4 v[36:39], v255, s[4:7], s58 offen offset:1024 sc1\n"
    "s_waitcnt lgkmcnt(7)\n"
    "v_mfma_f32_16x16x32_bf16 v[200:203], v[112:115], v[168:171], v[200:203]\n"
    "v_mfma_f32_16x16x32_bf16 v[216:219], v[116:119], v[168:171], v[216:219]\n"
    "ds_read_b128 v[168:171], v246 offset:16384\n"
    "s_waitcnt lgkmcnt(7)\n"
    "v_mfma_f32_16x16x32_bf16 v[204:207], v[112:115], v[172:175], v[204:207]\n"
    "v_mfma_f32_16x16x32_bf16 v[220:223], v[116:119], v[172:175], v[220:223]\n"
    "ds_read_b128 v[172:175], v246 offset:36864\n"
    "buffer_load_dwordx4 v[40:43], v255, s[4:7], s58 offen offset:2048 sc1\n"
    "s_waitcnt lgkmcnt(7)\n"
    "v_mfma_f32_16x16x32_bf16 v[192:195], v[120:123], v[176:179], v[192:195]\n"
    "v_mfma_f32_16x16x32_bf16 v[208:211], v[124:127], v[176:179], v[208:211]\n"
    "ds_read_b128 v[176:179], v255 offset:17408\n"
    "s_waitcnt lgkmcnt(7)\n"
    "v_mfma_f32_16x16x32_bf16 v[196:199], v[120:123], v[180:183], v[196:199]\n"
    "v_mfma_f32_16x16x32_bf16 v[212:215], v[124:127], v[180:183], v[212:215]\n"
    "ds_read_b128 v[180:183], v255 offset:37888\n"
    "buffer_load_dwordx4 v[44:47], v255, s[4:7], s58 offen offset:3072 sc1\n"
    "s_add_u32 s58, s58, 4096\n"
    "s_waitcnt lgkmcnt(7)\n"
    "v_mfma_f32_16x16x32_bf16 v[200:203], v[120:123], v[184:187], v[200:203]\n"
    "v_mfma_f32_16x16x32_bf16 v[216:219], v[124:127], v[184:187], v[216:219]\n"
    "ds_read_b128 v[184:187], v246 offset:17408\n"
    "s_waitcnt lgkmcnt(7)\n"
    "v_mfma_f32_16x16x32_bf16 v[204:207], v[120:123], v[188:191], v[204:207]\n"
    "v_mfma_f32_16x16x32_bf16 v[220:223], v[124:127], v[188:191], v[220:223]\n"
    "ds_read_b128 v[188:191], v246 offset:37888\n"
    "s_waitcnt lgkmcnt(7)\n"
    "v_mfma_f32_16x16x32_bf16 v[192:195], v[128:131], v[160:163], v[192:195]\n"
    "v_mfma_f32_16x16x32_bf16 v[208:211], v[132:135], v[160:163], v[208:211]\n"
    "ds_read_b128 v[160:163], v255 offset:18432\n"
    "s_waitcnt lgkmcnt(7)\n"
    "v_mfma_f32_16x16x32_bf16 v[196:199], v[128:131], v[164:167], v[196:199]\n"
    "v_mfma_f32_16x16x32_bf16 v[212:215], v[132:135], v[164:167], v[212:215]\n"
    "ds_read_b128 v[164:167], v255 offset:38912\n"
    "buffer_load_dwordx4 v[48:51], v255, s[4:7], s58 offen sc1\n"
    "s_waitcnt lgkmcnt(7)\n"
    "v_mfma_f32_16x16x32_bf16 v[200:203], v[128:131], v[168:171], v[200:203]\n"
    "v_mfma_f32_16x16x32_bf16 v[216:219], v[132:135], v[168:171], v[216:219]\n"
    "ds_read_b128 v[168:171], v246 offset:18432\n"
    "s_waitcnt lgkmcnt(7)\n"
    "v_mfma_f32_16x16x32_bf16 v[204:207], v[128:131], v[172:175], v[204:207]\n"
    "v_mfma_f32_16x16x32_bf16 v[220:223], v[132:135], v[172:175], v[220:223]\n"
    "ds_read_b128 v[172:175], v246 offset:38912\n"
    "buffer_load_dwordx4 v[52:55], v255, s[4:7], s58 offen offset:1024 sc1\n"
    "s_barrier\n"
    "s_waitcnt lgkmcnt(7)\n"
    "v_mfma_f32_16x16x32_bf16 v[192:195], v[136:139], v[176:179], v[192:195]\n"
    "ds_read_b128 v[80:83], v73\n"
    "v_mfma_f32_16x16x32_bf16 v[208:211], v[140:143], v[176:179], v[208:211]\n"
    "ds_read_b128 v[84:87], v73 offset:256\n"
    "ds_read_b128 v[176:179], v255 offset:19456\n"
    "s_waitcnt lgkmcnt(9)\n"
    "v_mfma_f32_16x16x32_bf16 v[196:199], v[136:139], v[180:183], v[196:199]\n"
    "ds_read_b128 v[88:91], v73 offset:512\n"
    "v_mfma_f32_16x16x32_bf16 v[212:215], v[140:143], v[180:183], v[212:215]\n"
    "ds_read_b128 v[92:95], v73 offset:768\n"
    "ds_read_b128 v[180:183], v255 offset:39936\n"
    "buffer_load_dwordx4 v[56:59], v255, s[4:7], s58 offen offset:2048 sc1\n"
    "s_waitcnt lgkmcnt(11)\n"
    "v_mfma_f32_16x16x32_bf16 v[200:203], v[136:139], v[184:187], v[200:203]\n"
    "ds_read_b128 v[96:99], v72\n"
    "v_mfma_f32_16x16x32_bf16 v[216:219], v[140:143], v[184:187], v[216:219]\n"
    "ds_read_b128 v[100:103], v72 offset:1024\n"
    "ds_read_b128 v[184:187], v246 offset:19456\n"
    "s_waitcnt lgkmcnt(13)\n"
    "v_mfma_f32_16x16x32_bf16 v[204:207], v[136:139], v[188:191], v[204:207]\n"
    "ds_read_b128 v[104:107], v72 offset:64\n"
    "v_mfma_f32_16x16x32_bf16 v[220:223], v[140:143], v[188:191], v[220:223]\n"
    "ds_read_b128 v[108:111], v72 offset:1088\n"
    "ds_read_b128 v[188:191], v246 offset:39936\n"
    "s_waitcnt lgkmcnt(15)\n"
    "v_mfma_f32_16x16x32_bf16 v[192:195], v[144:147], v[160:163], v[192:195]\n"
    "v_add_u32 v112, 262144, v250\n"
    "v_mfma_f32_16x16x32_bf16 v[208:211], v[148:151], v[160:163], v[208:211]\n"
    "v_lshrrev_b32 v115, 1, v249\n"
    "s_waitcnt lgkmcnt(14)\n"
    "v_mfma_f32_16x16x32_bf16 v[196:199], v[144:147], v[164:167], v[196:199]\n"
    "v_lshl_add_u32 v115, v115, 8, s60\n"
    "v_mfma_f32_16x16x32_bf16 v[212:215], v[148:151], v[164:167], v[212:215]\n"
    "v_lshl_add_u32 v115, v248, 4, v115\n"
    "buffer_load_dwordx4 v[60:63], v255, s[4:7], s58 offen offset:3072 sc1\n"
    "s_add_u32 s58, s58, 4096\n"
    "s_waitcnt lgkmcnt(13)\n"
    "v_mfma_f32_16x16x32_bf16 v[200:203], v[144:147], v[168:171], v[200:203]\n"
    "v_and_b32_e64 v113, 1, v249\n"
    "v_mfma_f32_16x16x32_bf16 v[216:219], v[148:151], v[168:171], v[216:219]\n"
    "v_lshl_add_u32 v115, v113, 3, v115\n"
    "s_waitcnt lgkmcnt(12)\n"
    "v_mfma_f32_16x16x32_bf16 v[204:207], v[144:147], v[172:175], v[204:207]\n"
    "v_mfma_f32_16x16x32_bf16 v[220:223], v[148:151], v[172:175], v[220:223]\n"
    "buffer_load_dwordx4 v[64:67], v255, s[4:7], s58 offen sc1\n"
    "s_waitcnt lgkmcnt(9)\n"
    "v_mfma_f32_16x16x32_bf16 v[192:195], v[152:155], v[176:179], v[192:195]\n"
    "v_add_u32_e64 v114, s59, v255\n"
    "v_mfma_f32_16x16x32_bf16 v[208:211], v[156:159], v[176:179], v[208:211]\n"
    "ds_read_b128 v[164:167], v114\n"
    "s_waitcnt lgkmcnt(7)\n"
    "v_mfma_f32_16x16x32_bf16 v[196:199], v[152:155], v[180:183], v[196:199]\n"
    "ds_read_b128 v[168:171], v114 offset:1024\n"
    "v_mfma_f32_16x16x32_bf16 v[212:215], v[156:159], v[180:183], v[212:215]\n"
    "ds_read_b64 v[172:173], v115\n"
    "buffer_load_dwordx4 v[68:71], v255, s[4:7], s58 offen offset:1024 sc1\n"
    ".Lk_pad4:\n"
    "s_cbranch_execz .Lk_tramp\n"
    "s_waitcnt lgkmcnt(6)\n"
    "v_mfma_f32_16x16x32_bf16 v[200:203], v[152:155], v[184:187], v[200:203]\n"
    "v_mfma_f32_16x16x32_bf16 v[216:219], v[156:159], v[184:187], v[216:219]\n"
    "s_waitcnt lgkmcnt(3)\n"
    "v_mfma_f32_16x16x32_bf16 v[204:207], v[152:155], v[188:191], v[204:207]\n"
    "v_mfma_f32_16x16x32_bf16 v[220:223], v[156:159], v[188:191], v[220:223]\n"
    "v_pk_fma_f32 v[116:117], v[80:81], v[96:97], v[100:101] neg_lo:[1,0,0] neg_hi:[1,0,0]\n"
    "v_pk_fma_f32 v[118:119], v[80:81], v[98:99], v[102:103] neg_lo:[1,0,0] neg_hi:[1,0,0]\n"
    "v_pk_fma_f32 v[120:121], v[84:85], v[96:97], v[100:101] neg_lo:[1,0,0] neg_hi:[1,0,0]\n"
    "v_pk_fma_f32 v[122:123], v[84:85], v[98:99], v[102:103] neg_lo:[1,0,0] neg_hi:[1,0,0]\n"
    "v_pk_fma_f32 v[116:117], v[82:83], v[192:193], v[116:117]\n"
    "v_pk_fma_f32 v[118:119], v[82:83], v[194:195], v[118:119]\n"
    "v_pk_fma_f32 v[120:121], v[86:87], v[196:197], v[120:121]\n"
    "v_pk_fma_f32 v[122:123], v[86:87], v[198:199], v[122:123]\n"
    "v_pk_mul_f32 v[124:125], v[116:117], v[116:117]\n"
    "v_pk_mul_f32 v[126:127], v[118:119], v[118:119]\n"
    "v_pk_mul_f32 v[128:129], v[120:121], v[120:121]\n"
    "v_pk_mul_f32 v[130:131], v[122:123], v[122:123]\n"
    "v_pk_fma_f32 v[124:125], v[124:125], v[76:77], v[74:75]\n"
    "v_pk_fma_f32 v[126:127], v[126:127], v[76:77], v[74:75]\n"
    "v_pk_fma_f32 v[128:129], v[128:129], v[76:77], v[74:75]\n"
    "v_pk_fma_f32 v[130:131], v[130:131], v[76:77], v[74:75]\n"
    "v_pk_mul_f32 v[124:125], v[124:125], v[116:117]\n"
    "v_pk_mul_f32 v[126:127], v[126:127], v[118:119]\n"
    "v_pk_mul_f32 v[128:129], v[128:129], v[120:121]\n"
    "v_pk_mul_f32 v[130:131], v[130:131], v[122:123]\n"
    "v_exp_f32 v124, v124\n"
    "v_exp_f32 v125, v125\n"
    "v_exp_f32 v126, v126\n"
    "v_exp_f32 v127, v127\n"
    "v_exp_f32 v128, v128\n"
    "v_exp_f32 v129, v129\n"
    "v_exp_f32 v130, v130\n"
    "v_exp_f32 v131, v131\n"
    "v_pk_add_f32 v[124:125], v[124:125], v[78:79]\n"
    "v_pk_add_f32 v[126:127], v[126:127], v[78:79]\n"
    "v_pk_add_f32 v[128:129], v[128:129], v[78:79]\n"
    "v_pk_add_f32 v[130:131], v[130:131], v[78:79]\n"
    "v_rcp_f32 v124, v124\n"
    "v_rcp_f32 v125, v125\n"
    "v_rcp_f32 v126, v126\n"
    "v_rcp_f32 v127, v127\n"
    "v_rcp_f32 v128, v128\n"
    "v_rcp_f32 v129, v129\n"
    "v_rcp_f32 v130, v130\n"
    "v_rcp_f32 v131, v131\n"
    "v_pk_mul_f32 v[116:117], v[116:117], v[124:125]\n"
    "v_pk_mul_f32 v[118:119], v[118:119], v[126:127]\n"
    "v_pk_mul_f32 v[120:121], v[120:121], v[128:129]\n"
    "v_pk_mul_f32 v[122:123], v[122:123], v[130:131]\n"
    "v_cvt_pk_bf16_f32 v132, v116, v117\n"
    "v_cvt_pk_bf16_f32 v133, v118, v119\n"
    "v_cvt_pk_bf16_f32 v134, v120, v121\n"
    "v_cvt_pk_bf16_f32 v135, v122, v123\n"
    "s_nop 0\n"
    "v_permlane16_swap_b32 v132, v134\n"
    "v_permlane16_swap_b32 v133, v135\n"
    "s_nop 0\n"
    "buffer_store_dwordx4 v[132:135], v250, s[8:11], 0 offen sc1\n"
    "v_pk_fma_f32 v[136:137], v[88:89], v[96:97], v[100:101] neg_lo:[1,0,0] neg_hi:[1,0,0]\n"
    "v_pk_fma_f32 v[138:139], v[88:89], v[98:99], v[102:103] neg_lo:[1,0,0] neg_hi:[1,0,0]\n"
    "v_pk_fma_f32 v[140:141], v[92:93], v[96:97], v[100:101] neg_lo:[1,0,0] neg_hi:[1,0,0]\n"
    "v_pk_fma_f32 v[142:143], v[92:93], v[98:99], v[102:103] neg_lo:[1,0,0] neg_hi:[1,0,0]\n"
    "v_pk_fma_f32 v[136:137], v[90:91], v[200:201], v[136:137]\n"
    "v_pk_fma_f32 v[138:139], v[90:91], v[202:203], v[138:139]\n"
    "v_pk_fma_f32 v[140:141], v[94:95], v[204:205], v[140:141]\n"
    "v_pk_fma_f32 v[142:143], v[94:95], v[206:207], v[142:143]\n"
    "v_pk_mul_f32 v[144:145], v[136:137], v[136:137]\n"
    "v_pk_mul_f32 v[146:147], v[138:139], v[138:139]\n"
    "v_pk_mul_f32 v[148:149], v[140:141], v[140:141]\n"
    "v_pk_mul_f32 v[150:151], v[142:143], v[142:143]\n"
    "v_pk_fma_f32 v[144:145], v[144:145], v[76:77], v[74:75]\n"
    "v_pk_fma_f32 v[146:147], v[146:147], v[76:77], v[74:75]\n"
    "v_pk_fma_f32 v[148:149], v[148:149], v[76:77], v[74:75]\n"
    "v_pk_fma_f32 v[150:151], v[150:151], v[76:77], v[74:75]\n"
    "v_pk_mul_f32 v[144:145], v[144:145], v[136:137]\n"
    "v_pk_mul_f32 v[146:147], v[146:147], v[138:139]\n"
    "v_pk_mul_f32 v[148:149], v[148:149], v[140:141]\n"
    "v_pk_mul_f32 v[150:151], v[150:151], v[142:143]\n"
    "v_exp_f32 v144, v144\n"
    "v_exp_f32 v145, v145\n"
    "v_exp_f32 v146, v146\n"
    "v_exp_f32 v147, v147\n"
    "v_exp_f32 v148, v148\n"
    "v_exp_f32 v149, v149\n"
    "v_exp_f32 v150, v150\n"
    "v_exp_f32 v151, v151\n"
    "v_pk_add_f32 v[144:145], v[144:145], v[78:79]\n"
    "v_pk_add_f32 v[146:147], v[146:147], v[78:79]\n"
    "v_pk_add_f32 v[148:149], v[148:149], v[78:79]\n"
    "v_pk_add_f32 v[150:151], v[150:151], v[78:79]\n"
    "v_rcp_f32 v144, v144\n"
    "v_rcp_f32 v145, v145\n"
    "v_rcp_f32 v146, v146\n"
    "v_rcp_f32 v147, v147\n"
    "v_rcp_f32 v148, v148\n"
    "v_rcp_f32 v149, v149\n"
    "v_rcp_f32 v150, v150\n"
    "v_rcp_f32 v151, v151\n"
    "v_pk_mul_f32 v[136:137], v[136:137], v[144:145]\n"
    "v_pk_mul_f32 v[138:139], v[138:139], v[146:147]\n"
    "v_pk_mul_f32 v[140:141], v[140:141], v[148:149]\n"
    "v_pk_mul_f32 v[142:143], v[142:143], v[150:151]\n"
    "v_cvt_pk_bf16_f32 v152, v136, v137\n"
    "v_cvt_pk_bf16_f32 v153, v138, v139\n"
    "v_cvt_pk_bf16_f32 v154, v140, v141\n"
    "v_cvt_pk_bf16_f32 v155, v142, v143\n"
    "s_nop 0\n"
    "v_permlane16_swap_b32 v152, v154\n"
    "v_permlane16_swap_b32 v153, v155\n"
    "s_nop 0\n"
    "buffer_store_dwordx4 v[152:155], v112, s[8:11], 0 offen sc1\n"
    "v_pk_fma_f32 v[116:117], v[80:81], v[104:105], v[108:109] neg_lo:[1,0,0] neg_hi:[1,0,0]\n"
    "v_pk_fma_f32 v[118:119], v[80:81], v[106:107], v[110:111] neg_lo:[1,0,0] neg_hi:[1,0,0]\n"
    "v_pk_fma_f32 v[120:121], v[84:85], v[104:105], v[108:109] neg_lo:[1,0,0] neg_hi:[1,0,0]\n"
    "v_pk_fma_f32 v[122:123], v[84:85], v[106:107], v[110:111] neg_lo:[1,0,0] neg_hi:[1,0,0]\n"
    "v_pk_fma_f32 v[116:117], v[82:83], v[208:209], v[116:117]\n"
    "v_pk_fma_f32 v[118:119], v[82:83], v[210:211], v[118:119]\n"
    "v_pk_fma_f32 v[120:121], v[86:87], v[212:213], v[120:121]\n"
    "v_pk_fma_f32 v[122:123], v[86:87], v[214:215], v[122:123]\n"
    "v_pk_mul_f32 v[124:125], v[116:117], v[116:117]\n"
    "v_pk_mul_f32 v[126:127], v[118:119], v[118:119]\n"
    "v_pk_mul_f32 v[128:129], v[120:121], v[120:121]\n"
    "v_pk_mul_f32 v[130:131], v[122:123], v[122:123]\n"
    "v_pk_fma_f32 v[124:125], v[124:125], v[76:77], v[74:75]\n"
    "v_pk_fma_f32 v[126:127], v[126:127], v[76:77], v[74:75]\n"
    "v_pk_fma_f32 v[128:129], v[128:129], v[76:77], v[74:75]\n"
    "v_pk_fma_f32 v[130:131], v[130:131], v[76:77], v[74:75]\n"
    "v_pk_mul_f32 v[124:125], v[124:125], v[116:117]\n"
    "v_pk_mul_f32 v[126:127], v[126:127], v[118:119]\n"
    "v_pk_mul_f32 v[128:129], v[128:129], v[120:121]\n"
    "v_pk_mul_f32 v[130:131], v[130:131], v[122:123]\n"
    "v_exp_f32 v124, v124\n"
    "v_exp_f32 v125, v125\n"
    "v_exp_f32 v126, v126\n"
    "v_exp_f32 v127, v127\n"
    "v_exp_f32 v128, v128\n"
    "v_exp_f32 v129, v129\n"
    "v_exp_f32 v130, v130\n"
    "v_exp_f32 v131, v131\n"
    "v_pk_add_f32 v[124:125], v[124:125], v[78:79]\n"
    "v_pk_add_f32 v[126:127], v[126:127], v[78:79]\n"
    "v_pk_add_f32 v[128:129], v[128:129], v[78:79]\n"
    "v_pk_add_f32 v[130:131], v[130:131], v[78:79]\n"
    "v_rcp_f32 v124, v124\n"
    "v_rcp_f32 v125, v125\n"
    "v_rcp_f32 v126, v126\n"
    "v_rcp_f32 v127, v127\n"
    "v_rcp_f32 v128, v128\n"
    "v_rcp_f32 v129, v129\n"
    "v_rcp_f32 v130, v130\n"
    "v_rcp_f32 v131, v131\n"
    "v_pk_mul_f32 v[116:117], v[116:117], v[124:125]\n"
    "v_pk_mul_f32 v[118:119], v[118:119], v[126:127]\n"
    "v_pk_mul_f32 v[120:121], v[120:121], v[128:129]\n"
    "v_pk_mul_f32 v[122:123], v[122:123], v[130:131]\n"
    "v_cvt_pk_bf16_f32 v132, v116, v117\n"
    "v_cvt_pk_bf16_f32 v133, v118, v119\n"
    "v_cvt_pk_bf16_f32 v134, v120, v121\n"
    "v_cvt_pk_bf16_f32 v135, v122, v123\n"
    "s_nop 0\n"
    "v_permlane16_swap_b32 v132, v134\n"
    "v_permlane16_swap_b32 v133, v135\n"
    "s_nop 0\n"
    "buffer_store_dwordx4 v[132:135], v250, s[8:11], 0 offen offset:512 sc1\n"
    "v_pk_fma_f32 v[136:137], v[88:89], v[104:105], v[108:109] neg_lo:[1,0,0] neg_hi:[1,0,0]\n"
    "v_pk_fma_f32 v[138:139], v[88:89], v[106:107], v[110:111] neg_lo:[1,0,0] neg_hi:[1,0,0]\n"
    "v_pk_fma_f32 v[140:141], v[92:93], v[104:105], v[108:109] neg_lo:[1,0,0] neg_hi:[1,0,0]\n"
    "v_pk_fma_f32 v[142:143], v[92:93], v[106:107], v[110:111] neg_lo:[1,0,0] neg_hi:[1,0,0]\n"
    "v_pk_fma_f32 v[136:137], v[90:91], v[216:217], v[136:137]\n"
    "v_pk_fma_f32 v[138:139], v[90:91], v[218:219], v[138:139]\n"
    "v_pk_fma_f32 v[140:141], v[94:95], v[220:221], v[140:141]\n"
    "v_pk_fma_f32 v[142:143], v[94:95], v[222:223], v[142:143]\n"
    "v_pk_mul_f32 v[144:145], v[136:137], v[136:137]\n"
    "v_pk_mul_f32 v[146:147], v[138:139], v[138:139]\n"
    "v_pk_mul_f32 v[148:149], v[140:141], v[140:141]\n"
    "v_pk_mul_f32 v[150:151], v[142:143], v[142:143]\n"
    "v_pk_fma_f32 v[144:145], v[144:145], v[76:77], v[74:75]\n"
    "v_pk_fma_f32 v[146:147], v[146:147], v[76:77], v[74:75]\n"
    "v_pk_fma_f32 v[148:149], v[148:149], v[76:77], v[74:75]\n"
    "v_pk_fma_f32 v[150:151], v[150:151], v[76:77], v[74:75]\n"
    "v_pk_mul_f32 v[144:145], v[144:145], v[136:137]\n"
    "v_pk_mul_f32 v[146:147], v[146:147], v[138:139]\n"
    "v_pk_mul_f32 v[148:149], v[148:149], v[140:141]\n"
    "v_pk_mul_f32 v[150:151], v[150:151], v[142:143]\n"
    "v_exp_f32 v144, v144\n"
    "v_exp_f32 v145, v145\n"
    "v_exp_f32 v146, v146\n"
    "v_exp_f32 v147, v147\n"
    "v_exp_f32 v148, v148\n"
    "v_exp_f32 v149, v149\n"
    "v_exp_f32 v150, v150\n"
    "v_exp_f32 v151, v151\n"
    "v_pk_add_f32 v[144:145], v[144:145], v[78:79]\n"
    "v_pk_add_f32 v[146:147], v[146:147], v[78:79]\n"
    "v_pk_add_f32 v[148:149], v[148:149], v[78:79]\n"
    "v_pk_add_f32 v[150:151], v[150:151], v[78:79]\n"
    "v_rcp_f32 v144, v144\n"
    "v_rcp_f32 v145, v145\n"
    "v_rcp_f32 v146, v146\n"
    "v_rcp_f32 v147, v147\n"
    "v_rcp_f32 v148, v148\n"
    "v_rcp_f32 v149, v149\n"
    "v_rcp_f32 v150, v150\n"
    "v_rcp_f32 v151, v151\n"
    "v_pk_mul_f32 v[136:137], v[136:137], v[144:145]\n"
    "v_pk_mul_f32 v[138:139], v[138:139], v[146:147]\n"
    "v_pk_mul_f32 v[140:141], v[140:141], v[148:149]\n"
    "v_pk_mul_f32 v[142:143], v[142:143], v[150:151]\n"
    "v_cvt_pk_bf16_f32 v152, v136, v137\n"
    "v_cvt_pk_bf16_f32 v153, v138, v139\n"
    "v_cvt_pk_bf16_f32 v154, v140, v141\n"
    "v_cvt_pk_bf16_f32 v155, v142, v143\n"
    "s_nop 0\n"
    "v_permlane16_swap_b32 v152, v154\n"
    "v_permlane16_swap_b32 v153, v155\n"
    "buffer_store_dwordx4 v[152:155], v112, s[8:11], 0 offen offset:512 sc1\n"
    "s_waitcnt lgkmcnt(0)\n"
    "s_barrier\n"
    "s_setprio 2\n"
    "v_add_u32 v178, 512, v250\n"
    "v_add_u32 v179, 512, v112\n"
    "v_mov_b32 v180, v112\n"
    "v_mov_b64 v[216:217], v[164:165]\n"
    "v_mov_b64 v[218:219], v[166:167]\n"
    "v_mov_b64 v[220:221], v[168:169]\n"
    "v_mov_b64 v[222:223], v[170:171]\n"
    "v_mov_b64 v[248:249], v[172:173]\n"
    "v_add_u32_e64 v247, s73, v255\n"
    "v_add_u32 v246, 0x10000, v247\n"
    "s_waitcnt vmcnt(0)\n"
    "ds_add_u32 v254, v253 offset:56\n"
    "s_cmp_lg_u32 s52, 0\n"
    "s_cbranch_scc1 .Lk_n84\n"
    "s_waitcnt lgkmcnt(0)\n"
    ".Lk_w86:\n"
    "ds_read_b32 v252, v254 offset:56\n"
    "s_waitcnt lgkmcnt(0)\n"
    "v_readfirstlane_b32 s71, v252\n"
    "s_cmp_ge_u32 s71, 4\n"
    "s_cbranch_scc1 .Lk_d87\n"
    "s_sleep 1\n"
    "s_sub_u32 s54, s54, 1\n"
    "s_cbranch_scc0 .Lk_w86\n"
    "s_mov_b32 s54, 0\n"
    ".Lk_d87:\n"
    "buffer_store_dwordx2 v[242:243], v240, s[8:11], 0 offen offset:2048 sc1\n"
    "s_sleep 13\n"
    ".Lk_pg88:\n"
    "buffer_load_dwordx2 v[244:245], v241, s[8:11], 0 offen offset:2048 sc1\n"
    "s_waitcnt vmcnt(0)\n"
    "v_cmp_eq_u64 vcc, v[244:245], v[242:243]\n"
    "s_cmp_eq_u64 vcc, exec\n"
    "s_cbranch_scc1 .Lk_pgd89\n"
    "s_sub_u32 s54, s54, 1\n"
    "s_cbranch_scc0 .Lk_pg88\n"
    "s_mov_b32 s54, 0\n"
    ".Lk_pgd89:\n"
    "ds_add_u32 v254, v253 offset:60\n"
    "s_branch .Lk_j85\n"
    ".Lk_n84:\n"
    "s_sleep 18\n"
    "s_waitcnt lgkmcnt(0)\n"
    ".Lk_w90:\n"
    "ds_read_b32 v252, v254 offset:60\n"
    "s_waitcnt lgkmcnt(0)\n"
    "v_readfirstlane_b32 s71, v252\n"
    "s_cmp_ge_u32 s71, 1\n"
    "s_cbranch_scc1 .Lk_d91\n"
    "s_sleep 2\n"
    "s_sub_u32 s54, s54, 1\n"
    "s_cbranch_scc0 .Lk_w90\n"
    "s_mov_b32 s54, 0\n"
    ".Lk_d91:\n"
    ".Lk_j85:\n"
    "s_setprio 0\n"
    "buffer_load_dwordx4 v[152:155], v255, s[8:11], s76 offen sc1\n"
    "buffer_load_dwordx4 v[156:159], v255, s[8:11], s77 offen sc1\n"
    "buffer_load_dwordx4 v[160:163], v255, s[8:11], s78 offen sc1\n"
    "buffer_load_dwordx4 v[164:167], v255, s[8:11], s79 offen sc1\n"
    "buffer_load_dwordx4 v[168:171], v255, s[8:11], s76 offen offset:1024 sc1\n"
    "buffer_load_dwordx4 v[172:175], v255, s[8:11], s77 offen offset:1024 sc1\n"
    "buffer_load_dwordx4 v[176:179], v255, s[8:11], s78 offen offset:1024 sc1\n"
    "buffer_load_dwordx4 v[180:183], v255, s[8:11], s79 offen offset:1024 sc1\n"
    "buffer_load_dwordx4 v[184:187], v255, s[8:11], s76 offen offset:2048 sc1\n"
    "buffer_load_dwordx4 v[188:191], v255, s[8:11], s77 offen offset:2048 sc1\n"
    "buffer_load_dwordx4 v[192:195], v255, s[8:11], s78 offen offset:2048 sc1\n"
    "buffer_load_dwordx4 v[196:199], v255, s[8:11], s79 offen offset:2048 sc1\n"
    "s_waitcnt vmcnt(12)\n"
    "buffer_load_dwordx4 v[200:203], v255, s[8:11], s76 offen offset:3072 sc1\n"
    "buffer_load_dwordx4 v[204:207], v255, s[8:11], s77 offen offset:3072 sc1\n"
    "buffer_load_dwordx4 v[208:211], v255, s[8:11], s78 offen offset:3072 sc1\n"
    "buffer_load_dwordx4 v[212:215], v255, s[8:11], s79 offen offset:3072 sc1\n"
    "s_add_u32 s76, s76, 4096\n"
    "s_add_u32 s77, s77, 4096\n"
    "s_add_u32 s78, s78, 4096\n"
    "s_add_u32 s79, s79, 4096\n"
    "s_setprio 2\n"
    "v_lshrrev_b32_e64 v250, 1, v255\n"
    "s_mul_i32 s56, s52, 0x5000\n"
    "s_add_u32 s56, s56, 0xe00\n"
    "v_add_u32 v244, s56, v250\n"
    "s_waitcnt lgkmcnt(0)\n"
    ".Lk_w92:\n"
    "ds_read_b32 v252, v254 offset:116\n"
    "s_waitcnt lgkmcnt(0)\n"
    "v_readfirstlane_b32 s71, v252\n"
    "s_cmp_ge_u32 s71, 4\n"
    "s_cbranch_scc1 .Lk_d93\n"
    "s_sleep 1\n"
    "s_sub_u32 s54, s54, 1\n"
    "s_cbranch_scc0 .Lk_w92\n"
    "s_mov_b32 s54, 0\n"
    ".Lk_d93:\n"
    "s_waitcnt vmcnt(15)\n"
    "v_mfma_f32_16x16x32_bf16 v[72:75], v[224:227], v[152:155], 0\n"
    "ds_write_b64 v244, v[216:217]\n"
    "s_waitcnt vmcnt(14)\n"
    "v_mfma_f32_16x16x32_bf16 v[92:95], v[224:227], v[156:159], 0\n"
    "ds_write_b64 v244, v[218:219] offset:4096\n"
    "s_waitcnt vmcnt(13)\n"
    "v_mfma_f32_16x16x32_bf16 v[112:115], v[224:227], v[160:163], 0\n"
    "ds_write_b64 v244, v[220:221] offset:8192\n"
    "s_waitcnt vmcnt(12)\n"
    "v_mfma_f32_16x16x32_bf16 v[132:135], v[224:227], v[164:167], 0\n"
    "ds_write_b64 v244, v[222:223] offset:12288\n"
    "v_mfma_f32_16x16x32_bf16 v[76:79], v[228:231], v[152:155], 0\n"
    "ds_write_b64 v244, v[248:249] offset:16384\n"
    "v_mfma_f32_16x16x32_bf16 v[96:99], v[228:231], v[156:159], 0\n"
    "s_add_u32 s56, s52, 0\n"
    "v_mfma_f32_16x16x32_bf16 v[116:119], v[228:231], v[160:163], 0\n"
    "s_and_b32 s56, s56, 3\n"
    "v_mfma_f32_16x16x32_bf16 v[136:139], v[228:231], v[164:167], 0\n"
    "s_mul_i32 s57, s56, 0x5000\n"
    "v_mfma_f32_16x16x32_bf16 v[80:83], v[232:235], v[152:155], 0\n"
    "s_mul_i32 s70, s52, 0x200\n"
    "v_mfma_f32_16x16x32_bf16 v[100:103], v[232:235], v[156:159], 0\n"
    "s_add_u32 s57, s57, s70\n"
    "v_mfma_f32_16x16x32_bf16 v[120:123], v[232:235], v[160:163], 0\n"
    "v_add_u32 v240, s57, v250\n"
    "v_mfma_f32_16x16x32_bf16 v[140:143], v[232:235], v[164:167], 0\n"
    "s_add_u32 s56, s52, 1\n"
    "v_mfma_f32_16x16x32_bf16 v[84:87], v[236:239], v[152:155], 0\n"
    "s_and_b32 s56, s56, 3\n"
    "v_mfma_f32_16x16x32_bf16 v[104:107], v[236:239], v[156:159], 0\n"
    "s_mul_i32 s57, s56, 0x5000\n"
    "v_mfma_f32_16x16x32_bf16 v[124:127], v[236:239], v[160:163], 0\n"
    "s_mul_i32 s70, s52, 0x200\n"
    "v_mfma_f32_16x16x32_bf16 v[144:147], v[236:239], v[164:167], 0\n"
    "s_add_u32 s57, s57, s70\n"
    "v_mfma_f32_16x16x32_bf16 v[88:91], v[0:3], v[152:155], 0\n"
    "v_add_u32 v241, s57, v250\n"
    "buffer_load_dwordx4 v[152:155], v255, s[8:11], s76 offen sc1\n"
    "v_mfma_f32_16x16x32_bf16 v[108:111], v[0:3], v[156:159], 0\n"
    "s_add_u32 s56, s52, 2\n"
    "buffer_load_dwordx4 v[156:159], v255, s[8:11], s77 offen sc1\n"
    "v_mfma_f32_16x16x32_bf16 v[128:131], v[0:3], v[160:163], 0\n"
    "s_and_b32 s56, s56, 3\n"
    "buffer_load_dwordx4 v[160:163], v255, s[8:11], s78 offen sc1\n"
    "v_mfma_f32_16x16x32_bf16 v[148:151], v[0:3], v[164:167], 0\n"
    "s_mul_i32 s57, s56, 0x5000\n"
    "buffer_load_dwordx4 v[164:167], v255, s[8:11], s79 offen sc1\n"
    "s_waitcnt vmcnt(15)\n"
    "v_mfma_f32_16x16x32_bf16 v[72:75], v[4:7], v[168:171], v[72:75]\n"
    "s_mul_i32 s70, s52, 0x200\n"
    "s_waitcnt vmcnt(14)\n"
    "v_mfma_f32_16x16x32_bf16 v[92:95], v[4:7], v[172:175], v[92:95]\n"
    "s_add_u32 s57, s57, s70\n"
    "s_waitcnt vmcnt(13)\n"
    "v_mfma_f32_16x16x32_bf16 v[112:115], v[4:7], v[176:179], v[112:115]\n"
    "v_add_u32 v242, s57, v250\n"
    "s_waitcnt vmcnt(12)\n"
    "v_mfma_f32_16x16x32_bf16 v[132:135], v[4:7], v[180:183], v[132:135]\n"
    "s_add_u32 s56, s52, 3\n"
    "v_mfma_f32_16x16x32_bf16 v[76:79], v[8:11], v[168:171], v[76:79]\n"
    "s_and_b32 s56, s56, 3\n"
    "v_mfma_f32_16x16x32_bf16 v[96:99], v[8:11], v[172:175], v[96:99]\n"
    "s_mul_i32 s57, s56, 0x5000\n"
    "v_mfma_f32_16x16x32_bf16 v[116:119], v[8:11], v[176:179], v[116:119]\n"
    "s_mul_i32 s70, s52, 0x200\n"
    "v_mfma_f32_16x16x32_bf16 v[136:139], v[8:11], v[180:183], v[136:139]\n"
    "s_add_u32 s57, s57, s70\n"
    "v_mfma_f32_16x16x32_bf16 v[80:83], v[12:15], v[168:171], v[80:83]\n"
    "v_add_u32 v243, s57, v250\n"
    "v_mfma_f32_16x16x32_bf16 v[100:103], v[12:15], v[172:175], v[100:103]\n"
    "s_lshl_b32 s56, 1, s52\n"
    "v_mfma_f32_16x16x32_bf16 v[120:123], v[12:15], v[176:179], v[120:123]\n"
    "v_mov_b32 v245, s56\n"
    "v_mfma_f32_16x16x32_bf16 v[140:143], v[12:15], v[180:183], v[140:143]\n"
    "v_mfma_f32_16x16x32_bf16 v[84:87], v[16:19], v[168:171], v[84:87]\n"
    "v_mfma_f32_16x16x32_bf16 v[104:107], v[16:19], v[172:175], v[104:107]\n"
    "v_mfma_f32_16x16x32_bf16 v[124:127], v[16:19], v[176:179], v[124:127]\n"
    "v_mfma_f32_16x16x32_bf16 v[144:147], v[16:19], v[180:183], v[144:147]\n"
    "v_mfma_f32_16x16x32_bf16 v[88:91], v[20:23], v[168:171], v[88:91]\n"
    "buffer_load_dwordx4 v[168:171], v255, s[8:11], s76 offen offset:1024 sc1\n"
    "v_mfma_f32_16x16x32_bf16 v[108:111], v[20:23], v[172:175], v[108:111]\n"
    "buffer_load_dwordx4 v[172:175], v255, s[8:11], s77 offen offset:1024 sc1\n"
    "v_mfma_f32_16x16x32_bf16 v[128:131], v[20:23], v[176:179], v[128:131]\n"
    "buffer_load_dwordx4 v[176:179], v255, s[8:11], s78 offen offset:1024 sc1\n"
    "v_mfma_f32_16x16x32_bf16 v[148:151], v[20:23], v[180:183], v[148:151]\n"
    "buffer_load_dwordx4 v[180:183], v255, s[8:11], s79 offen offset:1024 sc1\n"
    "s_waitcnt vmcnt(15)\n"
    "v_mfma_f32_16x16x32_bf16 v[72:75], v[24:27], v[184:187], v[72:75]\n"
    "s_waitcnt vmcnt(14)\n"
    "v_mfma_f32_16x16x32_bf16 v[92:95], v[24:27], v[188:191], v[92:95]\n"
    "s_waitcnt vmcnt(13)\n"
    "v_mfma_f32_16x16x32_bf16 v[112:115], v[24:27], v[192:195], v[112:115]\n"
    "s_waitcnt vmcnt(12)\n"
    "v_mfma_f32_16x16x32_bf16 v[132:135], v[24:27], v[196:199], v[132:135]\n"
    "v_mfma_f32_16x16x32_bf16 v[76:79], v[28:31], v[184:187], v[76:79]\n"
    "v_mfma_f32_16x16x32_bf16 v[96:99], v[28:31], v[188:191], v[96:99]\n"
    "v_mfma_f32_16x16x32_bf16 v[116:119], v[28:31], v[192:195], v[116:119]\n"
    "v_mfma_f32_16x16x32_bf16 v[136:139], v[28:31], v[196:199], v[136:139]\n"
    "v_mfma_f32_16x16x32_bf16 v[80:83], v[32:35], v[184:187], v[80:83]\n"
    "v_mfma_f32_16x16x32_bf16 v[100:103], v[32:35], v[188:191], v[100:103]\n"
    "v_mfma_f32_16x16x32_bf16 v[120:123], v[32:35], v[192:195], v[120:123]\n"
    "v_mfma_f32_16x16x32_bf16 v[140:143], v[32:35], v[196:199], v[140:143]\n"
    "v_mfma_f32_16x16x32_bf16 v[84:87], v[36:39], v[184:187], v[84:87]\n"
    "v_mfma_f32_16x16x32_bf16 v[104:107], v[36:39], v[188:191], v[104:107]\n"
    "v_mfma_f32_16x16x32_bf16 v[124:127], v[36:39], v[192:195], v[124:127]\n"
    "v_mfma_f32_16x16x32_bf16 v[144:147], v[36:39], v[196:199], v[144:147]\n"
    "v_mfma_f32_16x16x32_bf16 v[88:91], v[40:43], v[184:187], v[88:91]\n"
    "buffer_load_dwordx4 v[184:187], v255, s[8:11], s76 offen offset:2048 sc1\n"
    "v_mfma_f32_16x16x32_bf16 v[108:111], v[40:43], v[188:191], v[108:111]\n"
    "buffer_load_dwordx4 v[188:191], v255, s[8:11], s77 offen offset:2048 sc1\n"
    "v_mfma_f32_16x16x32_bf16 v[128:131], v[40:43], v[192:195], v[128:131]\n"
    "buffer_load_dwordx4 v[192:195], v255, s[8:11], s78 offen offset:2048 sc1\n"
    "v_mfma_f32_16x16x32_bf16 v[148:151], v[40:43], v[196:199], v[148:151]\n"
    "buffer_load_dwordx4 v[196:199], v255, s[8:11], s79 offen offset:2048 sc1\n"
    "s_waitcnt vmcnt(15)\n"
    "v_mfma_f32_16x16x32_bf16 v[72:75], v[44:47], v[200:203], v[72:75]\n"
    "s_waitcnt vmcnt(14)\n"
    "v_mfma_f32_16x16x32_bf16 v[92:95], v[44:47], v[204:207], v[92:95]\n"
    "s_waitcnt vmcnt(13)\n"
    "v_mfma_f32_16x16x32_bf16 v[112:115], v[44:47], v[208:211], v[112:115]\n"
    "s_waitcnt vmcnt(12)\n"
    "v_mfma_f32_16x16x32_bf16 v[132:135], v[44:47], v[212:215], v[132:135]\n"
    "v_mfma_f32_16x16x32_bf16 v[76:79], v[48:51], v[200:203], v[76:79]\n"
    "v_mfma_f32_16x16x32_bf16 v[96:99], v[48:51], v[204:207], v[96:99]\n"
    "v_mfma_f32_16x16x32_bf16 v[116:119], v[48:51], v[208:211], v[116:119]\n"
    "v_mfma_f32_16x16x32_bf16 v[136:139], v[48:51], v[212:215], v[136:139]\n"
    "v_mfma_f32_16x16x32_bf16 v[80:83], v[52:55], v[200:203], v[80:83]\n"
    "v_mfma_f32_16x16x32_bf16 v[100:103], v[52:55], v[204:207], v[100:103]\n"
    "v_mfma_f32_16x16x32_bf16 v[120:123], v[52:55], v[208:211], v[120:123]\n"
    "v_mfma_f32_16x16x32_bf16 v[140:143], v[52:55], v[212:215], v[140:143]\n"
    "v_mfma_f32_16x16x32_bf16 v[84:87], v[56:59], v[200:203], v[84:87]\n"
    "v_mfma_f32_16x16x32_bf16 v[104:107], v[56:59], v[204:207], v[104:107]\n"
    "v_mfma_f32_16x16x32_bf16 v[124:127], v[56:59], v[208:211], v[124:127]\n"
    "v_mfma_f32_16x16x32_bf16 v[144:147], v[56:59], v[212:215], v[144:147]\n"
    "v_mfma_f32_16x16x32_bf16 v[88:91], v[60:63], v[200:203], v[88:91]\n"
    "ds_read_b128 v[0:3], v247\n"
    "buffer_load_dwordx4 v[200:203], v255, s[8:11], s76 offen offset:3072 sc1\n"
    "v_mfma_f32_16x16x32_bf16 v[108:111], v[60:63], v[204:207], v[108:111]\n"
    "buffer_load_dwordx4 v[204:207], v255, s[8:11], s77 offen offset:3072 sc1\n"
    "v_mfma_f32_16x16x32_bf16 v[128:131], v[60:63], v[208:211], v[128:131]\n"
    "buffer_load_dwordx4 v[208:211], v255, s[8:11], s78 offen offset:3072 sc1\n"
    "v_mfma_f32_16x16x32_bf16 v[148:151], v[60:63], v[212:215], v[148:151]\n"
    "buffer_load_dwordx4 v[212:215], v255, s[8:11], s79 offen offset:3072 sc1\n"
    "s_waitcnt vmcnt(15)\n"
    "v_mfma_f32_16x16x32_bf16 v[72:75], v[64:67], v[152:155], v[72:75]\n"
    "ds_read_b128 v[4:7], v247 offset:4096\n"
    "s_waitcnt vmcnt(14)\n"
    "v_mfma_f32_16x16x32_bf16 v[92:95], v[64:67], v[156:159], v[92:95]\n"
    "s_waitcnt vmcnt(13)\n"
    "v_mfma_f32_16x16x32_bf16 v[112:115], v[64:67], v[160:163], v[112:115]\n"
    "s_waitcnt vmcnt(12)\n"
    "v_mfma_f32_16x16x32_bf16 v[132:135], v[64:67], v[164:167], v[132:135]\n"
    "v_mfma_f32_16x16x32_bf16 v[76:79], v[68:71], v[152:155], v[76:79]\n"
    "ds_read_b128 v[8:11], v247 offset:8192\n"
    "v_mfma_f32_16x16x32_bf16 v[96:99], v[68:71], v[156:159], v[96:99]\n"
    "v_mfma_f32_16x16x32_bf16 v[116:119], v[68:71], v[160:163], v[116:119]\n"
    "v_mfma_f32_16x16x32_bf16 v[136:139], v[68:71], v[164:167], v[136:139]\n"
    "s_waitcnt lgkmcnt(2)\n"
    "v_mfma_f32_16x16x32_bf16 v[80:83], v[0:3], v[152:155], v[80:83]\n"
    "ds_read_b128 v[12:15], v247 offset:12288\n"
    "v_mfma_f32_16x16x32_bf16 v[100:103], v[0:3], v[156:159], v[100:103]\n"
    "v_mfma_f32_16x16x32_bf16 v[120:123], v[0:3], v[160:163], v[120:123]\n"
    "v_mfma_f32_16x16x32_bf16 v[140:143], v[0:3], v[164:167], v[140:143]\n"
    "s_waitcnt lgkmcnt(2)\n"
    "v_mfma_f32_16x16x32_bf16 v[84:87], v[4:7], v[152:155], v[84:87]\n"
    "ds_read_b128 v[0:3], v247 offset:16384\n"
    "v_mfma_f32_16x16x32_bf16 v[104:107], v[4:7], v[156:159], v[104:107]\n"
    "v_mfma_f32_16x16x32_bf16 v[124:127], v[4:7], v[160:163], v[124:127]\n"
    "v_mfma_f32_16x16x32_bf16 v[144:147], v[4:7], v[164:167], v[144:147]\n"
    "s_waitcnt lgkmcnt(2)\n"
    "v_mfma_f32_16x16x32_bf16 v[88:91], v[8:11], v[152:155], v[88:91]\n"
    "ds_read_b128 v[4:7], v247 offset:20480\n"
    "v_mfma_f32_16x16x32_bf16 v[108:111], v[8:11], v[156:159], v[108:111]\n"
    "v_mfma_f32_16x16x32_bf16 v[128:131], v[8:11], v[160:163], v[128:131]\n"
    "v_mfma_f32_16x16x32_bf16 v[148:151], v[8:11], v[164:167], v[148:151]\n"
    "s_waitcnt vmcnt(11) lgkmcnt(2)\n"
    "v_mfma_f32_16x16x32_bf16 v[72:75], v[12:15], v[168:171], v[72:75]\n"
    "ds_read_b128 v[8:11], v247 offset:24576\n"
    "s_waitcnt vmcnt(10)\n"
    "v_mfma_f32_16x16x32_bf16 v[92:95], v[12:15], v[172:175], v[92:95]\n"
    "s_waitcnt vmcnt(9)\n"
    "v_mfma_f32_16x16x32_bf16 v[112:115], v[12:15], v[176:179], v[112:115]\n"
    "s_waitcnt vmcnt(8)\n"
    "v_mfma_f32_16x16x32_bf16 v[132:135], v[12:15], v[180:183], v[132:135]\n"
    "s_waitcnt lgkmcnt(2)\n"
    "v_mfma_f32_16x16x32_bf16 v[76:79], v[0:3], v[168:171], v[76:79]\n"
    "ds_read_b128 v[12:15], v247 offset:28672\n"
    "v_mfma_f32_16x16x32_bf16 v[96:99], v[0:3], v[172:175], v[96:99]\n"
    "v_mfma_f32_16x16x32_bf16 v[116:119], v[0:3], v[176:179], v[116:119]\n"
    "v_mfma_f32_16x16x32_bf16 v[136:139], v[0:3], v[180:183], v[136:139]\n"
    "s_waitcnt lgkmcnt(2)\n"
    "v_mfma_f32_16x16x32_bf16 v[80:83], v[4:7], v[168:171], v[80:83]\n"
    "ds_read_b128 v[0:3], v247 offset:32768\n"
    "v_mfma_f32_16x16x32_bf16 v[100:103], v[4:7], v[172:175], v[100:103]\n"
    "v_mfma_f32_16x16x32_bf16 v[120:123], v[4:7], v[176:179], v[120:123]\n"
    "v_mfma_f32_16x16x32_bf16 v[140:143], v[4:7], v[180:183], v[140:143]\n"
    "s_waitcnt lgkmcnt(2)\n"
    "v_mfma_f32_16x16x32_bf16 v[84:87], v[8:11], v[168:171], v[84:87]\n"
    "ds_read_b128 v[4:7], v247 offset:36864\n"
    "v_mfma_f32_16x16x32_bf16 v[104:107], v[8:11], v[172:175], v[104:107]\n"
    "v_mfma_f32_16x16x32_bf16 v[124:127], v[8:11], v[176:179], v[124:127]\n"
    "v_mfma_f32_16x16x32_bf16 v[144:147], v[8:11], v[180:183], v[144:147]\n"
    "s_waitcnt lgkmcnt(2)\n"
    "v_mfma_f32_16x16x32_bf16 v[88:91], v[12:15], v[168:171], v[88:91]\n"
    "ds_read_b128 v[8:11], v247 offset:40960\n"
    "v_mfma_f32_16x16x32_bf16 v[108:111], v[12:15], v[172:175], v[108:111]\n"
    "v_mfma_f32_16x16x32_bf16 v[128:131], v[12:15], v[176:179], v[128:131]\n"
    "v_mfma_f32_16x16x32_bf16 v[148:151], v[12:15], v[180:183], v[148:151]\n"
    "s_waitcnt vmcnt(7) lgkmcnt(2)\n"
    "v_mfma_f32_16x16x32_bf16 v[72:75], v[0:3], v[184:187], v[72:75]\n"
    "ds_read_b128 v[12:15], v247 offset:45056\n"
    "s_waitcnt vmcnt(6)\n"
    "v_mfma_f32_16x16x32_bf16 v[92:95], v[0:3], v[188:191], v[92:95]\n"
    "s_waitcnt vmcnt(5)\n"
    "v_mfma_f32_16x16x32_bf16 v[112:115], v[0:3], v[192:195], v[112:115]\n"
    "s_waitcnt vmcnt(4)\n"
    "v_mfma_f32_16x16x32_bf16 v[132:135], v[0:3], v[196:199], v[132:135]\n"
    "s_waitcnt lgkmcnt(2)\n"
    "v_mfma_f32_16x16x32_bf16 v[76:79], v[4:7], v[184:187], v[76:79]\n"
    "ds_read_b128 v[0:3], v247 offset:49152\n"
    "v_mfma_f32_16x16x32_bf16 v[96:99], v[4:7], v[188:191], v[96:99]\n"
    "v_mfma_f32_16x16x32_bf16 v[116:119], v[4:7], v[192:195], v[116:119]\n"
    "v_mfma_f32_16x16x32_bf16 v[136:139], v[4:7], v[196:199], v[136:139]\n"
    "s_waitcnt lgkmcnt(2)\n"
    "v_mfma_f32_16x16x32_bf16 v[80:83], v[8:11], v[184:187], v[80:83]\n"
    "ds_read_b128 v[4:7], v247 offset:53248\n"
    "v_mfma_f32_16x16x32_bf16 v[100:103], v[8:11], v[188:191], v[100:103]\n"
    "v_mfma_f32_16x16x32_bf16 v[120:123], v[8:11], v[192:195], v[120:123]\n"
    "v_mfma_f32_16x16x32_bf16 v[140:143], v[8:11], v[196:199], v[140:143]\n"
    "s_waitcnt lgkmcnt(2)\n"
    "v_mfma_f32_16x16x32_bf16 v[84:87], v[12:15], v[184:187], v[84:87]\n"
    "ds_read_b128 v[8:11], v247 offset:57344\n"
    "v_mfma_f32_16x16x32_bf16 v[104:107], v[12:15], v[188:191], v[104:107]\n"
    "v_mfma_f32_16x16x32_bf16 v[124:127], v[12:15], v[192:195], v[124:127]\n"
    "v_mfma_f32_16x16x32_bf16 v[144:147], v[12:15], v[196:199], v[144:147]\n"
    "s_waitcnt lgkmcnt(2)\n"
    "v_mfma_f32_16x16x32_bf16 v[88:91], v[0:3], v[184:187], v[88:91]\n"
    "ds_read_b128 v[12:15], v247 offset:61440\n"
    "v_mfma_f32_16x16x32_bf16 v[108:111], v[0:3], v[188:191], v[108:111]\n"
    "v_mfma_f32_16x16x32_bf16 v[128:131], v[0:3], v[192:195], v[128:131]\n"
    "v_mfma_f32_16x16x32_bf16 v[148:151], v[0:3], v[196:199], v[148:151]\n"
    "s_waitcnt vmcnt(3) lgkmcnt(2)\n"
    "v_mfma_f32_16x16x32_bf16 v[72:75], v[4:7], v[200:203], v[72:75]\n"
    "ds_read_b128 v[0:3], v246\n"
    "s_waitcnt vmcnt(2)\n"
    "v_mfma_f32_16x16x32_bf16 v[92:95], v[4:7], v[204:207], v[92:95]\n"
    "s_waitcnt vmcnt(1)\n"
    "v_mfma_f32_16x16x32_bf16 v[112:115], v[4:7], v[208:211], v[112:115]\n"
    "s_waitcnt vmcnt(0)\n"
    "v_mfma_f32_16x16x32_bf16 v[132:135], v[4:7], v[212:215], v[132:135]\n"
    "s_waitcnt lgkmcnt(2)\n"
    "v_mfma_f32_16x16x32_bf16 v[76:79], v[8:11], v[200:203], v[76:79]\n"
    "ds_read_b128 v[4:7], v246 offset:4096\n"
    "v_mfma_f32_16x16x32_bf16 v[96:99], v[8:11], v[204:207], v[96:99]\n"
    "v_mfma_f32_16x16x32_bf16 v[116:119], v[8:11], v[208:211], v[116:119]\n"
    "v_mfma_f32_16x16x32_bf16 v[136:139], v[8:11], v[212:215], v[136:139]\n"
    "s_waitcnt lgkmcnt(2)\n"
    "v_mfma_f32_16x16x32_bf16 v[80:83], v[12:15], v[200:203], v[80:83]\n"
    "v_mfma_f32_16x16x32_bf16 v[100:103], v[12:15], v[204:207], v[100:103]\n"
    "v_mfma_f32_16x16x32_bf16 v[120:123], v[12:15], v[208:211], v[120:123]\n"
    "v_mfma_f32_16x16x32_bf16 v[140:143], v[12:15], v[212:215], v[140:143]\n"
    "s_waitcnt lgkmcnt(1)\n"
    "v_mfma_f32_16x16x32_bf16 v[84:87], v[0:3], v[200:203], v[84:87]\n"
    "v_mfma_f32_16x16x32_bf16 v[104:107], v[0:3], v[204:207], v[104:107]\n"
    "v_mfma_f32_16x16x32_bf16 v[124:127], v[0:3], v[208:211], v[124:127]\n"
    "v_mfma_f32_16x16x32_bf16 v[144:147], v[0:3], v[212:215], v[144:147]\n"
    "s_waitcnt lgkmcnt(0)\n"
    "s_nop 0\n"
    "v_mfma_f32_16x16x32_bf16 v[88:91], v[4:7], v[200:203], v[88:91]\n"
    "v_mfma_f32_16x16x32_bf16 v[108:111], v[4:7], v[204:207], v[108:111]\n"
    "v_mfma_f32_16x16x32_bf16 v[128:131], v[4:7], v[208:211], v[128:131]\n"
    "v_mfma_f32_16x16x32_bf16 v[148:151], v[4:7], v[212:215], v[148:151]\n"
    "v_cvt_pk_bf16_f32 v72, v72, v73\n"
    "v_cvt_pk_bf16_f32 v73, v74, v75\n"
    "ds_write_b64 v240, v[72:73]\n"
    "v_cvt_pk_bf16_f32 v76, v76, v77\n"
    "v_cvt_pk_bf16_f32 v77, v78, v79\n"
    "ds_write_b64 v240, v[76:77] offset:4096\n"
    "v_cvt_pk_bf16_f32 v80, v80, v81\n"
    "v_cvt_pk_bf16_f32 v81, v82, v83\n"
    "ds_write_b64 v240, v[80:81] offset:8192\n"
    "v_cvt_pk_bf16_f32 v84, v84, v85\n"
    "v_cvt_pk_bf16_f32 v85, v86, v87\n"
    "ds_write_b64 v240, v[84:85] offset:12288\n"
    "v_cvt_pk_bf16_f32 v88, v88, v89\n"
    "v_cvt_pk_bf16_f32 v89, v90, v91\n"
    "ds_write_b64 v240, v[88:89] offset:16384\n"
    "v_cvt_pk_bf16_f32 v92, v92, v93\n"
    "v_cvt_pk_bf16_f32 v93, v94, v95\n"
    "ds_write_b64 v241, v[92:93]\n"
    "v_cvt_pk_bf16_f32 v96, v96, v97\n"
    "v_cvt_pk_bf16_f32 v97, v98, v99\n"
    "ds_write_b64 v241, v[96:97] offset:4096\n"
    "v_cvt_pk_bf16_f32 v100, v100, v101\n"
    "v_cvt_pk_bf16_f32 v101, v102, v103\n"
    "ds_write_b64 v241, v[100:101] offset:8192\n"
    "v_cvt_pk_bf16_f32 v104, v104, v105\n"
    "v_cvt_pk_bf16_f32 v105, v106, v107\n"
    "ds_write_b64 v241, v[104:105] offset:12288\n"
    "v_cvt_pk_bf16_f32 v108, v108, v109\n"
    "v_cvt_pk_bf16_f32 v109, v110, v111\n"
    "ds_write_b64 v241, v[108:109] offset:16384\n"
    "v_cvt_pk_bf16_f32 v112, v112, v113\n"
    "v_cvt_pk_bf16_f32 v113, v114, v115\n"
    "ds_write_b64 v242, v[112:113]\n"
    "v_cvt_pk_bf16_f32 v116, v116, v117\n"
    ".Lk_pad5:\n"
    "s_cbranch_execz .Lk_tramp\n"
    "s_nop 0\n"
    "v_cvt_pk_bf16_f32 v117, v118, v119\n"
    "ds_write_b64 v242, v[116:117] offset:4096\n"
    "v_cvt_pk_bf16_f32 v120, v120, v121\n"
    "v_cvt_pk_bf16_f32 v121, v122, v123\n"
    "ds_write_b64 v242, v[120:121] offset:8192\n"
    "v_cvt_pk_bf16_f32 v124, v124, v125\n"
    "v_cvt_pk_bf16_f32 v125, v126, v127\n"
    "ds_write_b64 v242, v[124:125] offset:12288\n"
    "v_cvt_pk_bf16_f32 v128, v128, v129\n"
    "v_cvt_pk_bf16_f32 v129, v130, v131\n"
    "ds_write_b64 v242, v[128:129] offset:16384\n"
    "v_cvt_pk_bf16_f32 v132, v132, v133\n"
    "v_cvt_pk_bf16_f32 v133, v134, v135\n"
    "ds_write_b64 v243, v[132:133]\n"
    "v_cvt_pk_bf16_f32 v136, v136, v137\n"
    "v_cvt_pk_bf16_f32 v137, v138, v139\n"
    "ds_write_b64 v243, v[136:137] offset:4096\n"
    "v_cvt_pk_bf16_f32 v140, v140, v141\n"
    "v_cvt_pk_bf16_f32 v141, v142, v143\n"
    "ds_write_b64 v243, v[140:141] offset:8192\n"
    "v_cvt_pk_bf16_f32 v144, v144, v145\n"
    "v_cvt_pk_bf16_f32 v145, v146, v147\n"
    "ds_write_b64 v243, v[144:145] offset:12288\n"
    "v_cvt_pk_bf16_f32 v148, v148, v149\n"
    "v_cvt_pk_bf16_f32 v149, v150, v151\n"
    "ds_write_b64 v243, v[148:149] offset:16384\n"
    "ds_add_u32 v254, v245 offset:76\n"
    "ds_add_u32 v254, v245 offset:80\n"
    "ds_add_u32 v254, v245 offset:84\n"
    "ds_add_u32 v254, v245 offset:88\n"
    "s_endpgm\n"
    ".Lk_tramp:\n"
    "s_setpc_b64 s[100:101]\n"
    ".Lk_main1:\n"
    "s_and_b32 s52, s46, 1\n"
    "s_lshr_b32 s53, s46, 1\n"
    "s_mul_i32 s72, s46, 0x5000\n"
    "v_bfe_u32 v248, v255, 4, 4\n"
    "v_lshrrev_b32_e64 v249, 8, v255\n"
    "s_mov_b32 s78, 0x22222222\n"
    "s_mov_b32 s79, 0x22222222\n"
    "s_mov_b32 s80, 0x44444444\n"
    "s_mov_b32 s81, 0x44444444\n"
    "s_mov_b32 s82, 0x88888888\n"
    "s_mov_b32 s83, 0x88888888\n"
    "s_mov_b32 s84, 0xf0000f\n"
    "s_mov_b32 s85, 0xf0000f00\n"
    "s_waitcnt lgkmcnt(0)\n"
    "s_mov_b32 s8, s16\n"
    "s_and_b32 s9, s17, 0xffff\n"
    "s_mov_b32 s10, 2621440\n"
    "s_mov_b32 s11, 0x20000\n"
    "s_mov_b32 s4, s36\n"
    "s_and_b32 s5, s37, 0xffff\n"
    "s_mov_b32 s6, 5701632\n"
    "s_mov_b32 s7, 0x20000\n"
    "s_barrier\n"
    "v_lshrrev_b32 v4, 3, v248\n"
    "v_lshlrev_b32 v4, 1, v4\n"
    "v_xor_b32 v4, v4, v249\n"
    "v_lshlrev_b32 v4, 4, v4\n"
    "v_lshl_add_u32 v3, v248, 6, v4\n"
    "v_lshrrev_b32_e64 v4, 2, v248\n"
    "v_lshl_add_u32 v4, v249, 3, v4\n"
    "v_mul_u32_u24 v4, 160, v4\n"
    "v_and_b32 v5, 1, v248\n"
    "v_mul_u32_u24 v5, 40, v5\n"
    "v_add_u32_e64 v4, v4, v5\n"
    "v_bfe_u32 v5, v248, 1, 1\n"
    "v_lshl_add_u32 v4, v5, 4, v4\n"
    "v_and_b32_e64 v7, 1, v249\n"
    "v_mul_u32_u24 v7, 640, v7\n"
    "v_add_u32 v6, v4, v7\n"
    "v_sub_u32 v1, v4, v7\n"
    "v_add_u32 v1, 640, v1\n"
    "s_mul_i32 s61, s47, 0x14000\n"
    "s_mul_i32 s70, s46, 0x5000\n"
    "s_add_u32 s61, s61, s70\n"
    "s_add_u32 s56, s46, 0\n"
    "s_and_b32 s56, s56, 3\n"
    "s_mul_i32 s57, s56, 0x1400\n"
    "s_mul_i32 s58, s56, 0x400\n"
    "s_add_u32 s58, s58, 0x1f500\n"
    "s_mul_i32 s88, s56, 0x40\n"
    "s_add_u32 s88, s88, s61\n"
    "v_add_u32 v186, s57, v6\n"
    "v_add_u32 v212, s57, v1\n"
    "v_add_u32 v190, s58, v3\n"
    "v_lshl_add_u32 v240, v5, 6, v186\n"
    "v_lshl_add_u32 v244, v5, 6, v212\n"
    "s_add_u32 s56, s46, 1\n"
    "s_and_b32 s56, s56, 3\n"
    "s_mul_i32 s57, s56, 0x1400\n"
    "s_mul_i32 s58, s56, 0x400\n"
    "s_add_u32 s58, s58, 0x1f500\n"
    "s_mul_i32 s89, s56, 0x40\n"
    "s_add_u32 s89, s89, s61\n"
    "v_add_u32 v187, s57, v6\n"
    "v_add_u32 v213, s57, v1\n"
    "v_add_u32_e64 v191, s58, v3\n"
    "v_lshl_add_u32 v241, v5, 6, v187\n"
    "v_lshl_add_u32 v245, v5, 6, v213\n"
    "s_add_u32 s56, s46, 2\n"
    "s_and_b32 s56, s56, 3\n"
    "s_mul_i32 s57, s56, 0x1400\n"
    "s_mul_i32 s58, s56, 0x400\n"
    "s_add_u32 s58, s58, 0x1f500\n"
    "s_mul_i32 s90, s56, 0x40\n"
    "s_add_u32 s90, s90, s61\n"
    "v_add_u32 v188, s57, v6\n"
    "v_add_u32 v214, s57, v1\n"
    "v_add_u32_e64 v192, s58, v3\n"
    "v_lshl_add_u32 v242, v5, 6, v188\n"
    "v_lshl_add_u32 v246, v5, 6, v214\n"
    "s_add_u32 s56, s46, 3\n"
    "s_and_b32 s56, s56, 3\n"
    "s_mul_i32 s57, s56, 0x1400\n"
    "s_mul_i32 s58, s56, 0x400\n"
    "s_add_u32 s58, s58, 0x1f500\n"
    "s_mul_i32 s91, s56, 0x40\n"
    "s_add_u32 s91, s91, s61\n"
    "v_add_u32 v189, s57, v6\n"
    "v_add_u32 v215, s57, v1\n"
    "v_add_u32_e64 v193, s58, v3\n"
    "v_lshl_add_u32 v243, v5, 6, v189\n"
    "v_lshl_add_u32 v247, v5, 6, v215\n"
    "s_mul_i32 s56, s48, 160\n"
    "s_and_b32 s56, s56, 0xffffffc0\n"
    "s_add_u32 s75, s61, s56\n"
    "s_lshl_b32 s56, s50, 1\n"
    "s_mul_i32 s57, s46, 0x800\n"
    "s_add_u32 s57, s57, 0x24500\n"
    "v_add_u32 v194, s57, v255\n"
    "v_add_u32 v195, s57, v3\n"
    "v_lshrrev_b32 v6, 3, v248\n"
    "v_lshlrev_b32 v6, 1, v6\n"
    "v_lshl_add_u32 v1, v248, 6, s57\n"
    "v_add_u32 v4, s56, v249\n"
    "v_xor_b32 v5, v4, v6\n"
    "v_and_b32_e64 v5, 3, v5\n"
    "v_lshl_add_u32 v5, v5, 4, v1\n"
    "v_lshrrev_b32_e64 v4, 2, v4\n"
    "v_lshl_add_u32 v210, v4, 10, v5\n"
    "v_xor_b32_e64 v4, 1, v4\n"
    "v_lshl_add_u32 v211, v4, 10, v5\n"
    "v_lshrrev_b32 v4, 1, v249\n"
    "v_add_u32 v4, s56, v4\n"
    "v_xor_b32_e64 v4, v4, v6\n"
    "v_lshl_add_u32 v4, v4, 4, v1\n"
    "v_and_b32_e64 v5, 1, v249\n"
    "v_lshl_add_u32 v238, v5, 3, v4\n"
    "s_mul_i32 s57, s46, 0x80\n"
    "s_add_u32 s57, s57, 0x27100\n"
    "s_mul_i32 s58, s46, 0x140\n"
    "s_add_u32 s58, s58, 0x27500\n"
    "v_lshl_add_u32 v208, v248, 3, s57\n"
    "v_lshl_add_u32 v209, v248, 2, s58\n"
    "v_mov_b32 v4, 162304\n"
    "v_lshl_add_u32 v239, v249, 4, v4\n"
    "v_bfe_u32 v4, v255, 4, 5\n"
    "v_lshrrev_b32 v5, 9, v255\n"
    "v_lshlrev_b32 v6, 4, v4\n"
    "v_add_u32 v217, 102400, v255\n"
    "v_add_u32 v218, 106496, v6\n"
    "v_mul_u32_u24 v5, 384, v5\n"
    "v_cmp_gt_u32 vcc, 24, v4\n"
    "v_mov_b32 v7, 304\n"
    "s_nop 0\n"
    "v_cndmask_b32 v6, v7, v6, vcc\n"
    "v_add_u32 v5, v5, v6\n"
    "v_add_u32 v219, 107520, v5\n"
    "v_mov_b32 v7, 0x80000000\n"
    "v_mov_b32 v216, 0xdddd\n"
    "s_mov_b32 s87, 0\n"
    "s_lshl_b32 s57, s46, 2\n"
    "v_add_u32_e64 v220, s57, v254\n"
    "v_add_u32 v235, 51200, v255\n"
    "s_and_b32 s60, s49, 1\n"
    "s_mov_b32 s28, 0xb0bc30b\n"
    "s_mov_b32 s29, 0xb110b\n"
    "s_mul_i32 s60, s60, 0xc00\n"
    "s_mul_i32 s70, s49, 0x4000\n"
    "s_add_u32 s60, s60, s70\n"
    "s_mul_i32 s20, s46, 0x10000\n"
    "s_add_u32 s20, s20, 0x8000\n"
    "s_mul_i32 s57, s49, 0x1b\n"
    "s_mul_i32 s70, s50, 0x6\n"
    "s_add_u32 s57, s57, s70\n"
    "s_add_u32 s57, s57, 0x10\n"
    "s_add_u32 s58, s57, 0x0\n"
    "s_add_u32 s58, s58, s46\n"
    "s_mul_i32 s21, s58, 0x5000\n"
    "s_mul_i32 s70, s52, 0x400\n"
    "s_add_u32 s21, s21, s70\n"
    "s_add_u32 s21, s21, 0x128000\n"
    "s_mul_i32 s27, s52, 0xfffff800\n"
    "s_add_u32 s27, s27, s21\n"
    "s_add_u32 s27, s27, 0x400\n"
    "s_add_u32 s58, s57, 0x4\n"
    "s_add_u32 s58, s58, s53\n"
    "s_mul_i32 s59, s49, 0x1b\n"
    "s_add_u32 s59, s59, 0x1a\n"
    "s_min_u32 s58, s58, s59\n"
    "s_mul_i32 s22, s58, 0x5000\n"
    "s_mul_i32 s70, s52, 0x400\n"
    "s_add_u32 s22, s22, s70\n"
    "s_add_u32 s22, s22, 0x128000\n"
    "s_mul_i32 s25, s46, 0x10000\n"
    "s_add_u32 s25, s25, 0x48000\n"
    "s_add_u32 s57, s60, 0x0\n"
    "s_add_u32 s57, s57, s25\n"
    "s_mul_i32 s58, s50, 0x800\n"
    "s_add_u32 s58, s58, s57\n"
    "s_mul_i32 s23, s50, 0x200\n"
    "s_add_u32 s23, s23, s57\n"
    "s_add_u32 s23, s23, 0x1000\n"
    "s_mul_i32 s26, s46, 0x400\n"
    "s_add_u32 s26, s26, 0x4000\n"
    "s_mul_i32 s24, s48, 0x80\n"
    "s_add_u32 s24, s24, s26\n"
    "v_add_u32 v236, s58, v255\n"
    "v_lshrrev_b32_e64 v4, 1, v249\n"
    "v_lshl_add_u32 v4, v4, 8, s23\n"
    "v_lshl_add_u32 v4, v248, 4, v4\n"
    "v_lshl_add_u32 v234, v249, 31, v4\n"
    "v_subrev_u32 v232, 262144, v236\n"
    "v_subrev_u32 v233, 262144, v234\n"
    "v_lshlrev_b32 v4, 3, v248\n"
    "v_cmp_eq_u32 vcc, 0, v249\n"
    "s_nop 1\n"
    "v_cndmask_b32 v237, v7, v4, vcc\n"
    "v_mov_b64_e64 v[180:181], 0\n"
    "v_mov_b32 v0, 0x3f803f80\n"
    "v_mov_b32 v1, 0x3f803f80\n"
    "v_mov_b32 v2, 0x3f803f80\n"
    "v_mov_b32 v3, 0x3f803f80\n"
    "s_mov_b32 s69, 0x3acccccd\n"
    "s_cmp_eq_u32 s52, 1\n"
    "s_cbranch_scc1 .Lk_iw94\n"
    "s_mov_b64 exec, 0\n"
    "s_call_b64 s[100:101], .Lk_pad0\n"
    "s_call_b64 s[100:101], .Lk_pad2\n"
    "s_call_b64 s[100:101], .Lk_pad7\n"
    "s_nop 0\n"
    "s_nop 0\n"
    "s_nop 0\n"
    "s_mov_b64 exec, -1\n"
    "s_branch .Lk_iw95\n"
    ".Lk_iw94:\n"
    "s_mov_b64 exec, 0\n"
    "s_call_b64 s[100:101], .Lk_pad1\n"
    "s_call_b64 s[100:101], .Lk_pad6\n"
    "s_nop 0\n"
    "s_nop 0\n"
    "s_nop 0\n"
    "s_nop 0\n"
    "s_mov_b64 exec, -1\n"
    ".Lk_iw95:\n"
    "s_cmp_eq_u32 s46, s51\n"
    "s_cbranch_scc1 .Lk_xk97\n"
    "s_cmp_eq_u32 s46, 0\n"
    "s_cbranch_scc1 .Lk_x099\n"
    "s_setprio 1\n"
    ".Lk_x099:\n"
    "buffer_load_dwordx4 v[64:67], v250, s[8:11], s88 offen\n"
    "buffer_load_dwordx4 v[68:71], v250, s[8:11], s89 offen\n"
    "buffer_load_dwordx4 v[72:75], v250, s[8:11], s90 offen\n"
    "buffer_load_dwordx4 v[76:79], v250, s[8:11], s91 offen\n"
    "buffer_load_dwordx4 v[80:83], v250, s[8:11], s88 offen offset:256\n"
    "buffer_load_dwordx4 v[84:87], v250, s[8:11], s89 offen offset:256\n"
    "buffer_load_dwordx4 v[88:91], v250, s[8:11], s90 offen offset:256\n"
    "buffer_load_dwordx4 v[92:95], v250, s[8:11], s91 offen offset:256\n"
    "buffer_load_dwordx4 v[96:99], v250, s[8:11], s88 offen offset:512\n"
    "buffer_load_dwordx4 v[100:103], v250, s[8:11], s89 offen offset:512\n"
    "buffer_load_dwordx4 v[104:107], v250, s[8:11], s90 offen offset:512\n"
    "buffer_load_dwordx4 v[108:111], v250, s[8:11], s91 offen offset:512\n"
    "buffer_load_dwordx4 v[112:115], v250, s[8:11], s88 offen offset:768\n"
    "buffer_load_dwordx4 v[116:119], v250, s[8:11], s89 offen offset:768\n"
    "buffer_load_dwordx4 v[120:123], v250, s[8:11], s90 offen offset:768\n"
    "buffer_load_dwordx4 v[124:127], v250, s[8:11], s91 offen offset:768\n"
    "buffer_load_dwordx4 v[48:51], v250, s[8:11], s88 offen offset:1024\n"
    "buffer_load_dwordx4 v[52:55], v250, s[8:11], s89 offen offset:1024\n"
    "buffer_load_dwordx4 v[56:59], v250, s[8:11], s90 offen offset:1024\n"
    "buffer_load_dwordx4 v[60:63], v250, s[8:11], s91 offen offset:1024\n"
    "buffer_load_dwordx4 v[196:199], v250, s[8:11], s75 offen\n"
    "buffer_load_dwordx4 v[200:203], v250, s[8:11], s75 offen offset:64\n"
    "buffer_load_dwordx4 v[204:207], v250, s[8:11], s75 offen offset:128\n"
    "s_setprio 0\n"
    "ds_add_u32 v254, v253 offset:20\n"
    "s_waitcnt vmcnt(22)\n"
    "ds_write_b128 v194, v[64:67]\n"
    "ds_read_b128 v[64:67], v195\n"
    "s_waitcnt vmcnt(21)\n"
    "ds_write_b128 v194, v[68:71] offset:1024\n"
    "ds_read_b128 v[68:71], v195 offset:1024\n"
    "s_waitcnt vmcnt(20)\n"
    "ds_write_b128 v194, v[72:75]\n"
    "ds_read_b128 v[72:75], v195\n"
    "s_waitcnt vmcnt(19)\n"
    "ds_write_b128 v194, v[76:79] offset:1024\n"
    "ds_read_b128 v[76:79], v195 offset:1024\n"
    "s_waitcnt lgkmcnt(6)\n"
    "v_mfma_f32_16x16x32_bf16 v[168:171], v[0:3], v[64:67], 0\n"
    "v_mfma_f32_16x16x32_bf16 v[172:175], v[64:67], v[64:67], 0\n"
    "s_waitcnt vmcnt(18)\n"
    "ds_write_b128 v194, v[80:83]\n"
    "ds_read_b128 v[80:83], v195\n"
    "s_waitcnt lgkmcnt(6)\n"
    "v_mfma_f32_16x16x32_bf16 v[168:171], v[0:3], v[68:71], v[168:171]\n"
    "v_mfma_f32_16x16x32_bf16 v[172:175], v[68:71], v[68:71], v[172:175]\n"
    "s_waitcnt vmcnt(17)\n"
    "ds_write_b128 v194, v[84:87] offset:1024\n"
    "ds_read_b128 v[84:87], v195 offset:1024\n"
    "s_waitcnt lgkmcnt(6)\n"
    "v_mfma_f32_16x16x32_bf16 v[168:171], v[0:3], v[72:75], v[168:171]\n"
    "v_mfma_f32_16x16x32_bf16 v[172:175], v[72:75], v[72:75], v[172:175]\n"
    "s_waitcnt vmcnt(16)\n"
    "ds_write_b128 v194, v[88:91]\n"
    "ds_read_b128 v[88:91], v195\n"
    "s_waitcnt lgkmcnt(6)\n"
    "v_mfma_f32_16x16x32_bf16 v[168:171], v[0:3], v[76:79], v[168:171]\n"
    "v_mfma_f32_16x16x32_bf16 v[172:175], v[76:79], v[76:79], v[172:175]\n"
    "s_waitcnt vmcnt(15)\n"
    "ds_write_b128 v194, v[92:95] offset:1024\n"
    "ds_read_b128 v[92:95], v195 offset:1024\n"
    "s_waitcnt lgkmcnt(6)\n"
    "v_mfma_f32_16x16x32_bf16 v[168:171], v[0:3], v[80:83], v[168:171]\n"
    "v_mfma_f32_16x16x32_bf16 v[172:175], v[80:83], v[80:83], v[172:175]\n"
    "s_waitcnt vmcnt(14)\n"
    "ds_write_b128 v194, v[96:99]\n"
    "ds_read_b128 v[96:99], v195\n"
    "s_waitcnt lgkmcnt(6)\n"
    "v_mfma_f32_16x16x32_bf16 v[168:171], v[0:3], v[84:87], v[168:171]\n"
    "v_mfma_f32_16x16x32_bf16 v[172:175], v[84:87], v[84:87], v[172:175]\n"
    "s_waitcnt vmcnt(13)\n"
    "ds_write_b128 v194, v[100:103] offset:1024\n"
    "ds_read_b128 v[100:103], v195 offset:1024\n"
    "s_waitcnt lgkmcnt(6)\n"
    "v_mfma_f32_16x16x32_bf16 v[168:171], v[0:3], v[88:91], v[168:171]\n"
    "v_mfma_f32_16x16x32_bf16 v[172:175], v[88:91], v[88:91], v[172:175]\n"
    "s_waitcnt vmcnt(12)\n"
    "ds_write_b128 v194, v[104:107]\n"
    "ds_read_b128 v[104:107], v195\n"
    "s_waitcnt lgkmcnt(6)\n"
    "v_mfma_f32_16x16x32_bf16 v[168:171], v[0:3], v[92:95], v[168:171]\n"
    "v_mfma_f32_16x16x32_bf16 v[172:175], v[92:95], v[92:95], v[172:175]\n"
    "s_waitcnt vmcnt(11)\n"
    "ds_write_b128 v194, v[108:111] offset:1024\n"
    "ds_read_b128 v[108:111], v195 offset:1024\n"
    "s_waitcnt lgkmcnt(6)\n"
    "v_mfma_f32_16x16x32_bf16 v[168:171], v[0:3], v[96:99], v[168:171]\n"
    "v_mfma_f32_16x16x32_bf16 v[172:175], v[96:99], v[96:99], v[172:175]\n"
    "s_waitcnt vmcnt(10)\n"
    "ds_write_b128 v194, v[112:115]\n"
    "ds_read_b128 v[112:115], v195\n"
    "s_waitcnt lgkmcnt(6)\n"
    "v_mfma_f32_16x16x32_bf16 v[168:171], v[0:3], v[100:103], v[168:171]\n"
    "v_mfma_f32_16x16x32_bf16 v[172:175], v[100:103], v[100:103], v[172:175]\n"
    "s_waitcnt vmcnt(9)\n"
    "ds_write_b128 v194, v[116:119] offset:1024\n"
    "ds_read_b128 v[116:119], v195 offset:1024\n"
    "s_waitcnt lgkmcnt(6)\n"
    "v_mfma_f32_16x16x32_bf16 v[168:171], v[0:3], v[104:107], v[168:171]\n"
    "v_mfma_f32_16x16x32_bf16 v[172:175], v[104:107], v[104:107], v[172:175]\n"
    "s_waitcnt vmcnt(8)\n"
    "ds_write_b128 v194, v[120:123]\n"
    "ds_read_b128 v[120:123], v195\n"
    "s_waitcnt lgkmcnt(6)\n"
    "v_mfma_f32_16x16x32_bf16 v[168:171], v[0:3], v[108:111], v[168:171]\n"
    "v_mfma_f32_16x16x32_bf16 v[172:175], v[108:111], v[108:111], v[172:175]\n"
    "s_waitcnt vmcnt(7)\n"
    "ds_write_b128 v194, v[124:127] offset:1024\n"
    "ds_read_b128 v[124:127], v195 offset:1024\n"
    "s_waitcnt lgkmcnt(6)\n"
    "v_mfma_f32_16x16x32_bf16 v[168:171], v[0:3], v[112:115], v[168:171]\n"
    "v_mfma_f32_16x16x32_bf16 v[172:175], v[112:115], v[112:115], v[172:175]\n"
    "s_waitcnt vmcnt(6)\n"
    "ds_write_b128 v194, v[48:51]\n"
    "ds_read_b128 v[48:51], v195\n"
    "s_waitcnt lgkmcnt(6)\n"
    "v_mfma_f32_16x16x32_bf16 v[168:171], v[0:3], v[116:119], v[168:171]\n"
    "v_mfma_f32_16x16x32_bf16 v[172:175], v[116:119], v[116:119], v[172:175]\n"
    "s_waitcnt vmcnt(5)\n"
    "ds_write_b128 v194, v[52:55] offset:1024\n"
    "ds_read_b128 v[52:55], v195 offset:1024\n"
    "s_waitcnt lgkmcnt(6)\n"
    "v_mfma_f32_16x16x32_bf16 v[168:171], v[0:3], v[120:123], v[168:171]\n"
    "v_mfma_f32_16x16x32_bf16 v[172:175], v[120:123], v[120:123], v[172:175]\n"
    "s_waitcnt vmcnt(4)\n"
    "ds_write_b128 v194, v[56:59]\n"
    "ds_read_b128 v[56:59], v195\n"
    "s_waitcnt lgkmcnt(6)\n"
    "v_mfma_f32_16x16x32_bf16 v[168:171], v[0:3], v[124:127], v[168:171]\n"
    "v_mfma_f32_16x16x32_bf16 v[172:175], v[124:127], v[124:127], v[172:175]\n"
    "s_waitcnt vmcnt(3)\n"
    "ds_write_b128 v194, v[60:63] offset:1024\n"
    "ds_read_b128 v[60:63], v195 offset:1024\n"
    "s_waitcnt lgkmcnt(6)\n"
    "v_mfma_f32_16x16x32_bf16 v[168:171], v[0:3], v[48:51], v[168:171]\n"
    "v_mfma_f32_16x16x32_bf16 v[172:175], v[48:51], v[48:51], v[172:175]\n"
    "s_waitcnt vmcnt(2)\n"
    "ds_write_b128 v194, v[196:199]\n"
    "s_waitcnt vmcnt(1)\n"
    "ds_write_b128 v194, v[200:203] offset:1024\n"
    "ds_read_b128 v[222:225], v210\n"
    "s_waitcnt vmcnt(0)\n"
    "ds_write_b128 v194, v[204:207]\n"
    "ds_read_b128 v[226:229], v211\n"
    "ds_read_b64 v[230:231], v238\n"
    "s_branch .Lk_xj98\n"
    ".Lk_xk97:\n"
    "buffer_load_dwordx4 v[196:199], v250, s[8:11], s75 offen\n"
    "buffer_load_dwordx4 v[200:203], v250, s[8:11], s75 offen offset:64\n"
    "buffer_load_dwordx4 v[204:207], v250, s[8:11], s75 offen offset:128\n"
    ".Lk_w100:\n"
    "ds_read_b32 v252, v254 offset:0\n"
    "s_waitcnt lgkmcnt(0)\n"
    "v_readfirstlane_b32 s71, v252\n"
    "s_cmp_ge_u32 s71, 4\n"
    "s_cbranch_scc1 .Lk_d101\n"
    "s_sleep 1\n"
    "s_sub_u32 s54, s54, 1\n"
    "s_cbranch_scc0 .Lk_w100\n"
    "s_mov_b32 s54, 0\n"
    ".Lk_d101:\n"
    "ds_read_b128 v[64:67], v190\n"
    "ds_read_b128 v[68:71], v191\n"
    "ds_read_b128 v[72:75], v192\n"
    "ds_read_b128 v[76:79], v193\n"
    "ds_read_b128 v[80:83], v190 offset:4096\n"
    "ds_read_b128 v[84:87], v191 offset:4096\n"
    "ds_read_b128 v[88:91], v192 offset:4096\n"
    "ds_read_b128 v[92:95], v193 offset:4096\n"
    "ds_read_b128 v[96:99], v190 offset:8192\n"
    "ds_read_b128 v[100:103], v191 offset:8192\n"
    "ds_read_b128 v[104:107], v192 offset:8192\n"
    "ds_read_b128 v[108:111], v193 offset:8192\n"
    "ds_read_b128 v[112:115], v190 offset:12288\n"
    "ds_read_b128 v[116:119], v191 offset:12288\n"
    "ds_read_b128 v[120:123], v192 offset:12288\n"
    "ds_read_b128 v[124:127], v193 offset:12288\n"
    "ds_read_b128 v[48:51], v190 offset:16384\n"
    "ds_read_b128 v[52:55], v191 offset:16384\n"
    "ds_read_b128 v[56:59], v192 offset:16384\n"
    "ds_read_b128 v[60:63], v193 offset:16384\n"
    "s_waitcnt vmcnt(2)\n"
    "ds_write_b128 v194, v[196:199]\n"
    "s_waitcnt vmcnt(1)\n"
    "ds_write_b128 v194, v[200:203] offset:1024\n"
    "ds_read_b128 v[222:225], v210\n"
    "s_waitcnt vmcnt(0)\n"
    "ds_write_b128 v194, v[204:207]\n"
    "ds_read_b128 v[226:229], v211\n"
    "ds_read_b64 v[230:231], v238\n"
    "s_waitcnt lgkmcnt(0)\n"
    ".Lk_w102:\n"
    "ds_read_b32 v252, v254 offset:12\n"
    "s_waitcnt lgkmcnt(0)\n"
    "v_readfirstlane_b32 s71, v252\n"
    "s_cmp_ge_u32 s71, 1\n"
    "s_cbranch_scc1 .Lk_d103\n"
    "s_sleep 1\n"
    "s_sub_u32 s54, s54, 1\n"
    "s_cbranch_scc0 .Lk_w102\n"
    "s_mov_b32 s54, 0\n"
    ".Lk_d103:\n"
    "v_mfma_f32_16x16x32_bf16 v[168:171], v[0:3], v[64:67], 0\n"
    "v_mfma_f32_16x16x32_bf16 v[172:175], v[64:67], v[64:67], 0\n"
    "v_mfma_f32_16x16x32_bf16 v[168:171], v[0:3], v[68:71], v[168:171]\n"
    "v_mfma_f32_16x16x32_bf16 v[172:175], v[68:71], v[68:71], v[172:175]\n"
    "v_mfma_f32_16x16x32_bf16 v[168:171], v[0:3], v[72:75], v[168:171]\n"
    "v_mfma_f32_16x16x32_bf16 v[172:175], v[72:75], v[72:75], v[172:175]\n"
    "v_mfma_f32_16x16x32_bf16 v[168:171], v[0:3], v[76:79], v[168:171]\n"
    "v_mfma_f32_16x16x32_bf16 v[172:175], v[76:79], v[76:79], v[172:175]\n"
    "v_mfma_f32_16x16x32_bf16 v[168:171], v[0:3], v[80:83], v[168:171]\n"
    "v_mfma_f32_16x16x32_bf16 v[172:175], v[80:83], v[80:83], v[172:175]\n"
    "v_mfma_f32_16x16x32_bf16 v[168:171], v[0:3], v[84:87], v[168:171]\n"
    "v_mfma_f32_16x16x32_bf16 v[172:175], v[84:87], v[84:87], v[172:175]\n"
    "v_mfma_f32_16x16x32_bf16 v[168:171], v[0:3], v[88:91], v[168:171]\n"
    "v_mfma_f32_16x16x32_bf16 v[172:175], v[88:91], v[88:91], v[172:175]\n"
    "v_mfma_f32_16x16x32_bf16 v[168:171], v[0:3], v[92:95], v[168:171]\n"
    "v_mfma_f32_16x16x32_bf16 v[172:175], v[92:95], v[92:95], v[172:175]\n"
    "v_mfma_f32_16x16x32_bf16 v[168:171], v[0:3], v[96:99], v[168:171]\n"
    "v_mfma_f32_16x16x32_bf16 v[172:175], v[96:99], v[96:99], v[172:175]\n"
    "v_mfma_f32_16x16x32_bf16 v[168:171], v[0:3], v[100:103], v[168:171]\n"
    "v_mfma_f32_16x16x32_bf16 v[172:175], v[100:103], v[100:103], v[172:175]\n"
    "v_mfma_f32_16x16x32_bf16 v[168:171], v[0:3], v[104:107], v[168:171]\n"
    "v_mfma_f32_16x16x32_bf16 v[172:175], v[104:107], v[104:107], v[172:175]\n"
    "v_mfma_f32_16x16x32_bf16 v[168:171], v[0:3], v[108:111], v[168:171]\n"
    "v_mfma_f32_16x16x32_bf16 v[172:175], v[108:111], v[108:111], v[172:175]\n"
    "v_mfma_f32_16x16x32_bf16 v[168:171], v[0:3], v[112:115], v[168:171]\n"
    "v_mfma_f32_16x16x32_bf16 v[172:175], v[112:115], v[112:115], v[172:175]\n"
    "v_mfma_f32_16x16x32_bf16 v[168:171], v[0:3], v[116:119], v[168:171]\n"
    "v_mfma_f32_16x16x32_bf16 v[172:175], v[116:119], v[116:119], v[172:175]\n"
    "v_mfma_f32_16x16x32_bf16 v[168:171], v[0:3], v[120:123], v[168:171]\n"
    "v_mfma_f32_16x16x32_bf16 v[172:175], v[120:123], v[120:123], v[172:175]\n"
    "v_mfma_f32_16x16x32_bf16 v[168:171], v[0:3], v[124:127], v[168:171]\n"
    "v_mfma_f32_16x16x32_bf16 v[172:175], v[124:127], v[124:127], v[172:175]\n"
    "v_mfma_f32_16x16x32_bf16 v[168:171], v[0:3], v[48:51], v[168:171]\n"
    "v_mfma_f32_16x16x32_bf16 v[172:175], v[48:51], v[48:51], v[172:175]\n"
    ".Lk_xj98:\n"
    "ds_read_b32 v4, v254 offset:16\n"
    "s_waitcnt lgkmcnt(11)\n"
    "v_mfma_f32_16x16x32_bf16 v[168:171], v[0:3], v[52:55], v[168:171]\n"
    "v_mfma_f32_16x16x32_bf16 v[172:175], v[52:55], v[52:55], v[172:175]\n"
    "s_waitcnt lgkmcnt(9)\n"
    "v_mfma_f32_16x16x32_bf16 v[168:171], v[0:3], v[56:59], v[168:171]\n"
    "v_mfma_f32_16x16x32_bf16 v[172:175], v[56:59], v[56:59], v[172:175]\n"
    "s_waitcnt lgkmcnt(7)\n"
    "v_mfma_f32_16x16x32_bf16 v[168:171], v[0:3], v[60:63], v[168:171]\n"
    "v_mfma_f32_16x16x32_bf16 v[172:175], v[60:63], v[60:63], v[172:175]\n"
    "s_mul_i32 s69, s47, 0x200000\n"
    "s_add_u32 s69, s69, 0x9fffff\n"
    "s_add_u32 s8, s36, s69\n"
    "s_addc_u32 s9, s37, 0\n"
    "s_and_b32 s8, s8, 0xffe00000\n"
    "s_and_b32 s9, s9, 0xffff\n"
    "s_mov_b32 s10, 1081344\n"
    "s_mov_b32 s11, 0x20000\n"
    "s_mov_b32 s69, 0x3acccccd\n"
    "v_cndmask_b32 v169, v172, v173, s[78:79]\n"
    "v_cndmask_b32 v169, v169, v174, s[80:81]\n"
    "v_cndmask_b32 v169, v169, v175, s[82:83]\n"
    "s_mov_b64 exec, s[84:85]\n"
    "ds_write_b64 v208, v[168:169]\n"
    "s_mov_b64 exec, -1\n"
    "ds_read_b64 v[176:177], v208\n"
    "s_waitcnt lgkmcnt(2)\n"
    "v_readfirstlane_b32 s71, v4\n"
    "s_cmp_ge_u32 s71, 1\n"
    "s_cbranch_scc0 .Lk_w104\n"
    ".Lk_d105:\n"
    "s_nop 0\n"
    "ds_read_b64_tr_b16 v[8:9], v186\n"
    "ds_read_b64_tr_b16 v[10:11], v212\n"
    "ds_read_b64_tr_b16 v[12:13], v186 offset:8\n"
    "ds_read_b64_tr_b16 v[14:15], v212 offset:8\n"
    "ds_read_b64_tr_b16 v[16:17], v186 offset:80\n"
    "ds_read_b64_tr_b16 v[18:19], v212 offset:80\n"
    "ds_read_b64_tr_b16 v[20:21], v186 offset:88\n"
    "ds_read_b64_tr_b16 v[22:23], v212 offset:88\n"
    "ds_read_b64_tr_b16 v[24:25], v240 offset:32\n"
    "ds_read_b64_tr_b16 v[26:27], v244 offset:32\n"
    "ds_read_b64_tr_b16 v[28:29], v187\n"
    "ds_read_b64_tr_b16 v[30:31], v213\n"
    "ds_read_b64_tr_b16 v[32:33], v187 offset:8\n"
    "ds_read_b64_tr_b16 v[34:35], v213 offset:8\n"
    "ds_read_b64_tr_b16 v[36:37], v187 offset:80\n"
    "ds_read_b64_tr_b16 v[38:39], v213 offset:80\n"
    "ds_read_b64_tr_b16 v[40:41], v187 offset:88\n"
    "ds_read_b64_tr_b16 v[42:43], v213 offset:88\n"
    "ds_read_b64_tr_b16 v[44:45], v241 offset:32\n"
    "ds_read_b64_tr_b16 v[46:47], v245 offset:32\n"
    "s_waitcnt lgkmcnt(15)\n"
    "v_mfma_f32_16x16x32_bf16 v[128:131], v[8:11], v[64:67], 0\n"
    "v_mul_f32 v176, s69, v176\n"
    "v_mul_f32_e64 v178, v176, v176\n"
    "v_mfma_f32_16x16x32_bf16 v[148:151], v[0:3], v[8:11], 0\n"
    "ds_read_b64_tr_b16 v[8:9], v188\n"
    "ds_read_b64_tr_b16 v[10:11], v214\n"
    "v_mfma_f32_16x16x32_bf16 v[132:135], v[12:15], v[64:67], 0\n"
    "v_fma_f32 v178, v177, s69, -v178\n"
    "v_max_f32_e64 v178, 0, v178\n"
    "v_mfma_f32_16x16x32_bf16 v[152:155], v[0:3], v[12:15], 0\n"
    "ds_read_b64_tr_b16 v[12:13], v188 offset:8\n"
    "ds_read_b64_tr_b16 v[14:15], v214 offset:8\n"
    "s_waitcnt lgkmcnt(15)\n"
    "v_mfma_f32_16x16x32_bf16 v[136:139], v[16:19], v[64:67], 0\n"
    "v_add_f32 v178, 0x3727c5ac, v178\n"
    "v_rsq_f32 v178, v178\n"
    "v_mfma_f32_16x16x32_bf16 v[156:159], v[0:3], v[16:19], 0\n"
    "ds_read_b64_tr_b16 v[16:17], v188 offset:80\n"
    "ds_read_b64_tr_b16 v[18:19], v214 offset:80\n"
    "v_mfma_f32_16x16x32_bf16 v[140:143], v[20:23], v[64:67], 0\n"
    "v_mul_f32 v178, 0x3ea52b5c, v178\n"
    "v_mul_f32_e64 v176, v176, v178\n"
    "v_mfma_f32_16x16x32_bf16 v[160:163], v[0:3], v[20:23], 0\n"
    "ds_read_b64_tr_b16 v[20:21], v188 offset:88\n"
    "ds_read_b64_tr_b16 v[22:23], v214 offset:88\n"
    "s_waitcnt lgkmcnt(15)\n"
    "v_mfma_f32_16x16x32_bf16 v[144:147], v[24:27], v[64:67], 0\n"
    "v_mfma_f32_16x16x32_bf16 v[164:167], v[0:3], v[24:27], 0\n"
    "ds_read_b64_tr_b16 v[24:25], v242 offset:32\n"
    "ds_read_b64_tr_b16 v[26:27], v246 offset:32\n"
    "v_mfma_f32_16x16x32_bf16 v[128:131], v[28:31], v[68:71], v[128:131]\n"
    "ds_read_b64_tr_b16 v[28:29], v189\n"
    "ds_read_b64_tr_b16 v[30:31], v215\n"
    "ds_read_b32 v4, v254 offset:16\n"
    "s_waitcnt lgkmcnt(15)\n"
    "v_mfma_f32_16x16x32_bf16 v[132:135], v[32:35], v[68:71], v[132:135]\n"
    "ds_read_b64_tr_b16 v[32:33], v189 offset:8\n"
    "ds_read_b64_tr_b16 v[34:35], v215 offset:8\n"
    "v_mfma_f32_16x16x32_bf16 v[136:139], v[36:39], v[68:71], v[136:139]\n"
    "ds_read_b64_tr_b16 v[36:37], v189 offset:80\n"
    "ds_read_b64_tr_b16 v[38:39], v215 offset:80\n"
    "v_mfma_f32_16x16x32_bf16 v[140:143], v[40:43], v[68:71], v[140:143]\n"
    "ds_read_b64_tr_b16 v[40:41], v189 offset:88\n"
    "ds_read_b64_tr_b16 v[42:43], v215 offset:88\n"
    "s_waitcnt lgkmcnt(15)\n"
    "v_mfma_f32_16x16x32_bf16 v[144:147], v[44:47], v[68:71], v[144:147]\n"
    "ds_read_b64_tr_b16 v[44:45], v243 offset:32\n"
    "ds_read_b64_tr_b16 v[46:47], v247 offset:32\n"
    "s_waitcnt lgkmcnt(8)\n"
    "v_readfirstlane_b32 s71, v4\n"
    "s_cmp_ge_u32 s71, 2\n"
    "s_cbranch_scc0 .Lk_w106\n"
    ".Lk_d107:\n"
    "s_nop 0\n"
    "v_mfma_f32_16x16x32_bf16 v[128:131], v[8:11], v[72:75], v[128:131]\n"
    "ds_read_b64_tr_b16 v[8:9], v186 offset:20480\n"
    "ds_read_b64_tr_b16 v[10:11], v212 offset:20480\n"
    "v_mfma_f32_16x16x32_bf16 v[132:135], v[12:15], v[72:75], v[132:135]\n"
    "ds_read_b64_tr_b16 v[12:13], v186 offset:20488\n"
    "ds_read_b64_tr_b16 v[14:15], v212 offset:20488\n"
    "v_mfma_f32_16x16x32_bf16 v[136:139], v[16:19], v[72:75], v[136:139]\n"
    "ds_read_b64_tr_b16 v[16:17], v186 offset:20560\n"
    "ds_read_b64_tr_b16 v[18:19], v212 offset:20560\n"
    "v_mfma_f32_16x16x32_bf16 v[140:143], v[20:23], v[72:75], v[140:143]\n"
    "ds_read_b64_tr_b16 v[20:21], v186 offset:20568\n"
    "ds_read_b64_tr_b16 v[22:23], v212 offset:20568\n"
    "v_mfma_f32_16x16x32_bf16 v[144:147], v[24:27], v[72:75], v[144:147]\n"
    "ds_read_b64_tr_b16 v[24:25], v240 offset:20512\n"
    ".Lk_pad6:\n"
    "s_cbranch_execz .Lk_tramp\n"
    "ds_read_b64_tr_b16 v[26:27], v244 offset:20512\n"
    "v_mfma_f32_16x16x32_bf16 v[128:131], v[28:31], v[76:79], v[128:131]\n"
    "ds_read_b64_tr_b16 v[28:29], v187 offset:20480\n"
    "ds_read_b64_tr_b16 v[30:31], v213 offset:20480\n"
    "s_waitcnt lgkmcnt(15)\n"
    "v_mfma_f32_16x16x32_bf16 v[132:135], v[32:35], v[76:79], v[132:135]\n"
    "ds_read_b64_tr_b16 v[32:33], v187 offset:20488\n"
    "ds_read_b64_tr_b16 v[34:35], v213 offset:20488\n"
    "v_mfma_f32_16x16x32_bf16 v[136:139], v[36:39], v[76:79], v[136:139]\n"
    "ds_read_b64_tr_b16 v[36:37], v187 offset:20560\n"
    "ds_read_b64_tr_b16 v[38:39], v213 offset:20560\n"
    "s_waitcnt lgkmcnt(15)\n"
    "v_mfma_f32_16x16x32_bf16 v[140:143], v[40:43], v[76:79], v[140:143]\n"
    "ds_read_b64_tr_b16 v[40:41], v187 offset:20568\n"
    "ds_read_b64_tr_b16 v[42:43], v213 offset:20568\n"
    "v_mfma_f32_16x16x32_bf16 v[144:147], v[44:47], v[76:79], v[144:147]\n"
    "ds_read_b64_tr_b16 v[44:45], v241 offset:20512\n"
    "ds_read_b64_tr_b16 v[46:47], v245 offset:20512\n"
    "s_waitcnt lgkmcnt(15)\n"
    "v_mfma_f32_16x16x32_bf16 v[128:131], v[8:11], v[80:83], v[128:131]\n"
    "v_mfma_f32_16x16x32_bf16 v[148:151], v[0:3], v[8:11], v[148:151]\n"
    "ds_read_b64_tr_b16 v[8:9], v188 offset:20480\n"
    "ds_read_b64_tr_b16 v[10:11], v214 offset:20480\n"
    "v_mfma_f32_16x16x32_bf16 v[132:135], v[12:15], v[80:83], v[132:135]\n"
    "v_mfma_f32_16x16x32_bf16 v[152:155], v[0:3], v[12:15], v[152:155]\n"
    "ds_read_b64_tr_b16 v[12:13], v188 offset:20488\n"
    "ds_read_b64_tr_b16 v[14:15], v214 offset:20488\n"
    "s_waitcnt lgkmcnt(15)\n"
    "v_mfma_f32_16x16x32_bf16 v[136:139], v[16:19], v[80:83], v[136:139]\n"
    "v_mfma_f32_16x16x32_bf16 v[156:159], v[0:3], v[16:19], v[156:159]\n"
    "ds_read_b64_tr_b16 v[16:17], v188 offset:20560\n"
    "ds_read_b64_tr_b16 v[18:19], v214 offset:20560\n"
    "v_mfma_f32_16x16x32_bf16 v[140:143], v[20:23], v[80:83], v[140:143]\n"
    "v_mfma_f32_16x16x32_bf16 v[160:163], v[0:3], v[20:23], v[160:163]\n"
    "ds_read_b64_tr_b16 v[20:21], v188 offset:20568\n"
    "ds_read_b64_tr_b16 v[22:23], v214 offset:20568\n"
    "s_waitcnt lgkmcnt(15)\n"
    "v_mfma_f32_16x16x32_bf16 v[144:147], v[24:27], v[80:83], v[144:147]\n"
    "v_mfma_f32_16x16x32_bf16 v[164:167], v[0:3], v[24:27], v[164:167]\n"
    "ds_read_b64_tr_b16 v[24:25], v242 offset:20512\n"
    "ds_read_b64_tr_b16 v[26:27], v246 offset:20512\n"
    "v_mfma_f32_16x16x32_bf16 v[128:131], v[28:31], v[84:87], v[128:131]\n"
    "ds_read_b64_tr_b16 v[28:29], v189 offset:20480\n"
    "ds_read_b64_tr_b16 v[30:31], v215 offset:20480\n"
    "ds_read_b32 v4, v254 offset:16\n"
    "s_waitcnt lgkmcnt(15)\n"
    "v_mfma_f32_16x16x32_bf16 v[132:135], v[32:35], v[84:87], v[132:135]\n"
    "ds_read_b64_tr_b16 v[32:33], v189 offset:20488\n"
    "ds_read_b64_tr_b16 v[34:35], v215 offset:20488\n"
    "v_mfma_f32_16x16x32_bf16 v[136:139], v[36:39], v[84:87], v[136:139]\n"
    "ds_read_b64_tr_b16 v[36:37], v189 offset:20560\n"
    "ds_read_b64_tr_b16 v[38:39], v215 offset:20560\n"
    "v_mfma_f32_16x16x32_bf16 v[140:143], v[40:43], v[84:87], v[140:143]\n"
    "ds_read_b64_tr_b16 v[40:41], v189 offset:20568\n"
    "ds_read_b64_tr_b16 v[42:43], v215 offset:20568\n"
    "s_waitcnt lgkmcnt(15)\n"
    "v_mfma_f32_16x16x32_bf16 v[144:147], v[44:47], v[84:87], v[144:147]\n"
    "ds_read_b64_tr_b16 v[44:45], v243 offset:20512\n"
    "ds_read_b64_tr_b16 v[46:47], v247 offset:20512\n"
    "s_waitcnt lgkmcnt(8)\n"
    "v_readfirstlane_b32 s71, v4\n"
    "s_cmp_ge_u32 s71, 3\n"
    "s_cbranch_scc0 .Lk_w108\n"
    ".Lk_d109:\n"
    "v_mfma_f32_16x16x32_bf16 v[128:131], v[8:11], v[88:91], v[128:131]\n"
    "ds_read_b64_tr_b16 v[8:9], v186 offset:40960\n"
    "ds_read_b64_tr_b16 v[10:11], v212 offset:40960\n"
    "v_mfma_f32_16x16x32_bf16 v[132:135], v[12:15], v[88:91], v[132:135]\n"
    "ds_read_b64_tr_b16 v[12:13], v186 offset:40968\n"
    "ds_read_b64_tr_b16 v[14:15], v212 offset:40968\n"
    "v_mfma_f32_16x16x32_bf16 v[136:139], v[16:19], v[88:91], v[136:139]\n"
    "ds_read_b64_tr_b16 v[16:17], v186 offset:41040\n"
    "ds_read_b64_tr_b16 v[18:19], v212 offset:41040\n"
    "v_mfma_f32_16x16x32_bf16 v[140:143], v[20:23], v[88:91], v[140:143]\n"
    "ds_read_b64_tr_b16 v[20:21], v186 offset:41048\n"
    "ds_read_b64_tr_b16 v[22:23], v212 offset:41048\n"
    "v_mfma_f32_16x16x32_bf16 v[144:147], v[24:27], v[88:91], v[144:147]\n"
    "ds_read_b64_tr_b16 v[24:25], v240 offset:40992\n"
    "ds_read_b64_tr_b16 v[26:27], v244 offset:40992\n"
    "v_mfma_f32_16x16x32_bf16 v[128:131], v[28:31], v[92:95], v[128:131]\n"
    "ds_read_b64_tr_b16 v[28:29], v187 offset:40960\n"
    "ds_read_b64_tr_b16 v[30:31], v213 offset:40960\n"
    "s_waitcnt lgkmcnt(15)\n"
    "v_mfma_f32_16x16x32_bf16 v[132:135], v[32:35], v[92:95], v[132:135]\n"
    "ds_read_b64_tr_b16 v[32:33], v187 offset:40968\n"
    "ds_read_b64_tr_b16 v[34:35], v213 offset:40968\n"
    "v_mfma_f32_16x16x32_bf16 v[136:139], v[36:39], v[92:95], v[136:139]\n"
    "ds_read_b64_tr_b16 v[36:37], v187 offset:41040\n"
    "ds_read_b64_tr_b16 v[38:39], v213 offset:41040\n"
    "s_waitcnt lgkmcnt(15)\n"
    "v_mfma_f32_16x16x32_bf16 v[140:143], v[40:43], v[92:95], v[140:143]\n"
    "ds_read_b64_tr_b16 v[40:41], v187 offset:41048\n"
    "ds_read_b64_tr_b16 v[42:43], v213 offset:41048\n"
    "v_mfma_f32_16x16x32_bf16 v[144:147], v[44:47], v[92:95], v[144:147]\n"
    "ds_read_b64_tr_b16 v[44:45], v241 offset:40992\n"
    "ds_read_b64_tr_b16 v[46:47], v245 offset:40992\n"
    "s_waitcnt lgkmcnt(15)\n"
    "v_mfma_f32_16x16x32_bf16 v[128:131], v[8:11], v[96:99], v[128:131]\n"
    "v_mfma_f32_16x16x32_bf16 v[148:151], v[0:3], v[8:11], v[148:151]\n"
    "ds_read_b64_tr_b16 v[8:9], v188 offset:40960\n"
    "ds_read_b64_tr_b16 v[10:11], v214 offset:40960\n"
    "v_mfma_f32_16x16x32_bf16 v[132:135], v[12:15], v[96:99], v[132:135]\n"
    "v_mfma_f32_16x16x32_bf16 v[152:155], v[0:3], v[12:15], v[152:155]\n"
    "ds_read_b64_tr_b16 v[12:13], v188 offset:40968\n"
    "ds_read_b64_tr_b16 v[14:15], v214 offset:40968\n"
    "s_waitcnt lgkmcnt(15)\n"
    "v_mfma_f32_16x16x32_bf16 v[136:139], v[16:19], v[96:99], v[136:139]\n"
    "v_mfma_f32_16x16x32_bf16 v[156:159], v[0:3], v[16:19], v[156:159]\n"
    "ds_read_b64_tr_b16 v[16:17], v188 offset:41040\n"
    "ds_read_b64_tr_b16 v[18:19], v214 offset:41040\n"
    "v_mfma_f32_16x16x32_bf16 v[140:143], v[20:23], v[96:99], v[140:143]\n"
    "v_mfma_f32_16x16x32_bf16 v[160:163], v[0:3], v[20:23], v[160:163]\n"
    "ds_read_b64_tr_b16 v[20:21], v188 offset:41048\n"
    "ds_read_b64_tr_b16 v[22:23], v214 offset:41048\n"
    "s_waitcnt lgkmcnt(15)\n"
    "v_mfma_f32_16x16x32_bf16 v[144:147], v[24:27], v[96:99], v[144:147]\n"
    "v_mfma_f32_16x16x32_bf16 v[164:167], v[0:3], v[24:27], v[164:167]\n"
    "ds_read_b64_tr_b16 v[24:25], v242 offset:40992\n"
    "ds_read_b64_tr_b16 v[26:27], v246 offset:40992\n"
    "v_mfma_f32_16x16x32_bf16 v[128:131], v[28:31], v[100:103], v[128:131]\n"
    "ds_read_b64_tr_b16 v[28:29], v189 offset:40960\n"
    "ds_read_b64_tr_b16 v[30:31], v215 offset:40960\n"
    "ds_read_b32 v4, v254 offset:16\n"
    "s_waitcnt lgkmcnt(15)\n"
    "v_mfma_f32_16x16x32_bf16 v[132:135], v[32:35], v[100:103], v[132:135]\n"
    "ds_read_b64_tr_b16 v[32:33], v189 offset:40968\n"
    "ds_read_b64_tr_b16 v[34:35], v215 offset:40968\n"
    "v_mfma_f32_16x16x32_bf16 v[136:139], v[36:39], v[100:103], v[136:139]\n"
    "ds_read_b64_tr_b16 v[36:37], v189 offset:41040\n"
    "ds_read_b64_tr_b16 v[38:39], v215 offset:41040\n"
    "v_mfma_f32_16x16x32_bf16 v[140:143], v[40:43], v[100:103], v[140:143]\n"
    "ds_read_b64_tr_b16 v[40:41], v189 offset:41048\n"
    "ds_read_b64_tr_b16 v[42:43], v215 offset:41048\n"
    "s_waitcnt lgkmcnt(15)\n"
    "v_mfma_f32_16x16x32_bf16 v[144:147], v[44:47], v[100:103], v[144:147]\n"
    "ds_read_b64_tr_b16 v[44:45], v243 offset:40992\n"
    "ds_read_b64_tr_b16 v[46:47], v247 offset:40992\n"
    "s_waitcnt lgkmcnt(8)\n"
    "v_readfirstlane_b32 s71, v4\n"
    "s_cmp_ge_u32 s71, 4\n"
    "s_cbranch_scc0 .Lk_w110\n"
    ".Lk_d111:\n"
    "s_nop 0\n"
    "v_mfma_f32_16x16x32_bf16 v[128:131], v[8:11], v[104:107], v[128:131]\n"
    "v_add_u32 v186, 61440, v186\n"
    "ds_read_b64_tr_b16 v[8:9], v186\n"
    "v_add_u32 v212, 61440, v212\n"
    "ds_read_b64_tr_b16 v[10:11], v212\n"
    "v_mfma_f32_16x16x32_bf16 v[132:135], v[12:15], v[104:107], v[132:135]\n"
    "ds_read_b64_tr_b16 v[12:13], v186 offset:8\n"
    "ds_read_b64_tr_b16 v[14:15], v212 offset:8\n"
    "v_mfma_f32_16x16x32_bf16 v[136:139], v[16:19], v[104:107], v[136:139]\n"
    "ds_read_b64_tr_b16 v[16:17], v186 offset:80\n"
    "ds_read_b64_tr_b16 v[18:19], v212 offset:80\n"
    "v_mfma_f32_16x16x32_bf16 v[140:143], v[20:23], v[104:107], v[140:143]\n"
    "ds_read_b64_tr_b16 v[20:21], v186 offset:88\n"
    "ds_read_b64_tr_b16 v[22:23], v212 offset:88\n"
    "v_mfma_f32_16x16x32_bf16 v[144:147], v[24:27], v[104:107], v[144:147]\n"
    "v_add_u32 v240, 61440, v240\n"
    "ds_read_b64_tr_b16 v[24:25], v240 offset:32\n"
    "v_add_u32 v244, 61440, v244\n"
    "ds_read_b64_tr_b16 v[26:27], v244 offset:32\n"
    "v_mfma_f32_16x16x32_bf16 v[128:131], v[28:31], v[108:111], v[128:131]\n"
    "v_add_u32 v187, 61440, v187\n"
    "ds_read_b64_tr_b16 v[28:29], v187\n"
    "v_add_u32 v213, 61440, v213\n"
    "ds_read_b64_tr_b16 v[30:31], v213\n"
    "s_waitcnt lgkmcnt(15)\n"
    "v_mfma_f32_16x16x32_bf16 v[132:135], v[32:35], v[108:111], v[132:135]\n"
    "ds_read_b64_tr_b16 v[32:33], v187 offset:8\n"
    "ds_read_b64_tr_b16 v[34:35], v213 offset:8\n"
    "v_mfma_f32_16x16x32_bf16 v[136:139], v[36:39], v[108:111], v[136:139]\n"
    "ds_read_b64_tr_b16 v[36:37], v187 offset:80\n"
    "ds_read_b64_tr_b16 v[38:39], v213 offset:80\n"
    "s_waitcnt lgkmcnt(15)\n"
    "v_mfma_f32_16x16x32_bf16 v[140:143], v[40:43], v[108:111], v[140:143]\n"
    "ds_read_b64_tr_b16 v[40:41], v187 offset:88\n"
    "ds_read_b64_tr_b16 v[42:43], v213 offset:88\n"
    "v_mfma_f32_16x16x32_bf16 v[144:147], v[44:47], v[108:111], v[144:147]\n"
    "v_add_u32 v241, 61440, v241\n"
    "ds_read_b64_tr_b16 v[44:45], v241 offset:32\n"
    "v_add_u32 v245, 61440, v245\n"
    "ds_read_b64_tr_b16 v[46:47], v245 offset:32\n"
    "s_waitcnt lgkmcnt(15)\n"
    "s_nop 0\n"
    "v_mfma_f32_16x16x32_bf16 v[128:131], v[8:11], v[112:115], v[128:131]\n"
    "v_mfma_f32_16x16x32_bf16 v[148:151], v[0:3], v[8:11], v[148:151]\n"
    "v_add_u32 v188, 61440, v188\n"
    "ds_read_b64_tr_b16 v[8:9], v188\n"
    "v_add_u32 v214, 61440, v214\n"
    "ds_read_b64_tr_b16 v[10:11], v214\n"
    "v_mfma_f32_16x16x32_bf16 v[132:135], v[12:15], v[112:115], v[132:135]\n"
    "v_mfma_f32_16x16x32_bf16 v[152:155], v[0:3], v[12:15], v[152:155]\n"
    "ds_read_b64_tr_b16 v[12:13], v188 offset:8\n"
    "ds_read_b64_tr_b16 v[14:15], v214 offset:8\n"
    "s_waitcnt lgkmcnt(15)\n"
    "v_mfma_f32_16x16x32_bf16 v[136:139], v[16:19], v[112:115], v[136:139]\n"
    "v_mfma_f32_16x16x32_bf16 v[156:159], v[0:3], v[16:19], v[156:159]\n"
    "ds_read_b64_tr_b16 v[16:17], v188 offset:80\n"
    "ds_read_b64_tr_b16 v[18:19], v214 offset:80\n"
    "v_mfma_f32_16x16x32_bf16 v[140:143], v[20:23], v[112:115], v[140:143]\n"
    "v_mfma_f32_16x16x32_bf16 v[160:163], v[0:3], v[20:23], v[160:163]\n"
    "ds_read_b64_tr_b16 v[20:21], v188 offset:88\n"
    "ds_read_b64_tr_b16 v[22:23], v214 offset:88\n"
    "s_waitcnt lgkmcnt(15)\n"
    "v_mfma_f32_16x16x32_bf16 v[144:147], v[24:27], v[112:115], v[144:147]\n"
    "v_mfma_f32_16x16x32_bf16 v[164:167], v[0:3], v[24:27], v[164:167]\n"
    "v_add_u32 v242, 61440, v242\n"
    "ds_read_b64_tr_b16 v[24:25], v242 offset:32\n"
    "v_add_u32 v246, 61440, v246\n"
    "ds_read_b64_tr_b16 v[26:27], v246 offset:32\n"
    "v_mfma_f32_16x16x32_bf16 v[128:131], v[28:31], v[116:119], v[128:131]\n"
    "v_add_u32 v189, 61440, v189\n"
    "ds_read_b64_tr_b16 v[28:29], v189\n"
    "v_add_u32 v215, 61440, v215\n"
    "ds_read_b64_tr_b16 v[30:31], v215\n"
    "s_waitcnt lgkmcnt(15)\n"
    "v_mfma_f32_16x16x32_bf16 v[132:135], v[32:35], v[116:119], v[132:135]\n"
    "ds_read_b64_tr_b16 v[32:33], v189 offset:8\n"
    "ds_read_b64_tr_b16 v[34:35], v215 offset:8\n"
    "v_mfma_f32_16x16x32_bf16 v[136:139], v[36:39], v[116:119], v[136:139]\n"
    "ds_read_b64_tr_b16 v[36:37], v189 offset:80\n"
    "ds_read_b64_tr_b16 v[38:39], v215 offset:80\n"
    "s_waitcnt lgkmcnt(15)\n"
    "v_mfma_f32_16x16x32_bf16 v[140:143], v[40:43], v[116:119], v[140:143]\n"
    "ds_read_b64_tr_b16 v[40:41], v189 offset:88\n"
    "ds_read_b64_tr_b16 v[42:43], v215 offset:88\n"
    "v_mfma_f32_16x16x32_bf16 v[144:147], v[44:47], v[116:119], v[144:147]\n"
    "v_add_u32 v243, 61440, v243\n"
    "ds_read_b64_tr_b16 v[44:45], v243 offset:32\n"
    "v_add_u32 v247, 61440, v247\n"
    "ds_read_b64_tr_b16 v[46:47], v247 offset:32\n"
    "s_waitcnt lgkmcnt(15)\n"
    "v_mfma_f32_16x16x32_bf16 v[128:131], v[8:11], v[120:123], v[128:131]\n"
    "v_mfma_f32_16x16x32_bf16 v[132:135], v[12:15], v[120:123], v[132:135]\n"
    "s_waitcnt lgkmcnt(14)\n"
    "v_mfma_f32_16x16x32_bf16 v[136:139], v[16:19], v[120:123], v[136:139]\n"
    "s_waitcnt lgkmcnt(12)\n"
    "v_mfma_f32_16x16x32_bf16 v[140:143], v[20:23], v[120:123], v[140:143]\n"
    "s_waitcnt lgkmcnt(10)\n"
    "v_mfma_f32_16x16x32_bf16 v[144:147], v[24:27], v[120:123], v[144:147]\n"
    "s_waitcnt lgkmcnt(8)\n"
    "v_mfma_f32_16x16x32_bf16 v[128:131], v[28:31], v[124:127], v[128:131]\n"
    "ds_read_b32 v4, v254 offset:16\n"
    "s_waitcnt lgkmcnt(7)\n"
    "v_mfma_f32_16x16x32_bf16 v[132:135], v[32:35], v[124:127], v[132:135]\n"
    "s_waitcnt lgkmcnt(5)\n"
    "v_mfma_f32_16x16x32_bf16 v[136:139], v[36:39], v[124:127], v[136:139]\n"
    "s_waitcnt lgkmcnt(3)\n"
    "v_mfma_f32_16x16x32_bf16 v[140:143], v[40:43], v[124:127], v[140:143]\n"
    "s_waitcnt lgkmcnt(1)\n"
    "v_mfma_f32_16x16x32_bf16 v[144:147], v[44:47], v[124:127], v[144:147]\n"
    "s_waitcnt lgkmcnt(0)\n"
    "v_readfirstlane_b32 s71, v4\n"
    "s_cmp_ge_u32 s71, 5\n"
    "s_cbranch_scc0 .Lk_w112\n"
    ".Lk_d113:\n"
    "s_nop 0\n"
    "ds_read_b64_tr_b16 v[8:9], v186 offset:20480\n"
    "ds_read_b64_tr_b16 v[10:11], v212 offset:20480\n"
    "ds_read_b64_tr_b16 v[12:13], v186 offset:20488\n"
    "ds_read_b64_tr_b16 v[14:15], v212 offset:20488\n"
    "ds_read_b64_tr_b16 v[16:17], v186 offset:20560\n"
    "ds_read_b64_tr_b16 v[18:19], v212 offset:20560\n"
    "ds_read_b64_tr_b16 v[20:21], v186 offset:20568\n"
    "ds_read_b64_tr_b16 v[22:23], v212 offset:20568\n"
    "ds_read_b64_tr_b16 v[24:25], v240 offset:20512\n"
    "ds_read_b64_tr_b16 v[26:27], v244 offset:20512\n"
    "ds_read_b64_tr_b16 v[28:29], v187 offset:20480\n"
    "ds_read_b64_tr_b16 v[30:31], v213 offset:20480\n"
    "ds_read_b64_tr_b16 v[32:33], v187 offset:20488\n"
    "ds_read_b64_tr_b16 v[34:35], v213 offset:20488\n"
    "ds_read_b64_tr_b16 v[36:37], v187 offset:20560\n"
    "ds_read_b64_tr_b16 v[38:39], v213 offset:20560\n"
    "ds_read_b64_tr_b16 v[40:41], v187 offset:20568\n"
    "ds_read_b64_tr_b16 v[42:43], v213 offset:20568\n"
    "ds_read_b64_tr_b16 v[44:45], v241 offset:20512\n"
    "ds_read_b64_tr_b16 v[46:47], v245 offset:20512\n"
    "s_waitcnt lgkmcnt(15)\n"
    "v_mfma_f32_16x16x32_bf16 v[148:151], v[0:3], v[8:11], v[148:151]\n"
    "v_mfma_f32_16x16x32_bf16 v[152:155], v[0:3], v[12:15], v[152:155]\n"
    "s_waitcnt lgkmcnt(14)\n"
    "v_mfma_f32_16x16x32_bf16 v[156:159], v[0:3], v[16:19], v[156:159]\n"
    "s_waitcnt lgkmcnt(12)\n"
    "v_mfma_f32_16x16x32_bf16 v[160:163], v[0:3], v[20:23], v[160:163]\n"
    "s_waitcnt lgkmcnt(10)\n"
    "v_mfma_f32_16x16x32_bf16 v[164:167], v[0:3], v[24:27], v[164:167]\n"
    "v_mfma_f32_16x16x32_bf16 v[128:131], v[8:11], v[48:51], v[128:131]\n"
    "ds_read_b64_tr_b16 v[8:9], v188 offset:20480\n"
    "ds_read_b64_tr_b16 v[10:11], v214 offset:20480\n"
    "v_mfma_f32_16x16x32_bf16 v[132:135], v[12:15], v[48:51], v[132:135]\n"
    "ds_read_b64_tr_b16 v[12:13], v188 offset:20488\n"
    "ds_read_b64_tr_b16 v[14:15], v214 offset:20488\n"
    "ds_write_b32 v209, v148\n"
    "v_mfma_f32_16x16x32_bf16 v[136:139], v[16:19], v[48:51], v[136:139]\n"
    "ds_read_b64_tr_b16 v[16:17], v188 offset:20560\n"
    "ds_read_b64_tr_b16 v[18:19], v214 offset:20560\n"
    "ds_write_b32 v209, v152 offset:64\n"
    "v_mfma_f32_16x16x32_bf16 v[140:143], v[20:23], v[48:51], v[140:143]\n"
    "ds_read_b64_tr_b16 v[20:21], v188 offset:20568\n"
    "ds_read_b64_tr_b16 v[22:23], v214 offset:20568\n"
    "ds_write_b32 v209, v156 offset:128\n"
    "v_mfma_f32_16x16x32_bf16 v[144:147], v[24:27], v[48:51], v[144:147]\n"
    "ds_read_b64_tr_b16 v[24:25], v242 offset:20512\n"
    "ds_read_b64_tr_b16 v[26:27], v246 offset:20512\n"
    "ds_write_b32 v209, v160 offset:192\n"
    "ds_write_b32 v209, v164 offset:256\n"
    "ds_add_u32 v254, v253 offset:100\n"
    "s_waitcnt lgkmcnt(15)\n"
    "s_nop 0\n"
    "v_mfma_f32_16x16x32_bf16 v[128:131], v[28:31], v[52:55], v[128:131]\n"
    "ds_read_b64_tr_b16 v[28:29], v189 offset:20480\n"
    "ds_read_b64_tr_b16 v[30:31], v215 offset:20480\n"
    "v_mfma_f32_16x16x32_bf16 v[132:135], v[32:35], v[52:55], v[132:135]\n"
    "ds_read_b64_tr_b16 v[32:33], v189 offset:20488\n"
    "ds_read_b64_tr_b16 v[34:35], v215 offset:20488\n"
    "v_mfma_f32_16x16x32_bf16 v[136:139], v[36:39], v[52:55], v[136:139]\n"
    "ds_read_b64_tr_b16 v[36:37], v189 offset:20560\n"
    "ds_read_b64_tr_b16 v[38:39], v215 offset:20560\n"
    "v_mfma_f32_16x16x32_bf16 v[140:143], v[40:43], v[52:55], v[140:143]\n"
    "ds_read_b64_tr_b16 v[40:41], v189 offset:20568\n"
    "ds_read_b64_tr_b16 v[42:43], v215 offset:20568\n"
    "v_mfma_f32_16x16x32_bf16 v[144:147], v[44:47], v[52:55], v[144:147]\n"
    "ds_read_b64_tr_b16 v[44:45], v243 offset:20512\n"
    "ds_read_b64_tr_b16 v[46:47], v247 offset:20512\n"
    "v_mfma_f32_32x32x8_bf16 v[184:199], v[180:181], v[180:181], 0\n"
    "v_mfma_f32_32x32x8_bf16 v[200:215], v[180:181], v[180:181], 0\n"
    "v_mov_b64 v[92:93], 0\n"
    "v_mov_b64 v[94:95], 0\n"
    "v_mov_b64 v[104:105], 0\n"
    "v_mov_b64 v[106:107], 0\n"
    "v_mov_b32 v183, 159944\n"
    "s_waitcnt lgkmcnt(15)\n"
    "v_mfma_f32_16x16x32_bf16 v[128:131], v[8:11], v[56:59], v[128:131]\n"
    "v_mfma_f32_16x16x32_bf16 v[132:135], v[12:15], v[56:59], v[132:135]\n"
    "v_mfma_f32_16x16x32_bf16 v[136:139], v[16:19], v[56:59], v[136:139]\n"
    "v_mfma_f32_16x16x32_bf16 v[140:143], v[20:23], v[56:59], v[140:143]\n"
    "s_waitcnt lgkmcnt(13)\n"
    "v_mfma_f32_16x16x32_bf16 v[144:147], v[24:27], v[56:59], v[144:147]\n"
    "ds_read_b32 v4, v254 offset:108\n"
    "ds_read_b32 v221, v254 offset:24\n"
    "s_waitcnt lgkmcnt(10)\n"
    "v_mfma_f32_16x16x32_bf16 v[128:131], v[28:31], v[60:63], v[128:131]\n"
    "s_waitcnt lgkmcnt(8)\n"
    "v_mfma_f32_16x16x32_bf16 v[132:135], v[32:35], v[60:63], v[132:135]\n"
    "s_waitcnt lgkmcnt(6)\n"
    "v_mfma_f32_16x16x32_bf16 v[136:139], v[36:39], v[60:63], v[136:139]\n"
    "s_waitcnt lgkmcnt(4)\n"
    "v_mfma_f32_16x16x32_bf16 v[140:143], v[40:43], v[60:63], v[140:143]\n"
    "s_waitcnt lgkmcnt(2)\n"
    "v_mfma_f32_16x16x32_bf16 v[144:147], v[44:47], v[60:63], v[144:147]\n"
    "s_waitcnt lgkmcnt(1)\n"
    "v_readfirstlane_b32 s71, v4\n"
    "s_cmp_ge_u32 s71, 1\n"
    "s_cbranch_scc0 .Lk_w114\n"
    ".Lk_d115:\n"
    "s_nop 0\n"
    "ds_read_b128 v[148:151], v239\n"
    "ds_read_b128 v[152:155], v239 offset:64\n"
    "ds_read_b128 v[156:159], v239 offset:128\n"
    "ds_read_b128 v[160:163], v239 offset:192\n"
    "ds_read_b128 v[164:167], v239 offset:256\n"
    "v_pk_mul_f32 v[128:129], v[178:179], v[128:129] op_sel_hi:[0,1]\n"
    "v_pk_mul_f32 v[130:131], v[178:179], v[130:131] op_sel_hi:[0,1]\n"
    "v_pk_mul_f32 v[132:133], v[178:179], v[132:133] op_sel_hi:[0,1]\n"
    "v_pk_mul_f32 v[134:135], v[178:179], v[134:135] op_sel_hi:[0,1]\n"
    "v_pk_mul_f32 v[136:137], v[178:179], v[136:137] op_sel_hi:[0,1]\n"
    "v_pk_mul_f32 v[138:139], v[178:179], v[138:139] op_sel_hi:[0,1]\n"
    "v_pk_mul_f32 v[140:141], v[178:179], v[140:141] op_sel_hi:[0,1]\n"
    "v_pk_mul_f32 v[142:143], v[178:179], v[142:143] op_sel_hi:[0,1]\n"
    "v_pk_mul_f32 v[144:145], v[178:179], v[144:145] op_sel_hi:[0,1]\n"
    "v_pk_mul_f32 v[146:147], v[178:179], v[146:147] op_sel_hi:[0,1]\n"
    "s_waitcnt lgkmcnt(5)\n"
    "ds_read_b32 v221, v254 offset:24\n"
    "s_waitcnt lgkmcnt(5)\n"
    "v_pk_fma_f32 v[128:129], v[176:177], v[148:149], v[128:129] op_sel_hi:[0,1,1] neg_lo:[1,0,0] neg_hi:[1,0,0]\n"
    "v_pk_fma_f32 v[130:131], v[176:177], v[150:151], v[130:131] op_sel_hi:[0,1,1] neg_lo:[1,0,0] neg_hi:[1,0,0]\n"
    "v_cvt_pk_bf16_f32 v96, v128, v129\n"
    "v_cvt_pk_bf16_f32 v97, v130, v131\n"
    "s_waitcnt lgkmcnt(4)\n"
    "v_pk_fma_f32 v[132:133], v[176:177], v[152:153], v[132:133] op_sel_hi:[0,1,1] neg_lo:[1,0,0] neg_hi:[1,0,0]\n"
    "v_pk_fma_f32 v[134:135], v[176:177], v[154:155], v[134:135] op_sel_hi:[0,1,1] neg_lo:[1,0,0] neg_hi:[1,0,0]\n"
    "v_cvt_pk_bf16_f32 v98, v132, v133\n"
    "v_cvt_pk_bf16_f32 v99, v134, v135\n"
    "s_waitcnt lgkmcnt(3)\n"
    "v_pk_fma_f32 v[136:137], v[176:177], v[156:157], v[136:137] op_sel_hi:[0,1,1] neg_lo:[1,0,0] neg_hi:[1,0,0]\n"
    "v_pk_fma_f32 v[138:139], v[176:177], v[158:159], v[138:139] op_sel_hi:[0,1,1] neg_lo:[1,0,0] neg_hi:[1,0,0]\n"
    "v_cvt_pk_bf16_f32 v100, v136, v137\n"
    "v_cvt_pk_bf16_f32 v101, v138, v139\n"
    "s_waitcnt lgkmcnt(2)\n"
    "v_pk_fma_f32 v[140:141], v[176:177], v[160:161], v[140:141] op_sel_hi:[0,1,1] neg_lo:[1,0,0] neg_hi:[1,0,0]\n"
    "v_pk_fma_f32 v[142:143], v[176:177], v[162:163], v[142:143] op_sel_hi:[0,1,1] neg_lo:[1,0,0] neg_hi:[1,0,0]\n"
    "v_cvt_pk_bf16_f32 v102, v140, v141\n"
    "v_cvt_pk_bf16_f32 v103, v142, v143\n"
    "s_waitcnt lgkmcnt(1)\n"
    "v_pk_fma_f32 v[144:145], v[176:177], v[164:165], v[144:145] op_sel_hi:[0,1,1] neg_lo:[1,0,0] neg_hi:[1,0,0]\n"
    "v_pk_fma_f32 v[146:147], v[176:177], v[166:167], v[146:147] op_sel_hi:[0,1,1] neg_lo:[1,0,0] neg_hi:[1,0,0]\n"
    "s_mov_b32 exec_hi, 0\n"
    "v_cvt_pk_bf16_f32 v104, v144, v145\n"
    "v_cvt_pk_bf16_f32 v105, v146, v147\n"
    "s_not_b64 exec, exec\n"
    "v_cvt_pk_bf16_f32 v106, v144, v145\n"
    "v_cvt_pk_bf16_f32 v107, v146, v147\n"
    "s_mov_b64 exec, -1\n"
    "s_waitcnt lgkmcnt(0)\n"
    "v_readfirstlane_b32 s71, v221\n"
    "s_andn2_b32 s70, 1, s71\n"
    "s_cmp_eq_u32 s70, 0\n"
    "s_cbranch_scc0 .Lk_w116\n"
    ".Lk_d117:\n"
    "ds_read_b128 v[64:67], v217\n"
    "ds_read_b128 v[72:75], v218\n"
    "ds_read_b128 v[68:71], v217 offset:1024\n"
    "ds_read_b128 v[80:83], v219\n"
    "ds_read_b128 v[88:91], v219 offset:3072\n"
    "ds_read_b128 v[84:87], v219 offset:768\n"
    "ds_read_b128 v[76:79], v218 offset:512\n"
    "ds_read_b32 v183, v183\n"
    "ds_read_b32 v221, v254 offset:24\n"
    "ds_add_u32 v254, v253 offset:44\n"
    "v_mov_b32 v182, 0\n"
    "v_dot2c_f32_bf16 v182, v96, v96\n"
    "v_dot2c_f32_bf16 v182, v97, v97\n"
    "v_dot2c_f32_bf16 v182, v98, v98\n"
    "v_dot2c_f32_bf16 v182, v99, v99\n"
    "v_dot2c_f32_bf16 v182, v100, v100\n"
    "v_dot2c_f32_bf16 v182, v101, v101\n"
    "v_dot2c_f32_bf16 v182, v102, v102\n"
    "v_dot2c_f32_bf16 v182, v103, v103\n"
    "v_dot2c_f32_bf16 v182, v104, v104\n"
    "v_dot2c_f32_bf16 v182, v105, v105\n"
    "v_dot2c_f32_bf16 v182, v106, v106\n"
    "v_dot2c_f32_bf16 v182, v107, v107\n"
    "v_mov_b64 v[56:57], 0\n"
    "v_mov_b64 v[58:59], 0\n"
    "v_mov_b64 v[60:61], 0\n"
    "v_mov_b64 v[62:63], 0\n"
    "s_waitcnt lgkmcnt(9)\n"
    "v_mfma_f32_32x32x16_bf16 v[0:15], v[64:67], v[96:99], 0\n"
    "s_waitcnt lgkmcnt(8)\n"
    "v_mfma_f32_32x32x8_bf16 v[0:15], v[72:73], v[104:105], v[0:15]\n"
    "v_mov_b32 v181, v182\n"
    "s_nop 1\n"
    "v_permlane32_swap_b32 v182, v181\n"
    "v_add_f32 v182, v182, v181\n"
    "s_waitcnt lgkmcnt(2)\n"
    "v_mul_f32_e64 v182, v182, v183\n"
    "v_cmp_ge_f32 vcc, 0x45c80000, v182\n"
    "s_cmp_eq_u64 vcc, exec\n"
    "s_cselect_b32 s86, 0, 1\n"
    "s_waitcnt lgkmcnt(1)\n"
    "v_readfirstlane_b32 s71, v221\n"
    "s_andn2_b32 s70, 512, s71\n"
    "s_cmp_eq_u32 s70, 0\n"
    "s_cbranch_scc0 .Lk_w118\n"
    ".Lk_d119:\n"
    "s_waitcnt lgkmcnt(0)\n"
    ".p2align 6\n"
    ".Lk_att120:\n"
    "v_mfma_f32_32x32x16_bf16 v[16:31], v[64:67], v[100:103], 0\n"
    "v_exp_f32 v0, v0\n"
    "v_exp_f32 v1, v1\n"
    "v_exp_f32 v2, v2\n"
    "v_exp_f32 v3, v3\n"
    "v_mfma_f32_32x32x8_bf16 v[16:31], v[72:73], v[106:107], v[16:31]\n"
    "v_exp_f32 v4, v4\n"
    "v_exp_f32 v5, v5\n"
    "v_exp_f32 v6, v6\n"
    "v_exp_f32 v7, v7\n"
    "v_smfmac_f32_32x32x32_bf16 v[200:215], v[92:95], v[56:63], v216\n"
    "ds_read_b128 v[92:95], v219 offset:3840\n"
    "v_exp_f32 v8, v8\n"
    "v_exp_f32 v9, v9\n"
    "v_exp_f32 v10, v10\n"
    "v_exp_f32 v11, v11\n"
    "v_smfmac_f32_32x32x32_bf16 v[184:199], v[80:83], v[0:7], v216\n"
    "s_waitcnt lgkmcnt(1)\n"
    "ds_read_b128 v[64:67], v217 offset:2048\n"
    "v_exp_f32 v12, v12\n"
    "v_exp_f32 v13, v13\n"
    "v_exp_f32 v14, v14\n"
    "v_exp_f32_e64 v15, v15\n"
    "v_mfma_f32_32x32x16_bf16 v[32:47], v[68:71], v[96:99], 0\n"
    "v_exp_f32 v16, v16\n"
    "v_exp_f32 v17, v17\n"
    "v_exp_f32 v18, v18\n"
    "v_exp_f32 v19, v19\n"
    "v_mfma_f32_32x32x8_bf16 v[32:47], v[74:75], v[104:105], v[32:47]\n"
    "v_exp_f32 v20, v20\n"
    "v_exp_f32 v21, v21\n"
    "v_exp_f32 v22, v22\n"
    "v_exp_f32 v23, v23\n"
    "v_smfmac_f32_32x32x32_bf16 v[184:199], v[88:91], v[8:15], v216\n"
    "v_exp_f32 v24, v24\n"
    "v_exp_f32 v25, v25\n"
    "v_exp_f32 v26, v26\n"
    "v_exp_f32 v27, v27\n"
    "v_smfmac_f32_32x32x32_bf16 v[200:215], v[80:83], v[16:23], v216\n"
    "ds_read_b128 v[80:83], v219 offset:1536\n"
    "v_exp_f32 v28, v28\n"
    "v_exp_f32 v29, v29\n"
    "v_exp_f32 v30, v30\n"
    "v_exp_f32 v31, v31\n"
    "v_mfma_f32_32x32x16_bf16 v[48:63], v[68:71], v[100:103], 0\n"
    "v_exp_f32 v32, v32\n"
    "v_exp_f32 v33, v33\n"
    "v_exp_f32 v34, v34\n"
    "v_exp_f32 v35, v35\n"
    "v_mfma_f32_32x32x8_bf16 v[48:63], v[74:75], v[106:107], v[48:63]\n"
    "v_exp_f32 v36, v36\n"
    "v_exp_f32 v37, v37\n"
    "v_exp_f32 v38, v38\n"
    "v_exp_f32 v39, v39\n"
    "v_smfmac_f32_32x32x32_bf16 v[200:215], v[88:91], v[24:31], v216\n"
    "ds_read_b128 v[88:91], v219 offset:4608\n"
    "v_exp_f32 v40, v40\n"
    "v_exp_f32 v41, v41\n"
    "v_exp_f32 v42, v42\n"
    "v_exp_f32 v43, v43\n"
    "v_smfmac_f32_32x32x32_bf16 v[184:199], v[84:87], v[32:39], v216\n"
    "s_waitcnt lgkmcnt(1)\n"
    "ds_read_b128 v[68:71], v217 offset:3072\n"
    "ds_read_b128 v[72:75], v218 offset:11264\n"
    "v_exp_f32 v44, v44\n"
    "v_exp_f32 v45, v45\n"
    "v_exp_f32 v46, v46\n"
    "v_exp_f32_e64 v47, v47\n"
    "v_mfma_f32_32x32x16_bf16 v[0:15], v[64:67], v[96:99], 0\n"
    "v_exp_f32 v48, v48\n"
    "v_exp_f32 v49, v49\n"
    "v_exp_f32 v50, v50\n"
    "v_exp_f32 v51, v51\n"
    "v_mfma_f32_32x32x8_bf16 v[0:15], v[76:77], v[104:105], v[0:15]\n"
    "v_exp_f32 v52, v52\n"
    "v_exp_f32 v53, v53\n"
    "v_exp_f32 v54, v54\n"
    "v_exp_f32 v55, v55\n"
    "v_smfmac_f32_32x32x32_bf16 v[184:199], v[92:95], v[40:47], v216\n"
    "v_exp_f32 v56, v56\n"
    "v_exp_f32 v57, v57\n"
    "v_exp_f32 v58, v58\n"
    "v_exp_f32 v59, v59\n"
    "v_smfmac_f32_32x32x32_bf16 v[200:215], v[84:87], v[48:55], v216\n"
    "ds_read_b128 v[84:87], v219 offset:2304\n"
    "v_exp_f32 v60, v60\n"
    "v_exp_f32 v61, v61\n"
    "v_exp_f32 v62, v62\n"
    "v_exp_f32 v63, v63\n"
    "v_mfma_f32_32x32x16_bf16 v[16:31], v[64:67], v[100:103], 0\n"
    "v_exp_f32 v0, v0\n"
    "v_exp_f32 v1, v1\n"
    "v_exp_f32 v2, v2\n"
    "v_exp_f32 v3, v3\n"
    "v_mfma_f32_32x32x8_bf16 v[16:31], v[76:77], v[106:107], v[16:31]\n"
    "v_exp_f32 v4, v4\n"
    "v_exp_f32 v5, v5\n"
    "v_exp_f32 v6, v6\n"
    "v_exp_f32 v7, v7\n"
    "v_smfmac_f32_32x32x32_bf16 v[200:215], v[92:95], v[56:63], v216\n"
    "ds_read_b128 v[92:95], v219 offset:5376\n"
    "v_exp_f32 v8, v8\n"
    "v_exp_f32 v9, v9\n"
    "v_exp_f32 v10, v10\n"
    "v_exp_f32 v11, v11\n"
    "v_smfmac_f32_32x32x32_bf16 v[184:199], v[80:83], v[0:7], v216\n"
    "s_waitcnt lgkmcnt(1)\n"
    "ds_read_b128 v[64:67], v217 offset:11264\n"
    "v_exp_f32 v12, v12\n"
    "v_exp_f32 v13, v13\n"
    "v_exp_f32 v14, v14\n"
    "v_exp_f32_e64 v15, v15\n"
    "v_mfma_f32_32x32x16_bf16 v[32:47], v[68:71], v[96:99], 0\n"
    "v_exp_f32 v16, v16\n"
    "v_exp_f32 v17, v17\n"
    "v_exp_f32 v18, v18\n"
    "v_exp_f32 v19, v19\n"
    "v_mfma_f32_32x32x8_bf16 v[32:47], v[78:79], v[104:105], v[32:47]\n"
    "v_exp_f32 v20, v20\n"
    "v_exp_f32 v21, v21\n"
    "v_exp_f32 v22, v22\n"
    "v_exp_f32 v23, v23\n"
    "v_smfmac_f32_32x32x32_bf16 v[184:199], v[88:91], v[8:15], v216\n"
    "s_sext_i32_i8 s92, s28\n"
    "s_lshl_b32 s92, s92, 11\n"
    "v_exp_f32 v24, v24\n"
    "v_exp_f32 v25, v25\n"
    "v_exp_f32 v26, v26\n"
    "v_exp_f32 v27, v27\n"
    "v_smfmac_f32_32x32x32_bf16 v[200:215], v[80:83], v[16:23], v216\n"
    "ds_read_b128 v[80:83], v219 offset:11264\n"
    "v_exp_f32 v28, v28\n"
    "v_exp_f32 v29, v29\n"
    "v_exp_f32 v30, v30\n"
    "v_exp_f32 v31, v31\n"
    "v_mfma_f32_32x32x16_bf16 v[48:63], v[68:71], v[100:103], 0\n"
    "v_exp_f32 v32, v32\n"
    "v_exp_f32 v33, v33\n"
    "v_exp_f32 v34, v34\n"
    "v_exp_f32 v35, v35\n"
    "v_mfma_f32_32x32x8_bf16 v[48:63], v[78:79], v[106:107], v[48:63]\n"
    "v_exp_f32 v36, v36\n"
    "v_exp_f32 v37, v37\n"
    "v_exp_f32 v38, v38\n"
    "v_exp_f32 v39, v39\n"
    "v_smfmac_f32_32x32x32_bf16 v[200:215], v[88:91], v[24:31], v216\n"
    "ds_read_b128 v[88:91], v219 offset:14336\n"
    "v_exp_f32 v40, v40\n"
    "v_exp_f32 v41, v41\n"
    "v_exp_f32 v42, v42\n"
    "v_exp_f32 v43, v43\n"
    "v_smfmac_f32_32x32x32_bf16 v[184:199], v[84:87], v[32:39], v216\n"
    "s_waitcnt lgkmcnt(1)\n"
    "ds_read_b128 v[68:71], v217 offset:12288\n"
    "ds_read_b128 v[76:79], v218 offset:11776\n"
    "v_exp_f32 v44, v44\n"
    "v_exp_f32 v45, v45\n"
    "v_exp_f32 v46, v46\n"
    "v_exp_f32_e64 v47, v47\n"
    "v_mfma_f32_32x32x16_bf16 v[0:15], v[64:67], v[96:99], 0\n"
    "v_exp_f32 v48, v48\n"
    "v_exp_f32 v49, v49\n"
    "v_exp_f32 v50, v50\n"
    "v_exp_f32 v51, v51\n"
    "v_mfma_f32_32x32x8_bf16 v[0:15], v[72:73], v[104:105], v[0:15]\n"
    "v_exp_f32 v52, v52\n"
    "v_exp_f32 v53, v53\n"
    "v_exp_f32 v54, v54\n"
    "v_exp_f32 v55, v55\n"
    "v_smfmac_f32_32x32x32_bf16 v[184:199], v[92:95], v[40:47], v216\n"
    "s_lshr_b64 s[28:29], s[28:29], 8\n"
    "s_add_u32 s87, s87, 1\n"
    "ds_read_b32 v221, v254 offset:24\n"
    "v_exp_f32 v56, v56\n"
    "v_exp_f32 v57, v57\n"
    ".Lk_pad7:\n"
    "s_cbranch_execz .Lk_tramp\n"
    "v_exp_f32 v58, v58\n"
    "v_exp_f32_e64 v59, v59\n"
    "v_smfmac_f32_32x32x32_bf16 v[200:215], v[84:87], v[48:55], v216\n"
    "ds_read_b128 v[84:87], v219 offset:12032\n"
    "v_exp_f32 v60, v60\n"
    "v_exp_f32 v61, v61\n"
    "v_exp_f32 v62, v62\n"
    "v_exp_f32 v63, v63\n"
    "v_mfma_f32_32x32x16_bf16 v[16:31], v[64:67], v[100:103], 0\n"
    "v_exp_f32 v0, v0\n"
    "v_exp_f32 v1, v1\n"
    "v_exp_f32 v2, v2\n"
    "v_exp_f32 v3, v3\n"
    "v_mfma_f32_32x32x8_bf16 v[16:31], v[72:73], v[106:107], v[16:31]\n"
    "v_exp_f32 v4, v4\n"
    "v_exp_f32 v5, v5\n"
    "v_exp_f32 v6, v6\n"
    "v_exp_f32 v7, v7\n"
    "v_smfmac_f32_32x32x32_bf16 v[200:215], v[92:95], v[56:63], v216\n"
    "ds_read_b128 v[92:95], v219 offset:15104\n"
    "v_exp_f32 v8, v8\n"
    "v_exp_f32 v9, v9\n"
    "v_exp_f32 v10, v10\n"
    "v_exp_f32 v11, v11\n"
    "v_smfmac_f32_32x32x32_bf16 v[184:199], v[80:83], v[0:7], v216\n"
    "s_waitcnt lgkmcnt(1)\n"
    "ds_read_b128 v[64:67], v217 offset:13312\n"
    "v_exp_f32 v12, v12\n"
    "v_exp_f32 v13, v13\n"
    "v_exp_f32 v14, v14\n"
    "v_exp_f32_e64 v15, v15\n"
    "v_mfma_f32_32x32x16_bf16 v[32:47], v[68:71], v[96:99], 0\n"
    "v_exp_f32 v16, v16\n"
    "v_exp_f32 v17, v17\n"
    "v_exp_f32 v18, v18\n"
    "v_exp_f32 v19, v19\n"
    "v_mfma_f32_32x32x8_bf16 v[32:47], v[74:75], v[104:105], v[32:47]\n"
    "v_exp_f32 v20, v20\n"
    "v_exp_f32 v21, v21\n"
    "v_exp_f32 v22, v22\n"
    "v_exp_f32 v23, v23\n"
    "v_smfmac_f32_32x32x32_bf16 v[184:199], v[88:91], v[8:15], v216\n"
    "v_readfirstlane_b32 s71, v221\n"
    "v_add_u32 v218, s92, v218\n"
    "v_exp_f32 v24, v24\n"
    "v_exp_f32 v25, v25\n"
    "v_exp_f32 v26, v26\n"
    "v_exp_f32 v27, v27\n"
    "v_smfmac_f32_32x32x32_bf16 v[200:215], v[80:83], v[16:23], v216\n"
    "s_bitcmp1_b32 s71, s87\n"
    "s_cbranch_scc0 .Lk_kspin121\n"
    ".Lk_kback122:\n"
    "ds_read_b128 v[80:83], v219 offset:12800\n"
    "v_exp_f32 v28, v28\n"
    "v_exp_f32 v29, v29\n"
    "v_exp_f32 v30, v30\n"
    "v_exp_f32 v31, v31\n"
    "v_mfma_f32_32x32x16_bf16 v[48:63], v[68:71], v[100:103], 0\n"
    "v_exp_f32 v32, v32\n"
    "v_exp_f32 v33, v33\n"
    "v_exp_f32 v34, v34\n"
    "v_exp_f32 v35, v35\n"
    "v_mfma_f32_32x32x8_bf16 v[48:63], v[74:75], v[106:107], v[48:63]\n"
    "v_exp_f32 v36, v36\n"
    "v_exp_f32 v37, v37\n"
    "v_exp_f32 v38, v38\n"
    "v_exp_f32 v39, v39\n"
    "v_smfmac_f32_32x32x32_bf16 v[200:215], v[88:91], v[24:31], v216\n"
    "ds_read_b128 v[88:91], v219 offset:15872\n"
    "v_exp_f32 v40, v40\n"
    "v_exp_f32 v41, v41\n"
    "v_exp_f32 v42, v42\n"
    "v_exp_f32 v43, v43\n"
    "v_smfmac_f32_32x32x32_bf16 v[184:199], v[84:87], v[32:39], v216\n"
    "s_waitcnt lgkmcnt(1)\n"
    "ds_read_b128 v[68:71], v217 offset:14336\n"
    "ds_read_b128 v[72:75], v218 offset:0\n"
    "v_exp_f32 v44, v44\n"
    "v_exp_f32 v45, v45\n"
    "v_exp_f32 v46, v46\n"
    "v_exp_f32_e64 v47, v47\n"
    "v_mfma_f32_32x32x16_bf16 v[0:15], v[64:67], v[96:99], 0\n"
    "v_exp_f32 v48, v48\n"
    "v_exp_f32 v49, v49\n"
    "v_exp_f32 v50, v50\n"
    "v_exp_f32 v51, v51\n"
    "v_mfma_f32_32x32x8_bf16 v[0:15], v[76:77], v[104:105], v[0:15]\n"
    "v_exp_f32 v52, v52\n"
    "v_exp_f32 v53, v53\n"
    "v_exp_f32 v54, v54\n"
    "v_exp_f32 v55, v55\n"
    "v_smfmac_f32_32x32x32_bf16 v[184:199], v[92:95], v[40:47], v216\n"
    "v_add_u32 v217, s92, v217\n"
    "v_exp_f32 v56, v56\n"
    "v_exp_f32 v57, v57\n"
    "v_exp_f32 v58, v58\n"
    "v_exp_f32_e64 v59, v59\n"
    "v_smfmac_f32_32x32x32_bf16 v[200:215], v[84:87], v[48:55], v216\n"
    "ds_read_b128 v[84:87], v219 offset:13568\n"
    "v_exp_f32 v60, v60\n"
    "v_exp_f32 v61, v61\n"
    "v_exp_f32 v62, v62\n"
    "v_exp_f32 v63, v63\n"
    "v_mfma_f32_32x32x16_bf16 v[16:31], v[64:67], v[100:103], 0\n"
    "v_exp_f32 v0, v0\n"
    "v_exp_f32 v1, v1\n"
    "v_exp_f32 v2, v2\n"
    "v_exp_f32 v3, v3\n"
    "v_mfma_f32_32x32x8_bf16 v[16:31], v[76:77], v[106:107], v[16:31]\n"
    "v_exp_f32 v4, v4\n"
    "v_exp_f32 v5, v5\n"
    "v_exp_f32 v6, v6\n"
    "v_exp_f32 v7, v7\n"
    "v_smfmac_f32_32x32x32_bf16 v[200:215], v[92:95], v[56:63], v216\n"
    "ds_read_b128 v[92:95], v219 offset:16640\n"
    "v_exp_f32 v8, v8\n"
    "v_exp_f32 v9, v9\n"
    "v_exp_f32 v10, v10\n"
    "v_exp_f32 v11, v11\n"
    "v_smfmac_f32_32x32x32_bf16 v[184:199], v[80:83], v[0:7], v216\n"
    "s_waitcnt lgkmcnt(1)\n"
    "ds_read_b128 v[64:67], v217 offset:0\n"
    "v_exp_f32 v12, v12\n"
    "v_exp_f32 v13, v13\n"
    "v_exp_f32 v14, v14\n"
    "v_exp_f32_e64 v15, v15\n"
    "v_mfma_f32_32x32x16_bf16 v[32:47], v[68:71], v[96:99], 0\n"
    "v_exp_f32 v16, v16\n"
    "v_exp_f32 v17, v17\n"
    "v_exp_f32 v18, v18\n"
    "v_exp_f32 v19, v19\n"
    "v_mfma_f32_32x32x8_bf16 v[32:47], v[78:79], v[104:105], v[32:47]\n"
    "v_exp_f32 v20, v20\n"
    "v_exp_f32 v21, v21\n"
    "v_exp_f32 v22, v22\n"
    "v_exp_f32 v23, v23\n"
    "v_smfmac_f32_32x32x32_bf16 v[184:199], v[88:91], v[8:15], v216\n"
    "ds_add_u32 v220, v253 offset:128\n"
    "v_add_u32 v219, s92, v219\n"
    "v_exp_f32 v24, v24\n"
    "v_exp_f32 v25, v25\n"
    "v_exp_f32 v26, v26\n"
    "v_exp_f32_e64 v27, v27\n"
    "v_smfmac_f32_32x32x32_bf16 v[200:215], v[80:83], v[16:23], v216\n"
    "ds_read_b128 v[80:83], v219 offset:0\n"
    "v_exp_f32 v28, v28\n"
    "v_exp_f32 v29, v29\n"
    "v_exp_f32 v30, v30\n"
    "v_exp_f32 v31, v31\n"
    "v_mfma_f32_32x32x16_bf16 v[48:63], v[68:71], v[100:103], 0\n"
    "v_exp_f32 v32, v32\n"
    "v_exp_f32 v33, v33\n"
    "v_exp_f32 v34, v34\n"
    "v_exp_f32 v35, v35\n"
    "v_mfma_f32_32x32x8_bf16 v[48:63], v[78:79], v[106:107], v[48:63]\n"
    "v_exp_f32 v36, v36\n"
    "v_exp_f32 v37, v37\n"
    "v_exp_f32 v38, v38\n"
    "v_exp_f32 v39, v39\n"
    "v_smfmac_f32_32x32x32_bf16 v[200:215], v[88:91], v[24:31], v216\n"
    "ds_read_b128 v[88:91], v219 offset:3072\n"
    "v_exp_f32 v40, v40\n"
    "v_exp_f32 v41, v41\n"
    "v_exp_f32 v42, v42\n"
    "v_exp_f32 v43, v43\n"
    "v_smfmac_f32_32x32x32_bf16 v[184:199], v[84:87], v[32:39], v216\n"
    "s_waitcnt lgkmcnt(1)\n"
    "ds_read_b128 v[68:71], v217 offset:1024\n"
    "ds_read_b128 v[76:79], v218 offset:512\n"
    "v_exp_f32 v44, v44\n"
    "v_exp_f32 v45, v45\n"
    "v_exp_f32 v46, v46\n"
    "v_exp_f32_e64 v47, v47\n"
    "v_mfma_f32_32x32x16_bf16 v[0:15], v[64:67], v[96:99], 0\n"
    "v_exp_f32 v48, v48\n"
    "v_exp_f32 v49, v49\n"
    "v_exp_f32 v50, v50\n"
    "v_exp_f32 v51, v51\n"
    "v_mfma_f32_32x32x8_bf16 v[0:15], v[72:73], v[104:105], v[0:15]\n"
    "v_exp_f32 v52, v52\n"
    "v_exp_f32 v53, v53\n"
    "v_exp_f32 v54, v54\n"
    "v_exp_f32 v55, v55\n"
    "v_smfmac_f32_32x32x32_bf16 v[184:199], v[92:95], v[40:47], v216\n"
    "s_cmp_lg_u32 s87, 7\n"
    "v_exp_f32 v56, v56\n"
    "v_exp_f32 v57, v57\n"
    "v_exp_f32 v58, v58\n"
    "v_exp_f32_e64 v59, v59\n"
    "v_smfmac_f32_32x32x32_bf16 v[200:215], v[84:87], v[48:55], v216\n"
    "ds_read_b128 v[84:87], v219 offset:768\n"
    "v_exp_f32 v60, v60\n"
    "v_exp_f32 v61, v61\n"
    "v_exp_f32 v62, v62\n"
    "v_exp_f32 v63, v63\n"
    "s_cbranch_scc1 .Lk_att120\n"
    "v_readfirstlane_b32 s71, v221\n"
    "s_andn2_b32 s70, 256, s71\n"
    "s_cmp_eq_u32 s70, 0\n"
    "s_cbranch_scc0 .Lk_w123\n"
    ".Lk_d124:\n"
    "s_and_b32 s71, s71, 8192\n"
    "s_or_b32 s8, s8, s71\n"
    "v_mfma_f32_32x32x16_bf16 v[16:31], v[64:67], v[100:103], 0\n"
    "v_exp_f32 v0, v0\n"
    "v_exp_f32 v1, v1\n"
    "v_exp_f32 v2, v2\n"
    "v_exp_f32_e64 v3, v3\n"
    "v_mfma_f32_32x32x8_bf16 v[16:31], v[72:73], v[106:107], v[16:31]\n"
    "v_exp_f32 v4, v4\n"
    "v_exp_f32 v5, v5\n"
    "v_exp_f32 v6, v6\n"
    "v_exp_f32 v7, v7\n"
    "v_smfmac_f32_32x32x32_bf16 v[200:215], v[92:95], v[56:63], v216\n"
    "ds_read_b128 v[92:95], v219 offset:3840\n"
    "buffer_load_dwordx4 v[108:111], v255, s[4:7], s21 offen sc1\n"
    "v_exp_f32 v8, v8\n"
    "v_exp_f32 v9, v9\n"
    "v_exp_f32 v10, v10\n"
    "v_exp_f32 v11, v11\n"
    "v_smfmac_f32_32x32x32_bf16 v[184:199], v[80:83], v[0:7], v216\n"
    "s_waitcnt lgkmcnt(1)\n"
    "ds_read_b128 v[64:67], v217 offset:2048\n"
    "v_exp_f32 v12, v12\n"
    "v_exp_f32 v13, v13\n"
    "v_exp_f32 v14, v14\n"
    "v_exp_f32_e64 v15, v15\n"
    "v_mfma_f32_32x32x16_bf16 v[32:47], v[68:71], v[96:99], 0\n"
    "v_exp_f32 v16, v16\n"
    "v_exp_f32 v17, v17\n"
    "v_exp_f32 v18, v18\n"
    "v_exp_f32 v19, v19\n"
    "v_mfma_f32_32x32x8_bf16 v[32:47], v[74:75], v[104:105], v[32:47]\n"
    "v_exp_f32 v20, v20\n"
    "v_exp_f32 v21, v21\n"
    "v_exp_f32 v22, v22\n"
    "v_exp_f32 v23, v23\n"
    "v_smfmac_f32_32x32x32_bf16 v[184:199], v[88:91], v[8:15], v216\n"
    "buffer_load_dwordx4 v[112:115], v255, s[4:7], s22 offen sc1\n"
    "v_exp_f32 v24, v24\n"
    "v_exp_f32 v25, v25\n"
    "v_exp_f32 v26, v26\n"
    "v_exp_f32 v27, v27\n"
    "v_smfmac_f32_32x32x32_bf16 v[200:215], v[80:83], v[16:23], v216\n"
    "ds_read_b128 v[80:83], v219 offset:1536\n"
    "buffer_load_dwordx4 v[116:119], v255, s[4:7], s27 offen sc1\n"
    "v_exp_f32 v28, v28\n"
    "v_exp_f32 v29, v29\n"
    "v_exp_f32 v30, v30\n"
    "v_exp_f32 v31, v31\n"
    "v_mfma_f32_32x32x16_bf16 v[48:63], v[68:71], v[100:103], 0\n"
    "v_exp_f32 v32, v32\n"
    "v_exp_f32 v33, v33\n"
    "v_exp_f32 v34, v34\n"
    "v_exp_f32 v35, v35\n"
    "v_mfma_f32_32x32x8_bf16 v[48:63], v[74:75], v[106:107], v[48:63]\n"
    "v_exp_f32 v36, v36\n"
    "v_exp_f32 v37, v37\n"
    "v_exp_f32 v38, v38\n"
    "v_exp_f32 v39, v39\n"
    "v_smfmac_f32_32x32x32_bf16 v[200:215], v[88:91], v[24:31], v216\n"
    "ds_read_b128 v[88:91], v219 offset:4608\n"
    "buffer_load_dwordx4 v[120:123], v255, s[4:7], s21 offen offset:2048 sc1\n"
    "v_exp_f32 v40, v40\n"
    "v_exp_f32 v41, v41\n"
    "v_exp_f32 v42, v42\n"
    "v_exp_f32 v43, v43\n"
    "v_smfmac_f32_32x32x32_bf16 v[184:199], v[84:87], v[32:39], v216\n"
    "s_add_u32 s21, s21, 4096\n"
    "s_waitcnt lgkmcnt(1)\n"
    "ds_read_b128 v[68:71], v217 offset:3072\n"
    "ds_read_b128 v[72:75], v218 offset:11264\n"
    "v_exp_f32 v44, v44\n"
    "v_exp_f32 v45, v45\n"
    "v_exp_f32 v46, v46\n"
    "v_exp_f32_e64 v47, v47\n"
    "v_mfma_f32_32x32x16_bf16 v[0:15], v[64:67], v[96:99], 0\n"
    "v_exp_f32 v48, v48\n"
    "v_exp_f32 v49, v49\n"
    "v_exp_f32 v50, v50\n"
    "v_exp_f32 v51, v51\n"
    "v_mfma_f32_32x32x8_bf16 v[0:15], v[76:77], v[104:105], v[0:15]\n"
    "v_exp_f32 v52, v52\n"
    "v_exp_f32 v53, v53\n"
    "v_exp_f32 v54, v54\n"
    "v_exp_f32 v55, v55\n"
    "v_smfmac_f32_32x32x32_bf16 v[184:199], v[92:95], v[40:47], v216\n"
    "buffer_load_dwordx4 v[124:127], v255, s[4:7], s22 offen offset:2048 sc1\n"
    "v_exp_f32 v56, v56\n"
    "v_exp_f32 v57, v57\n"
    "v_exp_f32 v58, v58\n"
    "v_exp_f32 v59, v59\n"
    "v_smfmac_f32_32x32x32_bf16 v[200:215], v[84:87], v[48:55], v216\n"
    "s_add_u32 s22, s22, 4096\n"
    "ds_read_b128 v[84:87], v219 offset:2304\n"
    "buffer_load_dwordx4 v[128:131], v255, s[4:7], s27 offen offset:2048 sc1\n"
    "v_exp_f32 v60, v60\n"
    "v_exp_f32 v61, v61\n"
    "v_exp_f32 v62, v62\n"
    "v_exp_f32 v63, v63\n"
    "v_mfma_f32_32x32x16_bf16 v[16:31], v[64:67], v[100:103], 0\n"
    "v_exp_f32 v0, v0\n"
    "v_exp_f32 v1, v1\n"
    "v_exp_f32 v2, v2\n"
    "v_exp_f32 v3, v3\n"
    "v_mfma_f32_32x32x8_bf16 v[16:31], v[76:77], v[106:107], v[16:31]\n"
    "v_exp_f32 v4, v4\n"
    "v_exp_f32 v5, v5\n"
    "v_exp_f32 v6, v6\n"
    "v_exp_f32 v7, v7\n"
    "v_smfmac_f32_32x32x32_bf16 v[200:215], v[92:95], v[56:63], v216\n"
    "s_add_u32 s27, s27, 4096\n"
    "ds_read_b128 v[92:95], v219 offset:5376\n"
    "buffer_load_dwordx4 v[132:135], v255, s[4:7], s21 offen sc1\n"
    "v_exp_f32 v8, v8\n"
    "v_exp_f32 v9, v9\n"
    "v_exp_f32 v10, v10\n"
    "v_exp_f32 v11, v11\n"
    "v_smfmac_f32_32x32x32_bf16 v[184:199], v[80:83], v[0:7], v216\n"
    "s_waitcnt lgkmcnt(1)\n"
    "ds_read_b128 v[64:67], v217 offset:11264\n"
    "v_exp_f32 v12, v12\n"
    "v_exp_f32 v13, v13\n"
    "v_exp_f32 v14, v14\n"
    "v_exp_f32_e64 v15, v15\n"
    "v_mfma_f32_32x32x16_bf16 v[32:47], v[68:71], v[96:99], 0\n"
    "v_exp_f32 v16, v16\n"
    "v_exp_f32 v17, v17\n"
    "v_exp_f32 v18, v18\n"
    "v_exp_f32 v19, v19\n"
    "v_mfma_f32_32x32x8_bf16 v[32:47], v[78:79], v[104:105], v[32:47]\n"
    "v_exp_f32 v20, v20\n"
    "v_exp_f32 v21, v21\n"
    "v_exp_f32 v22, v22\n"
    "v_exp_f32 v23, v23\n"
    "v_smfmac_f32_32x32x32_bf16 v[184:199], v[88:91], v[8:15], v216\n"
    "buffer_load_dwordx4 v[136:139], v255, s[4:7], s22 offen sc1\n"
    "v_exp_f32 v24, v24\n"
    "v_exp_f32 v25, v25\n"
    "v_exp_f32 v26, v26\n"
    "v_exp_f32 v27, v27\n"
    "v_smfmac_f32_32x32x32_bf16 v[200:215], v[80:83], v[16:23], v216\n"
    "ds_read_b128 v[80:83], v219 offset:11264\n"
    "buffer_load_dwordx4 v[140:143], v255, s[4:7], s27 offen sc1\n"
    "v_exp_f32 v28, v28\n"
    "v_exp_f32 v29, v29\n"
    "v_exp_f32 v30, v30\n"
    "v_exp_f32 v31, v31\n"
    "v_mfma_f32_32x32x16_bf16 v[48:63], v[68:71], v[100:103], 0\n"
    "v_exp_f32 v32, v32\n"
    "v_exp_f32 v33, v33\n"
    "v_exp_f32 v34, v34\n"
    "v_exp_f32 v35, v35\n"
    "v_mfma_f32_32x32x8_bf16 v[48:63], v[78:79], v[106:107], v[48:63]\n"
    "v_exp_f32 v36, v36\n"
    "v_exp_f32 v37, v37\n"
    "v_exp_f32 v38, v38\n"
    "v_exp_f32 v39, v39\n"
    "v_smfmac_f32_32x32x32_bf16 v[200:215], v[88:91], v[24:31], v216\n"
    "ds_read_b128 v[88:91], v219 offset:14336\n"
    "buffer_load_dwordx4 v[144:147], v255, s[4:7], s21 offen offset:2048 sc1\n"
    "v_exp_f32 v40, v40\n"
    "v_exp_f32 v41, v41\n"
    "v_exp_f32 v42, v42\n"
    "v_exp_f32 v43, v43\n"
    "v_smfmac_f32_32x32x32_bf16 v[184:199], v[84:87], v[32:39], v216\n"
    "s_add_u32 s21, s21, 4096\n"
    "s_waitcnt lgkmcnt(1)\n"
    "ds_read_b128 v[68:71], v217 offset:12288\n"
    "ds_read_b128 v[76:79], v218 offset:11776\n"
    "v_exp_f32 v44, v44\n"
    "v_exp_f32 v45, v45\n"
    "v_exp_f32 v46, v46\n"
    "v_exp_f32_e64 v47, v47\n"
    "v_mfma_f32_32x32x16_bf16 v[0:15], v[64:67], v[96:99], 0\n"
    "v_exp_f32 v48, v48\n"
    "v_exp_f32 v49, v49\n"
    "v_exp_f32 v50, v50\n"
    "v_exp_f32 v51, v51\n"
    "v_mfma_f32_32x32x8_bf16 v[0:15], v[72:73], v[104:105], v[0:15]\n"
    "v_exp_f32 v52, v52\n"
    "v_exp_f32 v53, v53\n"
    "v_exp_f32 v54, v54\n"
    "v_exp_f32 v55, v55\n"
    "v_smfmac_f32_32x32x32_bf16 v[184:199], v[92:95], v[40:47], v216\n"
    "buffer_load_dwordx4 v[148:151], v255, s[4:7], s22 offen offset:2048 sc1\n"
    "v_exp_f32 v56, v56\n"
    "v_exp_f32 v57, v57\n"
    "v_exp_f32 v58, v58\n"
    "v_exp_f32 v59, v59\n"
    "v_smfmac_f32_32x32x32_bf16 v[200:215], v[84:87], v[48:55], v216\n"
    "s_add_u32 s22, s22, 4096\n"
    "ds_read_b128 v[84:87], v219 offset:12032\n"
    "buffer_load_dwordx4 v[152:155], v255, s[4:7], s27 offen offset:2048 sc1\n"
    "v_exp_f32 v60, v60\n"
    "v_exp_f32 v61, v61\n"
    "v_exp_f32 v62, v62\n"
    "v_exp_f32 v63, v63\n"
    "v_mfma_f32_32x32x16_bf16 v[16:31], v[64:67], v[100:103], 0\n"
    "v_exp_f32 v0, v0\n"
    "v_exp_f32 v1, v1\n"
    "v_exp_f32 v2, v2\n"
    "v_exp_f32 v3, v3\n"
    "v_mfma_f32_32x32x8_bf16 v[16:31], v[72:73], v[106:107], v[16:31]\n"
    "v_exp_f32 v4, v4\n"
    "v_exp_f32 v5, v5\n"
    "v_exp_f32 v6, v6\n"
    "v_exp_f32 v7, v7\n"
    "v_smfmac_f32_32x32x32_bf16 v[200:215], v[92:95], v[56:63], v216\n"
    "s_add_u32 s27, s27, 4096\n"
    "ds_read_b128 v[92:95], v219 offset:15104\n"
    "buffer_load_dwordx4 v[156:159], v255, s[4:7], s21 offen sc1\n"
    "v_exp_f32 v8, v8\n"
    "v_exp_f32 v9, v9\n"
    "v_exp_f32 v10, v10\n"
    "v_exp_f32 v11, v11\n"
    "v_smfmac_f32_32x32x32_bf16 v[184:199], v[80:83], v[0:7], v216\n"
    "s_waitcnt lgkmcnt(1)\n"
    "ds_read_b128 v[64:67], v217 offset:13312\n"
    "v_exp_f32 v12, v12\n"
    "v_exp_f32 v13, v13\n"
    "v_exp_f32 v14, v14\n"
    "v_exp_f32_e64 v15, v15\n"
    "v_mfma_f32_32x32x16_bf16 v[32:47], v[68:71], v[96:99], 0\n"
    "v_exp_f32 v16, v16\n"
    "v_exp_f32 v17, v17\n"
    "v_exp_f32 v18, v18\n"
    "v_exp_f32 v19, v19\n"
    "v_mfma_f32_32x32x8_bf16 v[32:47], v[74:75], v[104:105], v[32:47]\n"
    "v_exp_f32 v20, v20\n"
    "v_exp_f32 v21, v21\n"
    "v_exp_f32 v22, v22\n"
    "v_exp_f32 v23, v23\n"
    "v_smfmac_f32_32x32x32_bf16 v[184:199], v[88:91], v[8:15], v216\n"
    "buffer_load_dwordx4 v[160:163], v255, s[4:7], s22 offen sc1\n"
    "v_exp_f32 v24, v24\n"
    "v_exp_f32 v25, v25\n"
    "v_exp_f32 v26, v26\n"
    "v_exp_f32 v27, v27\n"
    "v_smfmac_f32_32x32x32_bf16 v[200:215], v[80:83], v[16:23], v216\n"
    "ds_read_b128 v[80:83], v219 offset:12800\n"
    "buffer_load_dwordx4 v[164:167], v255, s[4:7], s27 offen sc1\n"
    "v_exp_f32 v28, v28\n"
    "v_exp_f32 v29, v29\n"
    "v_exp_f32 v30, v30\n"
    "v_exp_f32 v31, v31\n"
    "v_mfma_f32_32x32x16_bf16 v[48:63], v[68:71], v[100:103], 0\n"
    "v_exp_f32 v32, v32\n"
    "v_exp_f32 v33, v33\n"
    "v_exp_f32 v34, v34\n"
    "v_exp_f32 v35, v35\n"
    "v_mfma_f32_32x32x8_bf16 v[48:63], v[74:75], v[106:107], v[48:63]\n"
    "v_exp_f32 v36, v36\n"
    "v_exp_f32 v37, v37\n"
    "v_exp_f32 v38, v38\n"
    "v_exp_f32 v39, v39\n"
    "v_smfmac_f32_32x32x32_bf16 v[200:215], v[88:91], v[24:31], v216\n"
    "ds_read_b128 v[88:91], v219 offset:15872\n"
    "buffer_load_dwordx4 v[168:171], v255, s[4:7], s21 offen offset:2048 sc1\n"
    "v_exp_f32 v40, v40\n"
    "v_exp_f32 v41, v41\n"
    "v_exp_f32 v42, v42\n"
    "v_exp_f32 v43, v43\n"
    "v_smfmac_f32_32x32x32_bf16 v[184:199], v[84:87], v[32:39], v216\n"
    "s_add_u32 s21, s21, 4096\n"
    "s_waitcnt lgkmcnt(1)\n"
    "ds_read_b128 v[68:71], v217 offset:14336\n"
    "v_exp_f32 v44, v44\n"
    "v_exp_f32 v45, v45\n"
    "v_exp_f32 v46, v46\n"
    "v_exp_f32_e64 v47, v47\n"
    "v_mfma_f32_32x32x16_bf16 v[0:15], v[64:67], v[96:99], 0\n"
    "v_exp_f32 v48, v48\n"
    "v_exp_f32 v49, v49\n"
    "v_exp_f32 v50, v50\n"
    "v_exp_f32 v51, v51\n"
    "v_mfma_f32_32x32x8_bf16 v[0:15], v[76:77], v[104:105], v[0:15]\n"
    "v_exp_f32 v52, v52\n"
    "v_exp_f32 v53, v53\n"
    "v_exp_f32 v54, v54\n"
    "v_exp_f32 v55, v55\n"
    "v_smfmac_f32_32x32x32_bf16 v[184:199], v[92:95], v[40:47], v216\n"
    "buffer_load_dwordx4 v[172:175], v255, s[4:7], s22 offen offset:2048 sc1\n"
    "v_exp_f32 v56, v56\n"
    "v_exp_f32 v57, v57\n"
    "v_exp_f32 v58, v58\n"
    "v_exp_f32 v59, v59\n"
    "v_smfmac_f32_32x32x32_bf16 v[200:215], v[84:87], v[48:55], v216\n"
    "s_add_u32 s22, s22, 4096\n"
    "ds_read_b128 v[84:87], v219 offset:13568\n"
    "buffer_load_dwordx4 v[176:179], v255, s[4:7], s27 offen offset:2048 sc1\n"
    "v_exp_f32 v60, v60\n"
    "v_exp_f32 v61, v61\n"
    "v_exp_f32 v62, v62\n"
    "v_exp_f32 v63, v63\n"
    "v_mfma_f32_32x32x16_bf16 v[16:31], v[64:67], v[100:103], 0\n"
    "v_exp_f32 v0, v0\n"
    "v_exp_f32 v1, v1\n"
    "v_exp_f32 v2, v2\n"
    "v_exp_f32 v3, v3\n"
    "v_mfma_f32_32x32x8_bf16 v[16:31], v[76:77], v[106:107], v[16:31]\n"
    "v_exp_f32 v4, v4\n"
    "v_exp_f32 v5, v5\n"
    "v_exp_f32 v6, v6\n"
    "v_exp_f32 v7, v7\n"
    "v_smfmac_f32_32x32x32_bf16 v[200:215], v[92:95], v[56:63], v216\n"
    "s_add_u32 s27, s27, 4096\n"
    "ds_read_b128 v[92:95], v219 offset:16640\n"
    "buffer_load_dwordx4 v[180:183], v255, s[4:7], s21 offen sc1\n"
    "v_exp_f32 v8, v8\n"
    "v_exp_f32 v9, v9\n"
    "v_exp_f32 v10, v10\n"
    "v_exp_f32 v11, v11\n"
    "v_smfmac_f32_32x32x32_bf16 v[184:199], v[80:83], v[0:7], v216\n"
    "s_waitcnt lgkmcnt(1)\n"
    "v_exp_f32 v12, v12\n"
    "v_exp_f32 v13, v13\n"
    "v_exp_f32 v14, v14\n"
    "v_exp_f32_e64 v15, v15\n"
    "v_mfma_f32_32x32x16_bf16 v[32:47], v[68:71], v[96:99], 0\n"
    "v_exp_f32 v16, v16\n"
    "v_exp_f32 v17, v17\n"
    "v_exp_f32 v18, v18\n"
    "v_exp_f32 v19, v19\n"
    "v_mfma_f32_32x32x8_bf16 v[32:47], v[78:79], v[104:105], v[32:47]\n"
    "v_exp_f32 v20, v20\n"
    "v_exp_f32 v21, v21\n"
    "v_exp_f32 v22, v22\n"
    "v_exp_f32 v23, v23\n"
    "v_smfmac_f32_32x32x32_bf16 v[184:199], v[88:91], v[8:15], v216\n"
    "ds_add_u32 v220, v253 offset:128\n"
    "v_exp_f32 v24, v24\n"
    "v_exp_f32 v25, v25\n"
    "v_exp_f32 v26, v26\n"
    "v_exp_f32 v27, v27\n"
    "v_smfmac_f32_32x32x32_bf16 v[200:215], v[80:83], v[16:23], v216\n"
    "v_exp_f32 v28, v28\n"
    "v_exp_f32 v29, v29\n"
    "v_exp_f32 v30, v30\n"
    "v_exp_f32 v31, v31\n"
    "v_mfma_f32_32x32x16_bf16 v[48:63], v[68:71], v[100:103], 0\n"
    "v_exp_f32 v32, v32\n"
    "v_exp_f32 v33, v33\n"
    "v_exp_f32 v34, v34\n"
    "v_exp_f32 v35, v35\n"
    "v_mfma_f32_32x32x8_bf16 v[48:63], v[78:79], v[106:107], v[48:63]\n"
    "v_exp_f32 v36, v36\n"
    "v_exp_f32 v37, v37\n"
    "v_exp_f32 v38, v38\n"
    "v_exp_f32 v39, v39\n"
    "v_smfmac_f32_32x32x32_bf16 v[200:215], v[88:91], v[24:31], v216\n"
    "v_exp_f32 v40, v40\n"
    "v_exp_f32 v41, v41\n"
    "v_exp_f32 v42, v42\n"
    "v_exp_f32 v43, v43\n"
    "v_smfmac_f32_32x32x32_bf16 v[184:199], v[84:87], v[32:39], v216\n"
    "s_waitcnt lgkmcnt(0)\n"
    "v_exp_f32 v44, v44\n"
    "v_exp_f32 v45, v45\n"
    "v_exp_f32 v46, v46\n"
    "v_exp_f32 v47, v47\n"
    "v_exp_f32 v48, v48\n"
    "v_exp_f32 v49, v49\n"
    "v_exp_f32 v50, v50\n"
    "v_exp_f32 v51, v51\n"
    "v_exp_f32 v52, v52\n"
    "v_exp_f32 v53, v53\n"
    "v_exp_f32 v54, v54\n"
    "v_exp_f32_e64 v55, v55\n"
    "v_smfmac_f32_32x32x32_bf16 v[184:199], v[92:95], v[40:47], v216\n"
    "v_exp_f32 v56, v56\n"
    "v_exp_f32 v57, v57\n"
    "v_exp_f32 v58, v58\n"
    "v_exp_f32 v59, v59\n"
    "v_smfmac_f32_32x32x32_bf16 v[200:215], v[84:87], v[48:55], v216\n"
    "v_exp_f32 v60, v60\n"
    "v_exp_f32 v61, v61\n"
    "v_exp_f32 v62, v62\n"
    "v_exp_f32 v63, v63\n"
    "s_nop 1\n"
    "v_smfmac_f32_32x32x32_bf16 v[200:215], v[92:95], v[56:63], v216\n"
    "s_cmp_lg_u32 s86, 0\n"
    "s_cbranch_scc1 .Lk_slowp125\n"
    ".Lk_joinp126:\n"
    "v_rcp_f32 v32, v194\n"
    "s_nop 0\n"
    "s_nop 0\n"
    "v_pk_mul_f32 v[184:185], v[32:33], v[184:185] op_sel_hi:[0,1]\n"
    "v_pk_mul_f32 v[186:187], v[32:33], v[186:187] op_sel_hi:[0,1]\n"
    "v_pk_mul_f32 v[188:189], v[32:33], v[188:189] op_sel_hi:[0,1]\n"
    "v_pk_mul_f32 v[190:191], v[32:33], v[190:191] op_sel_hi:[0,1]\n"
    "v_pk_mul_f32 v[192:193], v[32:33], v[192:193] op_sel_hi:[0,1]\n"
    "v_cvt_pk_bf16_f32 v36, v184, v185\n"
    "v_cvt_pk_bf16_f32 v37, v186, v187\n"
    "v_cvt_pk_bf16_f32 v38, v188, v189\n"
    "v_cvt_pk_bf16_f32 v39, v190, v191\n"
    "v_cvt_pk_bf16_f32 v40, v192, v193\n"
    "buffer_store_dwordx4 v[36:39], v232, s[8:11], 0 offen sc1\n"
    "v_rcp_f32 v34, v210\n"
    "s_nop 0\n"
    "v_pk_mul_f32 v[200:201], v[34:35], v[200:201] op_sel_hi:[0,1]\n"
    "v_pk_mul_f32 v[202:203], v[34:35], v[202:203] op_sel_hi:[0,1]\n"
    "v_pk_mul_f32 v[204:205], v[34:35], v[204:205] op_sel_hi:[0,1]\n"
    "v_pk_mul_f32 v[206:207], v[34:35], v[206:207] op_sel_hi:[0,1]\n"
    "v_pk_mul_f32 v[208:209], v[34:35], v[208:209] op_sel_hi:[0,1]\n"
    "v_cvt_pk_bf16_f32 v41, v200, v201\n"
    "v_cvt_pk_bf16_f32 v42, v202, v203\n"
    "v_cvt_pk_bf16_f32 v43, v204, v205\n"
    "v_cvt_pk_bf16_f32 v44, v206, v207\n"
    "v_cvt_pk_bf16_f32 v45, v208, v209\n"
    "buffer_store_dwordx4 v[40:43], v232, s[8:11], 0 offen offset:1024 sc1\n"
    "v_permlane16_swap_b32 v44, v46\n"
    "v_permlane16_swap_b32 v45, v47\n"
    "buffer_store_dwordx4 v[44:47], v233, s[8:11], 0 offen sc1\n"
    "v_lshlrev_b32_e64 v56, 16, v222\n"
    "v_and_b32 v57, 0xffff0000, v222\n"
    "v_lshlrev_b32_e64 v58, 16, v223\n"
    "v_and_b32 v59, 0xffff0000, v223\n"
    "v_lshlrev_b32_e64 v60, 16, v224\n"
    "v_and_b32 v61, 0xffff0000, v224\n"
    "v_lshlrev_b32_e64 v62, 16, v225\n"
    "v_and_b32 v63, 0xffff0000, v225\n"
    "v_lshlrev_b32_e64 v64, 16, v226\n"
    "v_and_b32 v65, 0xffff0000, v226\n"
    "v_lshlrev_b32_e64 v66, 16, v227\n"
    "v_and_b32 v67, 0xffff0000, v227\n"
    "v_lshlrev_b32_e64 v68, 16, v228\n"
    "v_and_b32 v69, 0xffff0000, v228\n"
    "v_lshlrev_b32_e64 v70, 16, v229\n"
    "v_and_b32 v71, 0xffff0000, v229\n"
    "v_lshlrev_b32_e64 v72, 16, v230\n"
    "v_and_b32 v73, 0xffff0000, v230\n"
    "v_lshlrev_b32_e64 v74, 16, v231\n"
    "v_and_b32 v75, 0xffff0000, v231\n"
    "s_barrier\n"
    "s_nop 0\n"
    "ds_read_b128 v[76:79], v255\n"
    "ds_read_b128 v[80:83], v255 offset:1024\n"
    "ds_read_b128 v[84:87], v255 offset:2048\n"
    "ds_read_b128 v[88:91], v255 offset:3072\n"
    "ds_read_b128 v[92:95], v255 offset:4096\n"
    "ds_read_b128 v[96:99], v255 offset:5120\n"
    "ds_read_b128 v[100:103], v255 offset:6144\n"
    "ds_read_b128 v[104:107], v255 offset:7168\n"
    "buffer_load_dwordx4 v[184:187], v255, s[4:7], s22 offen sc1\n"
    "buffer_load_dwordx4 v[188:191], v255, s[4:7], s27 offen sc1\n"
    "buffer_load_dwordx4 v[192:195], v255, s[4:7], s21 offen offset:2048 sc1\n"
    "s_add_u32 s21, s21, 4096\n"
    "s_waitcnt vmcnt(3)\n"
    "s_waitcnt lgkmcnt(0)\n"
    "s_barrier\n"
    "buffer_load_dwordx4 v[196:199], v255, s[4:7], s22 offen offset:2048 sc1\n"
    "s_add_u32 s22, s22, 4096\n"
    "buffer_load_dwordx4 v[200:203], v255, s[4:7], s27 offen offset:2048 sc1\n"
    "s_add_u32 s27, s27, 4096\n"
    "buffer_load_dwordx4 v[204:207], v255, s[4:7], s21 offen sc1\n"
    "buffer_load_dwordx4 v[208:211], v255, s[4:7], s22 offen sc1\n"
    "buffer_load_dwordx4 v[212:215], v255, s[4:7], s27 offen sc1\n"
    "buffer_load_dwordx4 v[216:219], v255, s[4:7], s21 offen offset:2048 sc1\n"
    "s_add_u32 s21, s21, 4096\n"
    "s_barrier\n"
    "buffer_load_dwordx4 v[0:3], v255, s[8:11], s20 offen sc1\n"
    "buffer_load_dwordx4 v[4:7], v255, s[8:11], s20 offen offset:1024 sc1\n"
    "buffer_load_dwordx4 v[8:11], v255, s[8:11], s20 offen offset:2048 sc1\n"
    "buffer_load_dwordx4 v[12:15], v255, s[8:11], s20 offen offset:3072 sc1\n"
    "s_add_u32 s20, s20, 4096\n"
    "buffer_load_dwordx4 v[16:19], v255, s[8:11], s20 offen sc1\n"
    "s_add_u32 s20, s20, 15360\n"
    "buffer_load_dwordx4 v[20:23], v255, s[8:11], s20 offen sc1\n"
    "buffer_load_dwordx4 v[24:27], v255, s[8:11], s20 offen offset:1024 sc1\n"
    "buffer_load_dwordx4 v[28:31], v255, s[8:11], s20 offen offset:2048 sc1\n"
    "s_waitcnt vmcnt(7)\n"
    "s_nop 0\n"
    "v_mfma_f32_16x16x32_bf16 v[56:59], v[76:79], v[0:3], v[56:59]\n"
    "buffer_load_dwordx4 v[32:35], v255, s[8:11], s20 offen offset:3072 sc1\n"
    "s_add_u32 s20, s20, 4096\n"
    "ds_read_b128 v[76:79], v255 offset:8192\n"
    "v_mfma_f32_16x16x32_bf16 v[60:63], v[80:83], v[0:3], v[60:63]\n"
    "ds_read_b128 v[80:83], v255 offset:9216\n"
    "v_mfma_f32_16x16x32_bf16 v[64:67], v[84:87], v[0:3], v[64:67]\n"
    "ds_read_b128 v[84:87], v255 offset:10240\n"
    "v_mfma_f32_16x16x32_bf16 v[68:71], v[88:91], v[0:3], v[68:71]\n"
    "ds_read_b128 v[88:91], v255 offset:11264\n"
    "v_mfma_f32_16x16x32_bf16 v[72:75], v[92:95], v[0:3], v[72:75]\n"
    "buffer_load_dwordx4 v[36:39], v255, s[8:11], s20 offen sc1\n"
    "s_add_u32 s20, s20, 9216\n"
    "ds_read_b128 v[92:95], v255 offset:12288\n"
    "s_waitcnt vmcnt(8)\n"
    "v_mfma_f32_16x16x32_bf16 v[56:59], v[96:99], v[4:7], v[56:59]\n"
    "ds_read_b128 v[96:99], v255 offset:13312\n"
    "v_mfma_f32_16x16x32_bf16 v[60:63], v[100:103], v[4:7], v[60:63]\n"
    "ds_read_b128 v[100:103], v255 offset:14336\n"
    "v_mfma_f32_16x16x32_bf16 v[64:67], v[104:107], v[4:7], v[64:67]\n"
    "ds_read_b128 v[104:107], v255 offset:15360\n"
    "s_waitcnt lgkmcnt(7)\n"
    "v_mfma_f32_16x16x32_bf16 v[68:71], v[76:79], v[4:7], v[68:71]\n"
    "buffer_load_dwordx4 v[40:43], v255, s[8:11], s20 offen sc1\n"
    "ds_read_b128 v[76:79], v255 offset:16384\n"
    "s_waitcnt lgkmcnt(7)\n"
    "v_mfma_f32_16x16x32_bf16 v[72:75], v[80:83], v[4:7], v[72:75]\n"
    "ds_read_b128 v[80:83], v255 offset:17408\n"
    "s_waitcnt vmcnt(8) lgkmcnt(7)\n"
    "v_mfma_f32_16x16x32_bf16 v[56:59], v[84:87], v[8:11], v[56:59]\n"
    "ds_read_b128 v[84:87], v255 offset:18432\n"
    "s_waitcnt lgkmcnt(7)\n"
    "v_mfma_f32_16x16x32_bf16 v[60:63], v[88:91], v[8:11], v[60:63]\n"
    "ds_read_b128 v[88:91], v255 offset:19456\n"
    "s_waitcnt lgkmcnt(7)\n"
    "v_mfma_f32_16x16x32_bf16 v[64:67], v[92:95], v[8:11], v[64:67]\n"
    "buffer_load_dwordx4 v[44:47], v255, s[8:11], s20 offen offset:1024 sc1\n"
    "ds_read_b128 v[92:95], v255 offset:20480\n"
    "s_waitcnt lgkmcnt(7)\n"
    "v_mfma_f32_16x16x32_bf16 v[68:71], v[96:99], v[8:11], v[68:71]\n"
    "ds_read_b128 v[96:99], v255 offset:21504\n"
    "s_waitcnt lgkmcnt(7)\n"
    "v_mfma_f32_16x16x32_bf16 v[72:75], v[100:103], v[8:11], v[72:75]\n"
    "ds_read_b128 v[100:103], v255 offset:22528\n"
    "s_waitcnt vmcnt(8) lgkmcnt(7)\n"
    "v_mfma_f32_16x16x32_bf16 v[56:59], v[104:107], v[12:15], v[56:59]\n"
    "ds_read_b128 v[104:107], v255 offset:23552\n"
    "s_waitcnt lgkmcnt(7)\n"
    "v_mfma_f32_16x16x32_bf16 v[60:63], v[76:79], v[12:15], v[60:63]\n"
    "buffer_load_dwordx4 v[48:51], v255, s[8:11], s20 offen offset:2048 sc1\n"
    "ds_read_b128 v[76:79], v255 offset:24576\n"
    "s_waitcnt lgkmcnt(7)\n"
    "v_mfma_f32_16x16x32_bf16 v[64:67], v[80:83], v[12:15], v[64:67]\n"
    "ds_read_b128 v[80:83], v255 offset:25600\n"
    "s_waitcnt lgkmcnt(7)\n"
    ".Lk_pad8:\n"
    "s_cbranch_execz .Lk_tramp\n"
    "v_mfma_f32_16x16x32_bf16 v[68:71], v[84:87], v[12:15], v[68:71]\n"
    "ds_read_b128 v[84:87], v255 offset:26624\n"
    "s_waitcnt lgkmcnt(7)\n"
    "v_mfma_f32_16x16x32_bf16 v[72:75], v[88:91], v[12:15], v[72:75]\n"
    "ds_read_b128 v[88:91], v255 offset:27648\n"
    "s_waitcnt vmcnt(8) lgkmcnt(7)\n"
    "v_mfma_f32_16x16x32_bf16 v[56:59], v[92:95], v[16:19], v[56:59]\n"
    "buffer_load_dwordx4 v[52:55], v255, s[8:11], s20 offen offset:3072 sc1\n"
    "s_add_u32 s20, s20, 4096\n"
    "ds_read_b128 v[92:95], v255 offset:28672\n"
    "s_waitcnt lgkmcnt(7)\n"
    "v_mfma_f32_16x16x32_bf16 v[60:63], v[96:99], v[16:19], v[60:63]\n"
    "ds_read_b128 v[96:99], v255 offset:29696\n"
    "s_waitcnt lgkmcnt(7)\n"
    "v_mfma_f32_16x16x32_bf16 v[64:67], v[100:103], v[16:19], v[64:67]\n"
    "ds_read_b128 v[100:103], v255 offset:30720\n"
    "s_waitcnt lgkmcnt(7)\n"
    "v_mfma_f32_16x16x32_bf16 v[68:71], v[104:107], v[16:19], v[68:71]\n"
    "ds_read_b128 v[104:107], v255 offset:31744\n"
    "s_waitcnt lgkmcnt(7)\n"
    "v_mfma_f32_16x16x32_bf16 v[72:75], v[76:79], v[16:19], v[72:75]\n"
    "buffer_load_dwordx4 v[0:3], v255, s[8:11], s20 offen sc1\n"
    "s_add_u32 s20, s20, 15360\n"
    "ds_read_b128 v[76:79], v255 offset:32768\n"
    "s_waitcnt vmcnt(9) lgkmcnt(7)\n"
    "v_mfma_f32_16x16x32_bf16 v[56:59], v[80:83], v[20:23], v[56:59]\n"
    "ds_read_b128 v[80:83], v255 offset:33792\n"
    "s_waitcnt lgkmcnt(7)\n"
    "v_mfma_f32_16x16x32_bf16 v[60:63], v[84:87], v[20:23], v[60:63]\n"
    "ds_read_b128 v[84:87], v255 offset:34816\n"
    "s_waitcnt lgkmcnt(7)\n"
    "v_mfma_f32_16x16x32_bf16 v[64:67], v[88:91], v[20:23], v[64:67]\n"
    "ds_read_b128 v[88:91], v255 offset:35840\n"
    "s_waitcnt lgkmcnt(7)\n"
    "v_mfma_f32_16x16x32_bf16 v[68:71], v[92:95], v[20:23], v[68:71]\n"
    "buffer_load_dwordx4 v[4:7], v255, s[8:11], s20 offen sc1\n"
    "ds_read_b128 v[92:95], v255 offset:36864\n"
    "s_waitcnt lgkmcnt(7)\n"
    "v_mfma_f32_16x16x32_bf16 v[72:75], v[96:99], v[20:23], v[72:75]\n"
    "ds_read_b128 v[96:99], v255 offset:37888\n"
    "s_waitcnt vmcnt(9) lgkmcnt(7)\n"
    "v_mfma_f32_16x16x32_bf16 v[56:59], v[100:103], v[24:27], v[56:59]\n"
    "ds_read_b128 v[100:103], v255 offset:38912\n"
    "s_waitcnt lgkmcnt(7)\n"
    "v_mfma_f32_16x16x32_bf16 v[60:63], v[104:107], v[24:27], v[60:63]\n"
    "ds_read_b128 v[104:107], v255 offset:39936\n"
    "s_waitcnt lgkmcnt(7)\n"
    "v_mfma_f32_16x16x32_bf16 v[64:67], v[76:79], v[24:27], v[64:67]\n"
    "buffer_load_dwordx4 v[8:11], v255, s[8:11], s20 offen offset:1024 sc1\n"
    "ds_read_b128 v[76:79], v255 offset:40960\n"
    "s_waitcnt lgkmcnt(7)\n"
    "v_mfma_f32_16x16x32_bf16 v[68:71], v[80:83], v[24:27], v[68:71]\n"
    "ds_read_b128 v[80:83], v255 offset:41984\n"
    "s_waitcnt lgkmcnt(7)\n"
    "v_mfma_f32_16x16x32_bf16 v[72:75], v[84:87], v[24:27], v[72:75]\n"
    "ds_read_b128 v[84:87], v255 offset:43008\n"
    "s_waitcnt vmcnt(9) lgkmcnt(7)\n"
    "v_mfma_f32_16x16x32_bf16 v[56:59], v[88:91], v[28:31], v[56:59]\n"
    "ds_read_b128 v[88:91], v255 offset:44032\n"
    "s_waitcnt lgkmcnt(7)\n"
    "v_mfma_f32_16x16x32_bf16 v[60:63], v[92:95], v[28:31], v[60:63]\n"
    "buffer_load_dwordx4 v[12:15], v255, s[8:11], s20 offen offset:2048 sc1\n"
    "ds_read_b128 v[92:95], v255 offset:45056\n"
    "s_waitcnt lgkmcnt(7)\n"
    "v_mfma_f32_16x16x32_bf16 v[64:67], v[96:99], v[28:31], v[64:67]\n"
    "ds_read_b128 v[96:99], v255 offset:46080\n"
    "s_waitcnt lgkmcnt(7)\n"
    "v_mfma_f32_16x16x32_bf16 v[68:71], v[100:103], v[28:31], v[68:71]\n"
    "ds_read_b128 v[100:103], v255 offset:47104\n"
    "s_waitcnt lgkmcnt(7)\n"
    "v_mfma_f32_16x16x32_bf16 v[72:75], v[104:107], v[28:31], v[72:75]\n"
    "ds_read_b128 v[104:107], v255 offset:48128\n"
    "s_waitcnt vmcnt(9) lgkmcnt(7)\n"
    "v_mfma_f32_16x16x32_bf16 v[56:59], v[76:79], v[32:35], v[56:59]\n"
    "buffer_load_dwordx4 v[16:19], v255, s[8:11], s20 offen offset:3072 sc1\n"
    "s_add_u32 s20, s20, 4096\n"
    "ds_read_b128 v[76:79], v255 offset:49152\n"
    "s_waitcnt lgkmcnt(7)\n"
    "v_mfma_f32_16x16x32_bf16 v[60:63], v[80:83], v[32:35], v[60:63]\n"
    "ds_read_b128 v[80:83], v255 offset:50176\n"
    "s_waitcnt lgkmcnt(7)\n"
    "v_mfma_f32_16x16x32_bf16 v[64:67], v[84:87], v[32:35], v[64:67]\n"
    "ds_read_b128 v[84:87], v235\n"
    "s_waitcnt lgkmcnt(7)\n"
    "v_mfma_f32_16x16x32_bf16 v[68:71], v[88:91], v[32:35], v[68:71]\n"
    "ds_read_b128 v[88:91], v235 offset:1024\n"
    "s_waitcnt lgkmcnt(7)\n"
    "v_mfma_f32_16x16x32_bf16 v[72:75], v[92:95], v[32:35], v[72:75]\n"
    "buffer_load_dwordx4 v[20:23], v255, s[8:11], s20 offen sc1\n"
    "ds_read_b128 v[92:95], v235 offset:2048\n"
    "s_waitcnt vmcnt(10) lgkmcnt(7)\n"
    "v_mfma_f32_16x16x32_bf16 v[56:59], v[96:99], v[36:39], v[56:59]\n"
    "ds_read_b128 v[96:99], v235 offset:3072\n"
    "s_waitcnt lgkmcnt(7)\n"
    "v_mfma_f32_16x16x32_bf16 v[60:63], v[100:103], v[36:39], v[60:63]\n"
    "ds_read_b128 v[100:103], v235 offset:4096\n"
    "s_waitcnt lgkmcnt(7)\n"
    "v_mfma_f32_16x16x32_bf16 v[64:67], v[104:107], v[36:39], v[64:67]\n"
    "ds_read_b128 v[104:107], v235 offset:5120\n"
    "s_waitcnt lgkmcnt(7)\n"
    "v_mfma_f32_16x16x32_bf16 v[68:71], v[76:79], v[36:39], v[68:71]\n"
    "buffer_load_dwordx4 v[220:223], v255, s[4:7], s22 offen offset:2048 sc1\n"
    "s_add_u32 s22, s22, 4096\n"
    "ds_read_b128 v[76:79], v235 offset:6144\n"
    "s_waitcnt lgkmcnt(7)\n"
    "v_mfma_f32_16x16x32_bf16 v[72:75], v[80:83], v[36:39], v[72:75]\n"
    "ds_read_b128 v[80:83], v235 offset:7168\n"
    "s_waitcnt vmcnt(10) lgkmcnt(7)\n"
    "v_mfma_f32_16x16x32_bf16 v[56:59], v[84:87], v[40:43], v[56:59]\n"
    "ds_read_b128 v[84:87], v235 offset:8192\n"
    "s_waitcnt lgkmcnt(7)\n"
    "v_mfma_f32_16x16x32_bf16 v[60:63], v[88:91], v[40:43], v[60:63]\n"
    "ds_read_b128 v[88:91], v235 offset:9216\n"
    "s_waitcnt lgkmcnt(7)\n"
    "v_mfma_f32_16x16x32_bf16 v[64:67], v[92:95], v[40:43], v[64:67]\n"
    "buffer_load_dwordx4 v[224:227], v255, s[4:7], s27 offen offset:2048 sc1\n"
    "s_add_u32 s27, s27, 4096\n"
    "ds_read_b128 v[92:95], v235 offset:10240\n"
    "s_waitcnt lgkmcnt(7)\n"
    "v_mfma_f32_16x16x32_bf16 v[68:71], v[96:99], v[40:43], v[68:71]\n"
    "ds_read_b128 v[96:99], v235 offset:11264\n"
    "s_waitcnt lgkmcnt(7)\n"
    "v_mfma_f32_16x16x32_bf16 v[72:75], v[100:103], v[40:43], v[72:75]\n"
    "ds_read_b128 v[100:103], v235 offset:12288\n"
    "s_waitcnt vmcnt(10) lgkmcnt(7)\n"
    "v_mfma_f32_16x16x32_bf16 v[56:59], v[104:107], v[44:47], v[56:59]\n"
    "ds_read_b128 v[104:107], v235 offset:13312\n"
    "s_waitcnt lgkmcnt(7)\n"
    "v_mfma_f32_16x16x32_bf16 v[60:63], v[76:79], v[44:47], v[60:63]\n"
    "ds_read_b128 v[76:79], v235 offset:14336\n"
    "s_waitcnt lgkmcnt(7)\n"
    "v_mfma_f32_16x16x32_bf16 v[64:67], v[80:83], v[44:47], v[64:67]\n"
    "ds_read_b128 v[80:83], v235 offset:15360\n"
    "s_waitcnt lgkmcnt(7)\n"
    "v_mfma_f32_16x16x32_bf16 v[68:71], v[84:87], v[44:47], v[68:71]\n"
    "ds_read_b128 v[84:87], v235 offset:16384\n"
    "s_waitcnt lgkmcnt(7)\n"
    "v_mfma_f32_16x16x32_bf16 v[72:75], v[88:91], v[44:47], v[72:75]\n"
    "ds_read_b128 v[88:91], v235 offset:17408\n"
    "s_waitcnt vmcnt(9) lgkmcnt(7)\n"
    "v_mfma_f32_16x16x32_bf16 v[56:59], v[92:95], v[48:51], v[56:59]\n"
    "ds_read_b128 v[92:95], v235 offset:18432\n"
    "s_waitcnt lgkmcnt(7)\n"
    "v_mfma_f32_16x16x32_bf16 v[60:63], v[96:99], v[48:51], v[60:63]\n"
    "ds_read_b128 v[96:99], v235 offset:19456\n"
    "s_waitcnt lgkmcnt(7)\n"
    "v_mfma_f32_16x16x32_bf16 v[64:67], v[100:103], v[48:51], v[64:67]\n"
    "ds_read_b128 v[100:103], v235 offset:20480\n"
    "s_waitcnt lgkmcnt(7)\n"
    "v_mfma_f32_16x16x32_bf16 v[68:71], v[104:107], v[48:51], v[68:71]\n"
    "ds_read_b128 v[104:107], v235 offset:21504\n"
    "s_waitcnt lgkmcnt(7)\n"
    "v_mfma_f32_16x16x32_bf16 v[72:75], v[76:79], v[48:51], v[72:75]\n"
    "ds_read_b128 v[76:79], v235 offset:22528\n"
    "s_waitcnt vmcnt(8) lgkmcnt(7)\n"
    "v_mfma_f32_16x16x32_bf16 v[56:59], v[80:83], v[52:55], v[56:59]\n"
    "ds_read_b128 v[80:83], v235 offset:23552\n"
    "s_waitcnt lgkmcnt(7)\n"
    "v_mfma_f32_16x16x32_bf16 v[60:63], v[84:87], v[52:55], v[60:63]\n"
    "ds_read_b128 v[84:87], v235 offset:24576\n"
    "s_waitcnt lgkmcnt(7)\n"
    "v_mfma_f32_16x16x32_bf16 v[64:67], v[88:91], v[52:55], v[64:67]\n"
    "ds_read_b128 v[88:91], v235 offset:25600\n"
    "s_waitcnt lgkmcnt(7)\n"
    "v_mfma_f32_16x16x32_bf16 v[68:71], v[92:95], v[52:55], v[68:71]\n"
    "ds_read_b128 v[92:95], v235 offset:26624\n"
    "s_waitcnt lgkmcnt(7)\n"
    "v_mfma_f32_16x16x32_bf16 v[72:75], v[96:99], v[52:55], v[72:75]\n"
    "ds_read_b128 v[96:99], v235 offset:27648\n"
    "s_waitcnt vmcnt(7) lgkmcnt(7)\n"
    "v_mfma_f32_16x16x32_bf16 v[56:59], v[100:103], v[0:3], v[56:59]\n"
    "ds_read_b128 v[100:103], v235 offset:28672\n"
    "s_waitcnt lgkmcnt(7)\n"
    "v_mfma_f32_16x16x32_bf16 v[60:63], v[104:107], v[0:3], v[60:63]\n"
    "ds_read_b128 v[104:107], v235 offset:29696\n"
    "s_waitcnt lgkmcnt(7)\n"
    "v_mfma_f32_16x16x32_bf16 v[64:67], v[76:79], v[0:3], v[64:67]\n"
    "ds_read_b128 v[76:79], v235 offset:30720\n"
    "s_waitcnt lgkmcnt(7)\n"
    "v_mfma_f32_16x16x32_bf16 v[68:71], v[80:83], v[0:3], v[68:71]\n"
    "ds_read_b128 v[80:83], v235 offset:31744\n"
    "s_waitcnt lgkmcnt(7)\n"
    "v_mfma_f32_16x16x32_bf16 v[72:75], v[84:87], v[0:3], v[72:75]\n"
    "ds_read_b128 v[84:87], v235 offset:32768\n"
    "s_waitcnt vmcnt(6) lgkmcnt(7)\n"
    "v_mfma_f32_16x16x32_bf16 v[56:59], v[88:91], v[4:7], v[56:59]\n"
    "ds_read_b128 v[88:91], v235 offset:33792\n"
    "s_waitcnt lgkmcnt(7)\n"
    "v_mfma_f32_16x16x32_bf16 v[60:63], v[92:95], v[4:7], v[60:63]\n"
    "ds_read_b128 v[92:95], v235 offset:34816\n"
    "s_waitcnt lgkmcnt(7)\n"
    "v_mfma_f32_16x16x32_bf16 v[64:67], v[96:99], v[4:7], v[64:67]\n"
    "ds_read_b128 v[96:99], v235 offset:35840\n"
    "s_waitcnt lgkmcnt(7)\n"
    "v_mfma_f32_16x16x32_bf16 v[68:71], v[100:103], v[4:7], v[68:71]\n"
    "ds_read_b128 v[100:103], v235 offset:36864\n"
    "s_waitcnt lgkmcnt(7)\n"
    "v_mfma_f32_16x16x32_bf16 v[72:75], v[104:107], v[4:7], v[72:75]\n"
    "ds_read_b128 v[104:107], v235 offset:37888\n"
    "s_waitcnt vmcnt(5) lgkmcnt(7)\n"
    "v_mfma_f32_16x16x32_bf16 v[56:59], v[76:79], v[8:11], v[56:59]\n"
    "ds_read_b128 v[76:79], v235 offset:38912\n"
    "s_waitcnt lgkmcnt(7)\n"
    "v_mfma_f32_16x16x32_bf16 v[60:63], v[80:83], v[8:11], v[60:63]\n"
    "ds_read_b128 v[80:83], v235 offset:39936\n"
    "s_waitcnt lgkmcnt(7)\n"
    "v_mfma_f32_16x16x32_bf16 v[64:67], v[84:87], v[8:11], v[64:67]\n"
    "ds_read_b128 v[84:87], v235 offset:40960\n"
    "s_waitcnt lgkmcnt(7)\n"
    "v_mfma_f32_16x16x32_bf16 v[68:71], v[88:91], v[8:11], v[68:71]\n"
    "ds_read_b128 v[88:91], v235 offset:41984\n"
    "s_waitcnt lgkmcnt(7)\n"
    "v_mfma_f32_16x16x32_bf16 v[72:75], v[92:95], v[8:11], v[72:75]\n"
    "ds_read_b128 v[92:95], v235 offset:43008\n"
    "s_waitcnt vmcnt(4) lgkmcnt(7)\n"
    "v_mfma_f32_16x16x32_bf16 v[56:59], v[96:99], v[12:15], v[56:59]\n"
    "ds_read_b128 v[96:99], v235 offset:44032\n"
    "s_waitcnt lgkmcnt(7)\n"
    "v_mfma_f32_16x16x32_bf16 v[60:63], v[100:103], v[12:15], v[60:63]\n"
    "ds_read_b128 v[100:103], v235 offset:45056\n"
    "s_waitcnt lgkmcnt(7)\n"
    "v_mfma_f32_16x16x32_bf16 v[64:67], v[104:107], v[12:15], v[64:67]\n"
    "ds_read_b128 v[104:107], v235 offset:46080\n"
    "s_waitcnt lgkmcnt(7)\n"
    "v_mfma_f32_16x16x32_bf16 v[68:71], v[76:79], v[12:15], v[68:71]\n"
    "ds_read_b128 v[76:79], v235 offset:47104\n"
    "s_waitcnt lgkmcnt(7)\n"
    "v_mfma_f32_16x16x32_bf16 v[72:75], v[80:83], v[12:15], v[72:75]\n"
    "ds_read_b128 v[80:83], v235 offset:48128\n"
    "s_waitcnt vmcnt(3) lgkmcnt(7)\n"
    "v_mfma_f32_16x16x32_bf16 v[56:59], v[84:87], v[16:19], v[56:59]\n"
    "ds_read_b128 v[84:87], v235 offset:49152\n"
    "s_waitcnt lgkmcnt(7)\n"
    "v_mfma_f32_16x16x32_bf16 v[60:63], v[88:91], v[16:19], v[60:63]\n"
    "ds_read_b128 v[88:91], v235 offset:50176\n"
    "s_waitcnt lgkmcnt(7)\n"
    "v_mfma_f32_16x16x32_bf16 v[64:67], v[92:95], v[16:19], v[64:67]\n"
    "v_mov_b64 v[36:37], 0\n"
    "s_waitcnt lgkmcnt(6)\n"
    "v_mfma_f32_16x16x32_bf16 v[68:71], v[96:99], v[16:19], v[68:71]\n"
    "v_mov_b32 v38, 0x3f803f80\n"
    "s_waitcnt lgkmcnt(5)\n"
    "v_mfma_f32_16x16x32_bf16 v[72:75], v[100:103], v[16:19], v[72:75]\n"
    "v_mov_b32 v44, v234\n"
    "s_waitcnt vmcnt(2) lgkmcnt(4)\n"
    "v_mfma_f32_16x16x32_bf16 v[56:59], v[104:107], v[20:23], v[56:59]\n"
    "s_waitcnt lgkmcnt(3)\n"
    "v_mfma_f32_16x16x32_bf16 v[60:63], v[76:79], v[20:23], v[60:63]\n"
    "s_waitcnt lgkmcnt(2)\n"
    "v_mfma_f32_16x16x32_bf16 v[64:67], v[80:83], v[20:23], v[64:67]\n"
    "s_waitcnt lgkmcnt(1)\n"
    "v_mfma_f32_16x16x32_bf16 v[68:71], v[84:87], v[20:23], v[68:71]\n"
    "s_waitcnt lgkmcnt(0)\n"
    "v_mfma_f32_16x16x32_bf16 v[72:75], v[88:91], v[20:23], v[72:75]\n"
    "v_cvt_pk_bf16_f32 v24, v56, v57\n"
    "v_cvt_pk_bf16_f32 v25, v58, v59\n"
    "v_cvt_pk_bf16_f32 v26, v60, v61\n"
    "v_cvt_pk_bf16_f32 v27, v62, v63\n"
    "buffer_store_dwordx4 v[24:27], v236, s[8:11], 0 offen sc1\n"
    "v_cvt_pk_bf16_f32 v28, v64, v65\n"
    "v_cvt_pk_bf16_f32 v29, v66, v67\n"
    "v_cvt_pk_bf16_f32 v30, v68, v69\n"
    "v_cvt_pk_bf16_f32 v31, v70, v71\n"
    "buffer_store_dwordx4 v[28:31], v236, s[8:11], 0 offen offset:1024 sc1\n"
    "v_cvt_pk_bf16_f32 v32, v72, v73\n"
    "v_cvt_pk_bf16_f32 v33, v74, v75\n"
    "v_mov_b64 v[40:41], v[32:33]\n"
    "v_dot2c_f32_bf16 v36, v24, v38\n"
    "v_dot2c_f32_bf16 v37, v24, v24\n"
    "v_dot2c_f32_bf16 v36, v25, v38\n"
    "v_dot2c_f32_bf16 v37, v25, v25\n"
    "v_permlane16_swap_b32 v40, v42\n"
    "v_permlane16_swap_b32 v41, v43\n"
    "buffer_store_dwordx4 v[40:43], v44, s[8:11], 0 offen sc1\n"
    "v_dot2c_f32_bf16 v36, v26, v38\n"
    "v_dot2c_f32_bf16 v37, v26, v26\n"
    "v_dot2c_f32_bf16 v36, v27, v38\n"
    "v_dot2c_f32_bf16 v37, v27, v27\n"
    "v_dot2c_f32_bf16 v36, v28, v38\n"
    "v_dot2c_f32_bf16 v37, v28, v28\n"
    "v_dot2c_f32_bf16 v36, v29, v38\n"
    "v_dot2c_f32_bf16 v37, v29, v29\n"
    "v_dot2c_f32_bf16 v36, v30, v38\n"
    "v_dot2c_f32_bf16 v37, v30, v30\n"
    "v_dot2c_f32_bf16 v36, v31, v38\n"
    "v_dot2c_f32_bf16 v37, v31, v31\n"
    "v_dot2c_f32_bf16 v36, v32, v38\n"
    "v_dot2c_f32_bf16 v37, v32, v32\n"
    "v_dot2c_f32_bf16 v36, v33, v38\n"
    "v_dot2c_f32_bf16 v37, v33, v33\n"
    "s_nop 2\n"
    "v_mov_b64 v[38:39], v[36:37]\n"
    "s_nop 1\n"
    "v_permlane16_swap_b32 v36, v38\n"
    "v_permlane16_swap_b32 v37, v39\n"
    "v_pk_add_f32 v[36:37], v[36:37], v[38:39]\n"
    "v_mov_b64 v[38:39], v[36:37]\n"
    "s_nop 1\n"
    "v_permlane32_swap_b32 v36, v38\n"
    "v_permlane32_swap_b32 v37, v39\n"
    "v_pk_add_f32 v[36:37], v[36:37], v[38:39]\n"
    "buffer_store_dwordx2 v[36:37], v237, s[8:11], s24 offen sc1\n"
    "v_add_u32 v16, 1024, v236\n"
    "v_mov_b32 v17, v236\n"
    "v_mov_b32 v18, v237\n"
    "s_barrier\n"
    "s_mul_i32 s58, s46, 11\n"
    "s_add_u32 s58, s58, 1\n"
    "s_lshr_b32 s58, s58, 1\n"
    "s_mul_i32 s74, s48, 0x43800\n"
    "s_mul_i32 s70, s58, 0x1400\n"
    "s_add_u32 s74, s74, s70\n"
    "s_add_u32 s74, s74, 0x36c000\n"
    "s_lshl_b32 s56, s52, 31\n"
    "v_or_b32 v237, s56, v255\n"
    "buffer_load_dwordx4 v[64:67], v255, s[4:7], s74 offen sc1\n"
    "buffer_load_dwordx4 v[68:71], v255, s[4:7], s74 offen offset:1024 sc1\n"
    "buffer_load_dwordx4 v[72:75], v255, s[4:7], s74 offen offset:2048 sc1\n"
    "buffer_load_dwordx4 v[76:79], v255, s[4:7], s74 offen offset:3072 sc1\n"
    "s_add_u32 s74, s74, 4096\n"
    "buffer_load_dwordx4 v[80:83], v255, s[4:7], s74 offen sc1\n"
    "buffer_load_dwordx4 v[84:87], v255, s[4:7], s74 offen offset:1024 sc1\n"
    "s_waitcnt vmcnt(6)\n"
    "s_barrier\n"
    "s_nop 0\n"
    "buffer_load_dwordx4 v[88:91], v255, s[4:7], s74 offen offset:2048 sc1\n"
    "buffer_load_dwordx4 v[92:95], v255, s[4:7], s74 offen offset:3072 sc1\n"
    "s_add_u32 s74, s74, 4096\n"
    "buffer_load_dwordx4 v[96:99], v255, s[4:7], s74 offen sc1\n"
    "buffer_load_dwordx4 v[100:103], v255, s[4:7], s74 offen offset:1024 sc1\n"
    "buffer_load_dwordx4 v[104:107], v255, s[4:7], s74 offen offset:2048 sc1\n"
    "buffer_load_dwordx4 v[228:231], v255, s[4:7], s74 offen offset:3072 sc1\n"
    "s_add_u32 s74, s74, 4096\n"
    "s_mul_i32 s73, s46, 0x4000\n"
    "s_mul_i32 s70, s52, 0x800\n"
    "s_add_u32 s73, s73, s70\n"
    "s_add_u32 s73, s73, 0x98000\n"
    "s_mul_i32 s56, s52, 0xa400\n"
    "v_add_u32 v232, s56, v255\n"
    "s_mul_i32 s56, s52, 0x9c00\n"
    "s_add_u32 s56, s56, 0x400\n"
    "v_add_u32_e64 v233, s56, v255\n"
    "s_mul_i32 s56, s52, 0xffff6400\n"
    "s_add_u32 s56, s56, 0xa000\n"
    "v_add_u32_e64 v234, s56, v255\n"
    "s_mul_i32 s56, s52, 0xffff5c00\n"
    "s_add_u32 s56, s56, 0xa400\n"
    "v_add_u32_e64 v235, s56, v255\n"
    "s_mul_i32 s59, s46, 0x100\n"
    "s_add_u32 s59, s59, 0x27280\n"
    "v_and_b32 v0, 1, v249\n"
    "v_lshlrev_b32 v0, 17, v0\n"
    "v_lshrrev_b32_e64 v1, 1, v249\n"
    "v_lshl_add_u32 v0, v1, 8, v0\n"
    "v_lshl_add_u32 v0, v248, 4, v0\n"
    "v_add_u32 v236, 622592, v0\n"
    "v_lshl_add_u32 v238, v248, 4, s59\n"
    "v_add_u32 v239, s72, v255\n"
    "v_lshrrev_b32 v0, 1, v255\n"
    "s_mov_b32 m0, s72\n"
    "s_barrier\n"
    "buffer_load_dwordx4 v255, s[8:11], s25 offen sc1 lds\n"
    "buffer_load_dwordx4 v255, s[8:11], s25 offen offset:1024 sc1 lds\n"
    "buffer_load_dwordx4 v255, s[8:11], s25 offen offset:2048 sc1 lds\n"
    "buffer_load_dwordx4 v255, s[8:11], s25 offen offset:3072 sc1 lds\n"
    "s_add_u32 m0, s72, 4096\n"
    "s_add_u32 s25, s25, 4096\n"
    "buffer_load_dwordx4 v255, s[8:11], s25 offen sc1 lds\n"
    "s_add_u32 m0, s72, 5120\n"
    "s_add_u32 s25, s25, 15360\n"
    "buffer_load_dwordx4 v255, s[8:11], s25 offen sc1 lds\n"
    "buffer_load_dwordx4 v255, s[8:11], s25 offen offset:1024 sc1 lds\n"
    "buffer_load_dwordx4 v255, s[8:11], s25 offen offset:2048 sc1 lds\n"
    "buffer_load_dwordx4 v255, s[8:11], s25 offen offset:3072 sc1 lds\n"
    "s_add_u32 m0, s72, 9216\n"
    "s_add_u32 s25, s25, 4096\n"
    "buffer_load_dwordx4 v255, s[8:11], s25 offen sc1 lds\n"
    "s_add_u32 m0, s72, 10240\n"
    "s_add_u32 s25, s25, 9216\n"
    "buffer_load_dwordx4 v255, s[8:11], s25 offen sc1 lds\n"
    "buffer_load_dwordx4 v255, s[8:11], s25 offen offset:1024 sc1 lds\n"
    "buffer_load_dwordx4 v255, s[8:11], s25 offen offset:2048 sc1 lds\n"
    "s_waitcnt vmcnt(10)\n"
    "s_barrier\n"
    "buffer_load_dwordx4 v255, s[8:11], s25 offen offset:3072 sc1 lds\n"
    "s_add_u32 m0, s72, 14336\n"
    "s_add_u32 s25, s25, 4096\n"
    "buffer_load_dwordx4 v255, s[8:11], s25 offen sc1 lds\n"
    "s_add_u32 m0, s72, 15360\n"
    "s_add_u32 s25, s25, 15360\n"
    "buffer_load_dwordx4 v255, s[8:11], s25 offen sc1 lds\n"
    "s_waitcnt vmcnt(10)\n"
    "s_barrier\n"
    "buffer_load_dwordx4 v255, s[8:11], s25 offen offset:1024 sc1 lds\n"
    "buffer_load_dwordx4 v255, s[8:11], s25 offen offset:2048 sc1 lds\n"
    "buffer_load_dwordx4 v255, s[8:11], s25 offen offset:3072 sc1 lds\n"
    "s_waitcnt vmcnt(10)\n"
    "s_barrier\n"
    "s_add_u32 m0, s72, 19456\n"
    "s_add_u32 s25, s25, 4096\n"
    "buffer_load_dwordx4 v255, s[8:11], s25 offen sc1 lds\n"
    "buffer_load_dwordx2 v[2:3], v0, s[8:11], s26 offen sc1\n"
    "buffer_load_dwordx2 v[4:5], v0, s[8:11], s26 offen offset:512 sc1\n"
    "s_waitcnt vmcnt(10)\n"
    "s_barrier\n"
    "s_waitcnt vmcnt(6)\n"
    "s_barrier\n"
    "s_waitcnt vmcnt(0)\n"
    "v_add_f32 v2, v2, v4\n"
    "v_add_f32 v3, v3, v5\n"
    "v_mov_b64 v[4:5], v[2:3]\n"
    "s_nop 1\n"
    "v_permlane16_swap_b32 v2, v4\n"
    "v_permlane16_swap_b32 v3, v5\n"
    "v_add_f32 v2, v2, v4\n"
    "v_add_f32 v3, v3, v5\n"
    "v_mov_b64 v[4:5], v[2:3]\n"
    "s_nop 1\n"
    "v_permlane32_swap_b32 v2, v4\n"
    "v_permlane32_swap_b32 v3, v5\n"
    "v_add_f32 v2, v2, v4\n"
    "v_add_f32_e64 v3, v3, v5\n"
    "v_mul_f32 v2, 0x3acccccd, v2\n"
    "v_mul_f32 v3, 0x3acccccd, v3\n"
    "v_fma_f32 v3, -v2, v2, v3\n"
    "v_max_f32_e64 v3, 0, v3\n"
    "v_add_f32 v4, 0x3727c5ac, v3\n"
    "v_rsq_f32 v4, v4\n"
    "s_nop 0\n"
    "v_mul_f32 v3, v3, v4\n"
    "v_mul_f32 v3, v3, v4\n"
    "v_add_f32 v3, 0x3727c5ac, v3\n"
    "v_rsq_f32 v3, v3\n"
    "s_nop 0\n"
    "v_mul_f32 v4, v3, v4\n"
    "v_mov_b32 v5, v4\n"
    "v_mul_f32 v2, v2, v4\n"
    "v_mov_b32 v3, v2\n"
    "ds_write_b128 v238, v[2:5]\n"
    "s_barrier\n"
    "s_waitcnt lgkmcnt(0)\n"
    "s_barrier\n"
    "s_cmp_lg_u32 s46, 0\n"
    "s_cbranch_scc1 .Lk_m0127\n"
    "v_mov_b32 v0, 159936\n"
    "ds_read_b64 v[0:1], v0\n"
    "s_waitcnt lgkmcnt(0)\n"
    "v_readfirstlane_b32 s44, v0\n"
    "v_readfirstlane_b32 s45, v1\n"
    "s_nop 0\n"
    "v_mov_b32 v242, s44\n"
    "v_mov_b32_e64 v243, s45\n"
    "v_bfe_u32 v241, v255, 4, 3\n"
    "v_lshlrev_b32 v241, 7, v241\n"
    "s_lshl_b32 s56, s48, 7\n"
    "v_mov_b32_e64 v240, s56\n"
    "v_mov_b32 v244, 0x80000000\n"
    "v_cmp_eq_u32 vcc, 0, v255\n"
    "s_nop 1\n"
    "v_cndmask_b32 v240, v244, v240, vcc\n"
    "s_mov_b32 s94, 4096\n"
    ".Lk_m0127:\n"
    "s_mul_i32 s60, s46, 0x40\n"
    "s_add_u32 s60, s60, 0x27880\n"
    "s_mul_i32 s61, s53, 0x40\n"
    "s_add_u32 s61, s61, 0x27980\n"
    "s_mul_i32 s62, s52, 0x200\n"
    "s_add_u32 s62, s62, 0x27280\n"
    "s_mul_i32 s63, s52, 0xfffffe00\n"
    "s_add_u32 s63, s63, 0x27480\n"
    "s_mul_i32 s65, s49, 0xb\n"
    "s_mul_i32 s70, s50, 0x6\n"
    "s_add_u32 s65, s65, s70\n"
    "s_add_u32 s65, s65, s46\n"
    "s_lshl_b32 s67, s65, 9\n"
    "s_cmp_ge_u32 s65, 12\n"
    "s_cselect_b32 s66, 12288, 0\n"
    "s_add_u32 s67, s67, s66\n"
    "s_cmp_ge_u32 s65, 22\n"
    "s_cselect_b32 s66, 9216, 0\n"
    "s_add_u32 s67, s67, s66\n"
    "s_cmp_ge_u32 s65, 34\n"
    "s_cselect_b32 s66, 12288, 0\n"
    "s_add_u32 s67, s67, s66\n"
    "s_mul_i32 s65, s49, 0xb\n"
    "s_mul_i32 s70, s50, 0x6\n"
    "s_add_u32 s65, s65, s70\n"
    "s_add_u32 s65, s65, s53\n"
    "s_add_u32 s65, s65, 0x4\n"
    "s_lshl_b32 s68, s65, 9\n"
    "s_cmp_ge_u32 s65, 12\n"
    "s_cselect_b32 s66, 12288, 0\n"
    "s_add_u32 s68, s68, s66\n"
    "s_cmp_ge_u32 s65, 22\n"
    "s_cselect_b32 s66, 9216, 0\n"
    "s_add_u32 s68, s68, s66\n"
    "s_cmp_ge_u32 s65, 34\n"
    "s_cselect_b32 s66, 12288, 0\n"
    "s_add_u32 s68, s68, s66\n"
    "s_mul_i32 s56, s52, 0x40000\n"
    "s_add_u32 s56, s56, s67\n"
    "s_mul_i32 s57, s52, 0xfffc0000\n"
    "s_add_u32 s57, s57, s67\n"
    "s_add_u32 s57, s57, 0x40000\n"
    "s_and_b32 s59, s50, s53\n"
    "s_lshl_b32 s59, s59, 31\n"
    "s_mul_i32 s58, s52, 0x40000\n"
    "s_add_u32 s58, s58, s68\n"
    "s_add_u32 s58, s58, s59\n"
    "s_mul_i32 s64, s52, 0xffff6000\n"
    "s_add_u32 s64, s64, s72\n"
    "s_add_u32 s64, s64, 0x5000\n"
    "v_add_u32 v246, s64, v255\n"
    "s_lshl_b32 s64, s53, 2\n"
    "v_add_u32 v247, s64, v254\n"
    "s_mov_b32 s12, s34\n"
    "s_and_b32 s13, s35, 0xffff\n"
    "s_mov_b32 s14, 2621440\n"
    "s_mov_b32 s15, 0x20000\n"
    "s_add_u32 s64, s46, 1\n"
    "s_and_b32 s64, s64, 3\n"
    "s_mul_i32 s78, s64, 0x20000\n"
    "s_add_u32 s78, s78, s73\n"
    "s_add_u32 s82, s78, 4096\n"
    "s_mul_i32 s28, s64, 0x5000\n"
    "s_add_u32 s28, s28, 0x800\n"
    "s_lshl_b32 s88, s64, 2\n"
    "s_add_u32 s64, s46, 2\n"
    "s_and_b32 s64, s64, 3\n"
    "s_mul_i32 s79, s64, 0x20000\n"
    "s_add_u32 s79, s79, s73\n"
    "s_add_u32 s83, s79, 4096\n"
    "s_mul_i32 s29, s64, 0x5000\n"
    "s_add_u32 s29, s29, 0xa00\n"
    "s_lshl_b32 s89, s64, 2\n"
    "s_add_u32 s64, s46, 3\n"
    "s_and_b32 s64, s64, 3\n"
    "s_mul_i32 s80, s64, 0x20000\n"
    "s_add_u32 s80, s80, s73\n"
    "s_add_u32 s84, s80, 4096\n"
    "s_mul_i32 s30, s64, 0x5000\n"
    "s_add_u32 s30, s30, 0xc00\n"
    "s_lshl_b32 s90, s64, 2\n"
    "s_add_u32 s64, s46, 4\n"
    "s_and_b32 s64, s64, 3\n"
    "s_mul_i32 s81, s64, 0x20000\n"
    "s_add_u32 s81, s81, s73\n"
    "s_add_u32 s85, s81, 4096\n"
    "s_mul_i32 s77, s47, 0x14000\n"
    "s_mul_i32 s70, s46, 0x5000\n"
    "s_add_u32 s77, s77, s70\n"
    "s_mul_i32 s70, s48, 0xa0\n"
    "s_add_u32 s77, s77, s70\n"
    "ds_read_b128 v[0:3], v232\n"
    "ds_read_b128 v[4:7], v232 offset:20480\n"
    "ds_read_b128 v[8:11], v234\n"
    "ds_read_b128 v[12:15], v234 offset:20480\n"
    "ds_read_b128 v[16:19], v233\n"
    "ds_read_b128 v[20:23], v233 offset:20480\n"
    "ds_read_b128 v[24:27], v235\n"
    "ds_read_b128 v[28:31], v235 offset:20480\n"
    "s_barrier\n"
    "s_waitcnt lgkmcnt(7)\n"
    "v_mfma_f32_16x16x32_bf16 v[32:35], v[108:111], v[0:3], 0\n"
    "v_mfma_f32_16x16x32_bf16 v[48:51], v[112:115], v[0:3], 0\n"
    "ds_read_b128 v[0:3], v232 offset:2048\n"
    "s_waitcnt lgkmcnt(7)\n"
    "v_mfma_f32_16x16x32_bf16 v[36:39], v[108:111], v[4:7], 0\n"
    "v_mfma_f32_16x16x32_bf16 v[52:55], v[112:115], v[4:7], 0\n"
    "ds_read_b128 v[4:7], v232 offset:22528\n"
    "s_waitcnt lgkmcnt(7)\n"
    "v_mfma_f32_16x16x32_bf16 v[40:43], v[108:111], v[8:11], 0\n"
    "v_mfma_f32_16x16x32_bf16 v[56:59], v[112:115], v[8:11], 0\n"
    "ds_read_b128 v[8:11], v234 offset:2048\n"
    "s_waitcnt lgkmcnt(7)\n"
    "v_mfma_f32_16x16x32_bf16 v[44:47], v[108:111], v[12:15], 0\n"
    "v_mfma_f32_16x16x32_bf16 v[60:63], v[112:115], v[12:15], 0\n"
    "ds_read_b128 v[12:15], v234 offset:22528\n"
    "s_waitcnt lgkmcnt(7)\n"
    "v_mfma_f32_16x16x32_bf16 v[32:35], v[116:119], v[16:19], v[32:35]\n"
    "ds_read_b128 v[16:19], v233 offset:2048\n"
    "s_waitcnt lgkmcnt(7)\n"
    "v_mfma_f32_16x16x32_bf16 v[36:39], v[116:119], v[20:23], v[36:39]\n"
    "ds_read_b128 v[20:23], v233 offset:22528\n"
    "s_waitcnt lgkmcnt(7)\n"
    "v_mfma_f32_16x16x32_bf16 v[40:43], v[116:119], v[24:27], v[40:43]\n"
    "ds_read_b128 v[24:27], v235 offset:2048\n"
    "s_waitcnt lgkmcnt(7)\n"
    "v_mfma_f32_16x16x32_bf16 v[44:47], v[116:119], v[28:31], v[44:47]\n"
    "ds_read_b128 v[28:31], v235 offset:22528\n"
    "s_waitcnt lgkmcnt(7)\n"
    "v_mfma_f32_16x16x32_bf16 v[32:35], v[120:123], v[0:3], v[32:35]\n"
    "buffer_load_dwordx4 v[108:111], v255, s[4:7], s74 offen sc1\n"
    "v_mfma_f32_16x16x32_bf16 v[48:51], v[124:127], v[0:3], v[48:51]\n"
    "ds_read_b128 v[0:3], v232 offset:4096\n"
    "s_waitcnt lgkmcnt(7)\n"
    "v_mfma_f32_16x16x32_bf16 v[36:39], v[120:123], v[4:7], v[36:39]\n"
    "v_mfma_f32_16x16x32_bf16 v[52:55], v[124:127], v[4:7], v[52:55]\n"
    "ds_read_b128 v[4:7], v232 offset:24576\n"
    "s_waitcnt lgkmcnt(7)\n"
    "v_mfma_f32_16x16x32_bf16 v[40:43], v[120:123], v[8:11], v[40:43]\n"
    "v_mfma_f32_16x16x32_bf16 v[56:59], v[124:127], v[8:11], v[56:59]\n"
    "ds_read_b128 v[8:11], v234 offset:4096\n"
    "s_waitcnt lgkmcnt(7)\n"
    "v_mfma_f32_16x16x32_bf16 v[44:47], v[120:123], v[12:15], v[44:47]\n"
    "buffer_load_dwordx4 v[112:115], v255, s[4:7], s74 offen offset:1024 sc1\n"
    "v_mfma_f32_16x16x32_bf16 v[60:63], v[124:127], v[12:15], v[60:63]\n"
    "ds_read_b128 v[12:15], v234 offset:24576\n"
    "s_waitcnt lgkmcnt(7)\n"
    "v_mfma_f32_16x16x32_bf16 v[32:35], v[128:131], v[16:19], v[32:35]\n"
    "ds_read_b128 v[16:19], v233 offset:4096\n"
    "s_waitcnt lgkmcnt(7)\n"
    "v_mfma_f32_16x16x32_bf16 v[36:39], v[128:131], v[20:23], v[36:39]\n"
    "ds_read_b128 v[20:23], v233 offset:24576\n"
    "s_waitcnt lgkmcnt(7)\n"
    "v_mfma_f32_16x16x32_bf16 v[40:43], v[128:131], v[24:27], v[40:43]\n"
    "ds_read_b128 v[24:27], v235 offset:4096\n"
    "s_waitcnt lgkmcnt(7)\n"
    "v_mfma_f32_16x16x32_bf16 v[44:47], v[128:131], v[28:31], v[44:47]\n"
    "ds_read_b128 v[28:31], v235 offset:24576\n"
    "ds_add_u32 v254, v253 offset:144\n"
    "s_waitcnt lgkmcnt(8)\n"
    "v_mfma_f32_16x16x32_bf16 v[32:35], v[132:135], v[0:3], v[32:35]\n"
    "buffer_load_dwordx4 v[116:119], v255, s[4:7], s74 offen offset:2048 sc1\n"
    "v_mfma_f32_16x16x32_bf16 v[48:51], v[136:139], v[0:3], v[48:51]\n"
    "ds_read_b128 v[0:3], v232 offset:6144\n"
    "s_waitcnt lgkmcnt(8)\n"
    "v_mfma_f32_16x16x32_bf16 v[36:39], v[132:135], v[4:7], v[36:39]\n"
    "v_mfma_f32_16x16x32_bf16 v[52:55], v[136:139], v[4:7], v[52:55]\n"
    "ds_read_b128 v[4:7], v232 offset:26624\n"
    "s_waitcnt lgkmcnt(8)\n"
    "v_mfma_f32_16x16x32_bf16 v[40:43], v[132:135], v[8:11], v[40:43]\n"
    "v_mfma_f32_16x16x32_bf16 v[56:59], v[136:139], v[8:11], v[56:59]\n"
    "ds_read_b128 v[8:11], v234 offset:6144\n"
    "s_waitcnt lgkmcnt(8)\n"
    "v_mfma_f32_16x16x32_bf16 v[44:47], v[132:135], v[12:15], v[44:47]\n"
    "buffer_load_dwordx4 v[120:123], v255, s[4:7], s74 offen offset:3072 sc1\n"
    "s_add_u32 s74, s74, 4096\n"
    "v_mfma_f32_16x16x32_bf16 v[60:63], v[136:139], v[12:15], v[60:63]\n"
    "ds_read_b128 v[12:15], v234 offset:26624\n"
    "s_waitcnt lgkmcnt(8)\n"
    "v_mfma_f32_16x16x32_bf16 v[32:35], v[140:143], v[16:19], v[32:35]\n"
    ".Lk_pad9:\n"
    "s_cbranch_execz .Lk_tramp\n"
    "ds_read_b128 v[16:19], v233 offset:6144\n"
    "s_waitcnt lgkmcnt(8)\n"
    "v_mfma_f32_16x16x32_bf16 v[36:39], v[140:143], v[20:23], v[36:39]\n"
    "ds_read_b128 v[20:23], v233 offset:26624\n"
    "s_waitcnt lgkmcnt(8)\n"
    "v_mfma_f32_16x16x32_bf16 v[40:43], v[140:143], v[24:27], v[40:43]\n"
    "ds_read_b128 v[24:27], v235 offset:6144\n"
    "s_waitcnt lgkmcnt(8)\n"
    "v_mfma_f32_16x16x32_bf16 v[44:47], v[140:143], v[28:31], v[44:47]\n"
    "ds_read_b128 v[28:31], v235 offset:26624\n"
    "s_waitcnt lgkmcnt(7)\n"
    "v_mfma_f32_16x16x32_bf16 v[32:35], v[144:147], v[0:3], v[32:35]\n"
    "buffer_load_dwordx4 v[124:127], v255, s[4:7], s74 offen sc1\n"
    "v_mfma_f32_16x16x32_bf16 v[48:51], v[148:151], v[0:3], v[48:51]\n"
    "ds_read_b128 v[0:3], v232 offset:8192\n"
    "s_waitcnt lgkmcnt(7)\n"
    "v_mfma_f32_16x16x32_bf16 v[36:39], v[144:147], v[4:7], v[36:39]\n"
    "v_mfma_f32_16x16x32_bf16 v[52:55], v[148:151], v[4:7], v[52:55]\n"
    "ds_read_b128 v[4:7], v232 offset:28672\n"
    "s_waitcnt lgkmcnt(7)\n"
    "v_mfma_f32_16x16x32_bf16 v[40:43], v[144:147], v[8:11], v[40:43]\n"
    "v_mfma_f32_16x16x32_bf16 v[56:59], v[148:151], v[8:11], v[56:59]\n"
    "ds_read_b128 v[8:11], v234 offset:8192\n"
    "s_waitcnt lgkmcnt(7)\n"
    "v_mfma_f32_16x16x32_bf16 v[44:47], v[144:147], v[12:15], v[44:47]\n"
    "buffer_load_dwordx4 v[128:131], v255, s[4:7], s74 offen offset:1024 sc1\n"
    "v_mfma_f32_16x16x32_bf16 v[60:63], v[148:151], v[12:15], v[60:63]\n"
    "ds_read_b128 v[12:15], v234 offset:28672\n"
    "s_waitcnt lgkmcnt(7)\n"
    "v_mfma_f32_16x16x32_bf16 v[32:35], v[152:155], v[16:19], v[32:35]\n"
    "ds_read_b128 v[16:19], v233 offset:8192\n"
    "s_waitcnt lgkmcnt(7)\n"
    "v_mfma_f32_16x16x32_bf16 v[36:39], v[152:155], v[20:23], v[36:39]\n"
    "ds_read_b128 v[20:23], v233 offset:28672\n"
    "s_waitcnt lgkmcnt(7)\n"
    "v_mfma_f32_16x16x32_bf16 v[40:43], v[152:155], v[24:27], v[40:43]\n"
    "ds_read_b128 v[24:27], v235 offset:8192\n"
    "s_waitcnt lgkmcnt(7)\n"
    "v_mfma_f32_16x16x32_bf16 v[44:47], v[152:155], v[28:31], v[44:47]\n"
    "ds_read_b128 v[28:31], v235 offset:28672\n"
    "s_waitcnt lgkmcnt(7)\n"
    "v_mfma_f32_16x16x32_bf16 v[32:35], v[156:159], v[0:3], v[32:35]\n"
    "buffer_load_dwordx4 v[132:135], v255, s[4:7], s74 offen offset:2048 sc1\n"
    "v_mfma_f32_16x16x32_bf16 v[48:51], v[160:163], v[0:3], v[48:51]\n"
    "ds_read_b128 v[0:3], v232 offset:10240\n"
    "s_waitcnt lgkmcnt(7)\n"
    "v_mfma_f32_16x16x32_bf16 v[36:39], v[156:159], v[4:7], v[36:39]\n"
    "v_mfma_f32_16x16x32_bf16 v[52:55], v[160:163], v[4:7], v[52:55]\n"
    "ds_read_b128 v[4:7], v232 offset:30720\n"
    "s_waitcnt lgkmcnt(7)\n"
    "v_mfma_f32_16x16x32_bf16 v[40:43], v[156:159], v[8:11], v[40:43]\n"
    "v_mfma_f32_16x16x32_bf16 v[56:59], v[160:163], v[8:11], v[56:59]\n"
    "ds_read_b128 v[8:11], v234 offset:10240\n"
    "s_waitcnt lgkmcnt(7)\n"
    "v_mfma_f32_16x16x32_bf16 v[44:47], v[156:159], v[12:15], v[44:47]\n"
    "buffer_load_dwordx4 v[136:139], v255, s[4:7], s74 offen offset:3072 sc1\n"
    "s_add_u32 s74, s74, 4096\n"
    "v_mfma_f32_16x16x32_bf16 v[60:63], v[160:163], v[12:15], v[60:63]\n"
    "ds_read_b128 v[12:15], v234 offset:30720\n"
    "s_waitcnt lgkmcnt(7)\n"
    "v_mfma_f32_16x16x32_bf16 v[32:35], v[164:167], v[16:19], v[32:35]\n"
    "ds_read_b128 v[16:19], v233 offset:10240\n"
    "s_waitcnt lgkmcnt(7)\n"
    "v_mfma_f32_16x16x32_bf16 v[36:39], v[164:167], v[20:23], v[36:39]\n"
    "ds_read_b128 v[20:23], v233 offset:30720\n"
    "s_waitcnt lgkmcnt(7)\n"
    "v_mfma_f32_16x16x32_bf16 v[40:43], v[164:167], v[24:27], v[40:43]\n"
    "ds_read_b128 v[24:27], v235 offset:10240\n"
    "s_waitcnt lgkmcnt(7)\n"
    "v_mfma_f32_16x16x32_bf16 v[44:47], v[164:167], v[28:31], v[44:47]\n"
    "ds_read_b128 v[28:31], v235 offset:30720\n"
    "s_waitcnt lgkmcnt(7)\n"
    "v_mfma_f32_16x16x32_bf16 v[32:35], v[168:171], v[0:3], v[32:35]\n"
    "buffer_load_dwordx4 v[140:143], v255, s[4:7], s74 offen sc1\n"
    "v_mfma_f32_16x16x32_bf16 v[48:51], v[172:175], v[0:3], v[48:51]\n"
    "ds_read_b128 v[0:3], v232 offset:12288\n"
    "s_waitcnt lgkmcnt(7)\n"
    "v_mfma_f32_16x16x32_bf16 v[36:39], v[168:171], v[4:7], v[36:39]\n"
    "v_mfma_f32_16x16x32_bf16 v[52:55], v[172:175], v[4:7], v[52:55]\n"
    "ds_read_b128 v[4:7], v232 offset:32768\n"
    "s_waitcnt lgkmcnt(7)\n"
    "v_mfma_f32_16x16x32_bf16 v[40:43], v[168:171], v[8:11], v[40:43]\n"
    "v_mfma_f32_16x16x32_bf16 v[56:59], v[172:175], v[8:11], v[56:59]\n"
    "ds_read_b128 v[8:11], v234 offset:12288\n"
    "s_waitcnt lgkmcnt(7)\n"
    "v_mfma_f32_16x16x32_bf16 v[44:47], v[168:171], v[12:15], v[44:47]\n"
    "buffer_load_dwordx4 v[144:147], v255, s[4:7], s74 offen offset:1024 sc1\n"
    "v_mfma_f32_16x16x32_bf16 v[60:63], v[172:175], v[12:15], v[60:63]\n"
    "ds_read_b128 v[12:15], v234 offset:32768\n"
    "s_waitcnt lgkmcnt(7)\n"
    "v_mfma_f32_16x16x32_bf16 v[32:35], v[176:179], v[16:19], v[32:35]\n"
    "ds_read_b128 v[16:19], v233 offset:12288\n"
    "s_waitcnt lgkmcnt(7)\n"
    "v_mfma_f32_16x16x32_bf16 v[36:39], v[176:179], v[20:23], v[36:39]\n"
    "ds_read_b128 v[20:23], v233 offset:32768\n"
    "s_waitcnt lgkmcnt(7)\n"
    "v_mfma_f32_16x16x32_bf16 v[40:43], v[176:179], v[24:27], v[40:43]\n"
    "ds_read_b128 v[24:27], v235 offset:12288\n"
    "s_waitcnt lgkmcnt(7)\n"
    "v_mfma_f32_16x16x32_bf16 v[44:47], v[176:179], v[28:31], v[44:47]\n"
    "ds_read_b128 v[28:31], v235 offset:32768\n"
    "s_waitcnt lgkmcnt(7)\n"
    "v_mfma_f32_16x16x32_bf16 v[32:35], v[180:183], v[0:3], v[32:35]\n"
    "buffer_load_dwordx4 v[148:151], v255, s[4:7], s74 offen offset:2048 sc1\n"
    "v_mfma_f32_16x16x32_bf16 v[48:51], v[184:187], v[0:3], v[48:51]\n"
    "ds_read_b128 v[0:3], v232 offset:14336\n"
    "s_waitcnt lgkmcnt(7)\n"
    "v_mfma_f32_16x16x32_bf16 v[36:39], v[180:183], v[4:7], v[36:39]\n"
    "v_mfma_f32_16x16x32_bf16 v[52:55], v[184:187], v[4:7], v[52:55]\n"
    "ds_read_b128 v[4:7], v232 offset:34816\n"
    "s_waitcnt lgkmcnt(7)\n"
    "v_mfma_f32_16x16x32_bf16 v[40:43], v[180:183], v[8:11], v[40:43]\n"
    "v_mfma_f32_16x16x32_bf16 v[56:59], v[184:187], v[8:11], v[56:59]\n"
    "ds_read_b128 v[8:11], v234 offset:14336\n"
    "s_waitcnt lgkmcnt(7)\n"
    "v_mfma_f32_16x16x32_bf16 v[44:47], v[180:183], v[12:15], v[44:47]\n"
    "buffer_load_dwordx4 v[152:155], v255, s[4:7], s74 offen offset:3072 sc1\n"
    "s_add_u32 s74, s74, 4096\n"
    "v_mfma_f32_16x16x32_bf16 v[60:63], v[184:187], v[12:15], v[60:63]\n"
    "ds_read_b128 v[12:15], v234 offset:34816\n"
    "s_waitcnt lgkmcnt(7)\n"
    "v_mfma_f32_16x16x32_bf16 v[32:35], v[188:191], v[16:19], v[32:35]\n"
    "ds_read_b128 v[16:19], v233 offset:14336\n"
    "s_waitcnt lgkmcnt(7)\n"
    "v_mfma_f32_16x16x32_bf16 v[36:39], v[188:191], v[20:23], v[36:39]\n"
    "ds_read_b128 v[20:23], v233 offset:34816\n"
    "s_waitcnt lgkmcnt(7)\n"
    "v_mfma_f32_16x16x32_bf16 v[40:43], v[188:191], v[24:27], v[40:43]\n"
    "ds_read_b128 v[24:27], v235 offset:14336\n"
    "s_waitcnt lgkmcnt(7)\n"
    "v_mfma_f32_16x16x32_bf16 v[44:47], v[188:191], v[28:31], v[44:47]\n"
    "ds_read_b128 v[28:31], v235 offset:34816\n"
    "s_waitcnt lgkmcnt(7)\n"
    "v_mfma_f32_16x16x32_bf16 v[32:35], v[192:195], v[0:3], v[32:35]\n"
    "v_lshl_add_u32 v180, v249, 4, s60\n"
    "buffer_load_dwordx4 v[156:159], v255, s[4:7], s74 offen sc1\n"
    "v_mfma_f32_16x16x32_bf16 v[48:51], v[196:199], v[0:3], v[48:51]\n"
    "v_lshl_add_u32 v181, v249, 4, s61\n"
    "ds_read_b128 v[0:3], v232 offset:16384\n"
    "s_waitcnt lgkmcnt(7)\n"
    "v_mfma_f32_16x16x32_bf16 v[36:39], v[192:195], v[4:7], v[36:39]\n"
    "v_lshl_add_u32 v182, v248, 4, s62\n"
    "v_mfma_f32_16x16x32_bf16 v[52:55], v[196:199], v[4:7], v[52:55]\n"
    "v_lshl_add_u32 v183, v248, 4, s63\n"
    "ds_read_b128 v[4:7], v232 offset:36864\n"
    "s_waitcnt lgkmcnt(7)\n"
    "v_mfma_f32_16x16x32_bf16 v[40:43], v[192:195], v[8:11], v[40:43]\n"
    "v_mov_b32 v184, 0xc0135761\n"
    "v_mfma_f32_16x16x32_bf16 v[56:59], v[196:199], v[8:11], v[56:59]\n"
    "v_mov_b32 v185, 0xc0135761\n"
    "ds_read_b128 v[8:11], v234 offset:16384\n"
    "s_waitcnt lgkmcnt(7)\n"
    "v_mfma_f32_16x16x32_bf16 v[44:47], v[192:195], v[12:15], v[44:47]\n"
    "v_mov_b32 v186, 0xbdd2d3e8\n"
    "buffer_load_dwordx4 v[160:163], v237, s[4:7], s74 offen offset:1024 sc1\n"
    "v_mfma_f32_16x16x32_bf16 v[60:63], v[196:199], v[12:15], v[60:63]\n"
    "v_mov_b32 v187, 0xbdd2d3e8\n"
    "ds_read_b128 v[12:15], v234 offset:36864\n"
    "s_waitcnt lgkmcnt(7)\n"
    "v_mfma_f32_16x16x32_bf16 v[32:35], v[200:203], v[16:19], v[32:35]\n"
    "v_mov_b32 v188, 0x3f800000\n"
    "ds_read_b128 v[16:19], v233 offset:16384\n"
    "s_waitcnt lgkmcnt(7)\n"
    "v_mfma_f32_16x16x32_bf16 v[36:39], v[200:203], v[20:23], v[36:39]\n"
    "v_mov_b32 v189, 0x3f800000\n"
    "ds_read_b128 v[20:23], v233 offset:36864\n"
    "s_waitcnt lgkmcnt(7)\n"
    "v_mfma_f32_16x16x32_bf16 v[40:43], v[200:203], v[24:27], v[40:43]\n"
    "ds_read_b32 v190, v254 offset:144\n"
    "ds_read_b128 v[24:27], v235 offset:16384\n"
    "s_waitcnt lgkmcnt(8)\n"
    "v_mfma_f32_16x16x32_bf16 v[44:47], v[200:203], v[28:31], v[44:47]\n"
    "ds_read_b128 v[28:31], v235 offset:36864\n"
    "s_waitcnt lgkmcnt(8)\n"
    "v_mfma_f32_16x16x32_bf16 v[32:35], v[204:207], v[0:3], v[32:35]\n"
    "buffer_load_dwordx4 v[164:167], v237, s[4:7], s74 offen offset:2048 sc1\n"
    "v_mfma_f32_16x16x32_bf16 v[48:51], v[208:211], v[0:3], v[48:51]\n"
    "ds_read_b128 v[0:3], v232 offset:18432\n"
    "s_waitcnt lgkmcnt(8)\n"
    "v_mfma_f32_16x16x32_bf16 v[36:39], v[204:207], v[4:7], v[36:39]\n"
    "v_mfma_f32_16x16x32_bf16 v[52:55], v[208:211], v[4:7], v[52:55]\n"
    "ds_read_b128 v[4:7], v232 offset:38912\n"
    "s_waitcnt lgkmcnt(8)\n"
    "v_mfma_f32_16x16x32_bf16 v[40:43], v[204:207], v[8:11], v[40:43]\n"
    "v_mfma_f32_16x16x32_bf16 v[56:59], v[208:211], v[8:11], v[56:59]\n"
    "ds_read_b128 v[8:11], v234 offset:18432\n"
    "s_waitcnt lgkmcnt(8)\n"
    "v_mfma_f32_16x16x32_bf16 v[44:47], v[204:207], v[12:15], v[44:47]\n"
    "buffer_load_dwordx4 v[168:171], v237, s[4:7], s74 offen offset:3072 sc1\n"
    "s_add_u32 s74, s74, 4096\n"
    "v_mfma_f32_16x16x32_bf16 v[60:63], v[208:211], v[12:15], v[60:63]\n"
    "ds_read_b128 v[12:15], v234 offset:38912\n"
    "s_waitcnt lgkmcnt(8)\n"
    "v_mfma_f32_16x16x32_bf16 v[32:35], v[212:215], v[16:19], v[32:35]\n"
    "ds_read_b128 v[16:19], v233 offset:18432\n"
    "s_waitcnt lgkmcnt(8)\n"
    "v_mfma_f32_16x16x32_bf16 v[36:39], v[212:215], v[20:23], v[36:39]\n"
    "ds_read_b128 v[20:23], v233 offset:38912\n"
    "s_waitcnt lgkmcnt(7)\n"
    "v_mfma_f32_16x16x32_bf16 v[40:43], v[212:215], v[24:27], v[40:43]\n"
    "ds_read_b128 v[24:27], v235 offset:18432\n"
    "s_waitcnt lgkmcnt(7)\n"
    "v_mfma_f32_16x16x32_bf16 v[44:47], v[212:215], v[28:31], v[44:47]\n"
    "ds_read_b128 v[28:31], v235 offset:38912\n"
    "v_readfirstlane_b32 s71, v190\n"
    "s_cmp_ge_u32 s71, 4\n"
    "s_cbranch_scc0 .Lk_w128\n"
    ".Lk_d129:\n"
    "s_waitcnt lgkmcnt(7)\n"
    "v_mfma_f32_16x16x32_bf16 v[32:35], v[216:219], v[0:3], v[32:35]\n"
    "ds_read_b128 v[192:195], v182\n"
    "buffer_load_dwordx4 v[172:175], v237, s[4:7], s74 offen sc1\n"
    "v_mfma_f32_16x16x32_bf16 v[48:51], v[220:223], v[0:3], v[48:51]\n"
    "ds_read_b128 v[196:199], v182 offset:256\n"
    "s_waitcnt lgkmcnt(8)\n"
    "v_mfma_f32_16x16x32_bf16 v[36:39], v[216:219], v[4:7], v[36:39]\n"
    "ds_read_b128 v[200:203], v183\n"
    "v_mfma_f32_16x16x32_bf16 v[52:55], v[220:223], v[4:7], v[52:55]\n"
    "ds_read_b128 v[204:207], v183 offset:256\n"
    "s_waitcnt lgkmcnt(9)\n"
    "v_mfma_f32_16x16x32_bf16 v[40:43], v[216:219], v[8:11], v[40:43]\n"
    "ds_read_b128 v[208:211], v180\n"
    "v_mfma_f32_16x16x32_bf16 v[56:59], v[220:223], v[8:11], v[56:59]\n"
    "ds_read_b128 v[212:215], v180 offset:1024\n"
    "s_waitcnt lgkmcnt(10)\n"
    "v_mfma_f32_16x16x32_bf16 v[44:47], v[216:219], v[12:15], v[44:47]\n"
    "buffer_load_dwordx4 v[176:179], v237, s[4:7], s74 offen offset:1024 sc1\n"
    "v_mfma_f32_16x16x32_bf16 v[60:63], v[220:223], v[12:15], v[60:63]\n"
    "s_waitcnt lgkmcnt(9)\n"
    "v_mfma_f32_16x16x32_bf16 v[32:35], v[224:227], v[16:19], v[32:35]\n"
    "ds_read_b128 v[216:219], v181\n"
    "s_waitcnt lgkmcnt(9)\n"
    "v_mfma_f32_16x16x32_bf16 v[36:39], v[224:227], v[20:23], v[36:39]\n"
    "ds_read_b128 v[220:223], v181 offset:1024\n"
    "s_waitcnt lgkmcnt(9)\n"
    "v_mfma_f32_16x16x32_bf16 v[40:43], v[224:227], v[24:27], v[40:43]\n"
    "s_waitcnt lgkmcnt(8)\n"
    "v_mfma_f32_16x16x32_bf16 v[44:47], v[224:227], v[28:31], v[44:47]\n"
    "ds_write_b128 v239, v[56:59]\n"
    "ds_write_b128 v239, v[60:63] offset:1024\n"
    "ds_add_u32 v247, v253 offset:148\n"
    "v_add_u32 v180, s56, v236\n"
    "v_add_u32 v181, s57, v236\n"
    "v_add_u32 v182, s58, v236\n"
    "ds_read_b32 v190, v247 offset:148\n"
    "s_waitcnt lgkmcnt(6)\n"
    "s_nop 0\n"
    "v_pk_fma_f32 v[0:1], v[192:193], v[208:209], v[212:213] neg_lo:[1,0,0] neg_hi:[1,0,0]\n"
    "v_pk_fma_f32 v[2:3], v[192:193], v[210:211], v[214:215] neg_lo:[1,0,0] neg_hi:[1,0,0]\n"
    "v_pk_fma_f32 v[4:5], v[196:197], v[208:209], v[212:213] neg_lo:[1,0,0] neg_hi:[1,0,0]\n"
    "v_pk_fma_f32 v[6:7], v[196:197], v[210:211], v[214:215] neg_lo:[1,0,0] neg_hi:[1,0,0]\n"
    "v_pk_fma_f32 v[0:1], v[194:195], v[32:33], v[0:1]\n"
    "v_pk_fma_f32 v[2:3], v[194:195], v[34:35], v[2:3]\n"
    "v_pk_fma_f32 v[4:5], v[198:199], v[36:37], v[4:5]\n"
    "v_pk_fma_f32 v[6:7], v[198:199], v[38:39], v[6:7]\n"
    "v_pk_mul_f32 v[8:9], v[0:1], v[0:1]\n"
    "v_pk_mul_f32 v[10:11], v[2:3], v[2:3]\n"
    "v_pk_mul_f32 v[12:13], v[4:5], v[4:5]\n"
    "v_pk_mul_f32 v[14:15], v[6:7], v[6:7]\n"
    "v_pk_fma_f32 v[8:9], v[8:9], v[186:187], v[184:185]\n"
    "v_pk_fma_f32 v[10:11], v[10:11], v[186:187], v[184:185]\n"
    "v_pk_fma_f32 v[12:13], v[12:13], v[186:187], v[184:185]\n"
    "v_pk_fma_f32 v[14:15], v[14:15], v[186:187], v[184:185]\n"
    "v_pk_mul_f32 v[8:9], v[8:9], v[0:1]\n"
    "v_pk_mul_f32 v[10:11], v[10:11], v[2:3]\n"
    "v_pk_mul_f32 v[12:13], v[12:13], v[4:5]\n"
    "v_pk_mul_f32 v[14:15], v[14:15], v[6:7]\n"
    "v_exp_f32 v8, v8\n"
    "v_exp_f32 v9, v9\n"
    "v_exp_f32 v10, v10\n"
    "v_exp_f32 v11, v11\n"
    "v_exp_f32 v12, v12\n"
    "v_exp_f32 v13, v13\n"
    "v_exp_f32 v14, v14\n"
    "v_exp_f32 v15, v15\n"
    "v_pk_add_f32 v[8:9], v[8:9], v[188:189]\n"
    "v_pk_add_f32 v[10:11], v[10:11], v[188:189]\n"
    "v_pk_add_f32 v[12:13], v[12:13], v[188:189]\n"
    "v_pk_add_f32 v[14:15], v[14:15], v[188:189]\n"
    "v_rcp_f32 v8, v8\n"
    "v_rcp_f32 v9, v9\n"
    "v_rcp_f32 v10, v10\n"
    "v_rcp_f32 v11, v11\n"
    "v_rcp_f32 v12, v12\n"
    "v_rcp_f32 v13, v13\n"
    "v_rcp_f32 v14, v14\n"
    "v_rcp_f32 v15, v15\n"
    "v_pk_mul_f32 v[0:1], v[0:1], v[8:9]\n"
    "v_pk_mul_f32 v[2:3], v[2:3], v[10:11]\n"
    "v_pk_mul_f32 v[4:5], v[4:5], v[12:13]\n"
    "v_pk_mul_f32 v[6:7], v[6:7], v[14:15]\n"
    "v_cvt_pk_bf16_f32 v16, v0, v1\n"
    "v_cvt_pk_bf16_f32 v17, v2, v3\n"
    "v_cvt_pk_bf16_f32 v18, v4, v5\n"
    "v_cvt_pk_bf16_f32 v19, v6, v7\n"
    "s_nop 0\n"
    "v_permlane16_swap_b32 v16, v18\n"
    "v_permlane16_swap_b32 v17, v19\n"
    "buffer_store_dwordx4 v[16:19], v180, s[8:11], 0 offen sc1\n"
    "s_waitcnt lgkmcnt(0)\n"
    "v_readfirstlane_b32 s71, v190\n"
    "s_cmp_ge_u32 s71, 2\n"
    "s_cbranch_scc0 .Lk_w130\n"
    ".Lk_d131:\n"
    "s_nop 0\n"
    "ds_read_b128 v[56:59], v246\n"
    "ds_read_b128 v[60:63], v246 offset:1024\n"
    "v_pk_fma_f32 v[0:1], v[200:201], v[208:209], v[212:213] neg_lo:[1,0,0] neg_hi:[1,0,0]\n"
    "v_pk_fma_f32 v[2:3], v[200:201], v[210:211], v[214:215] neg_lo:[1,0,0] neg_hi:[1,0,0]\n"
    "v_pk_fma_f32 v[4:5], v[204:205], v[208:209], v[212:213] neg_lo:[1,0,0] neg_hi:[1,0,0]\n"
    "v_pk_fma_f32 v[6:7], v[204:205], v[210:211], v[214:215] neg_lo:[1,0,0] neg_hi:[1,0,0]\n"
    "v_pk_fma_f32 v[0:1], v[202:203], v[40:41], v[0:1]\n"
    "v_pk_fma_f32 v[2:3], v[202:203], v[42:43], v[2:3]\n"
    "v_pk_fma_f32 v[4:5], v[206:207], v[44:45], v[4:5]\n"
    "v_pk_fma_f32 v[6:7], v[206:207], v[46:47], v[6:7]\n"
    "v_pk_mul_f32 v[8:9], v[0:1], v[0:1]\n"
    "v_pk_mul_f32 v[10:11], v[2:3], v[2:3]\n"
    "v_pk_mul_f32 v[12:13], v[4:5], v[4:5]\n"
    "v_pk_mul_f32 v[14:15], v[6:7], v[6:7]\n"
    "v_pk_fma_f32 v[8:9], v[8:9], v[186:187], v[184:185]\n"
    "v_pk_fma_f32 v[10:11], v[10:11], v[186:187], v[184:185]\n"
    "v_pk_fma_f32 v[12:13], v[12:13], v[186:187], v[184:185]\n"
    "v_pk_fma_f32 v[14:15], v[14:15], v[186:187], v[184:185]\n"
    "v_pk_mul_f32 v[8:9], v[8:9], v[0:1]\n"
    "v_pk_mul_f32 v[10:11], v[10:11], v[2:3]\n"
    "v_pk_mul_f32 v[12:13], v[12:13], v[4:5]\n"
    "v_pk_mul_f32 v[14:15], v[14:15], v[6:7]\n"
    "v_exp_f32 v8, v8\n"
    "v_exp_f32 v9, v9\n"
    "v_exp_f32 v10, v10\n"
    "v_exp_f32 v11, v11\n"
    "v_exp_f32 v12, v12\n"
    "v_exp_f32 v13, v13\n"
    "v_exp_f32 v14, v14\n"
    "v_exp_f32 v15, v15\n"
    "v_pk_add_f32 v[8:9], v[8:9], v[188:189]\n"
    "v_pk_add_f32 v[10:11], v[10:11], v[188:189]\n"
    "v_pk_add_f32 v[12:13], v[12:13], v[188:189]\n"
    "v_pk_add_f32 v[14:15], v[14:15], v[188:189]\n"
    "v_rcp_f32 v8, v8\n"
    "v_rcp_f32 v9, v9\n"
    "v_rcp_f32 v10, v10\n"
    "v_rcp_f32 v11, v11\n"
    "v_rcp_f32 v12, v12\n"
    "v_rcp_f32 v13, v13\n"
    "v_rcp_f32 v14, v14\n"
    "v_rcp_f32 v15, v15\n"
    "v_pk_mul_f32 v[0:1], v[0:1], v[8:9]\n"
    "v_pk_mul_f32 v[2:3], v[2:3], v[10:11]\n"
    "v_pk_mul_f32 v[4:5], v[4:5], v[12:13]\n"
    "v_pk_mul_f32 v[6:7], v[6:7], v[14:15]\n"
    "v_cvt_pk_bf16_f32 v20, v0, v1\n"
    "v_cvt_pk_bf16_f32 v21, v2, v3\n"
    "v_cvt_pk_bf16_f32 v22, v4, v5\n"
    "v_cvt_pk_bf16_f32 v23, v6, v7\n"
    "s_nop 0\n"
    "v_permlane16_swap_b32 v20, v22\n"
    "v_permlane16_swap_b32 v21, v23\n"
    "buffer_store_dwordx4 v[20:23], v181, s[8:11], 0 offen sc1\n"
    "s_waitcnt lgkmcnt(1)\n"
    "v_pk_add_f32 v[48:49], v[48:49], v[56:57]\n"
    "v_pk_add_f32 v[50:51], v[50:51], v[58:59]\n"
    "s_waitcnt lgkmcnt(0)\n"
    "s_nop 0\n"
    "v_pk_add_f32 v[52:53], v[52:53], v[60:61]\n"
    "v_pk_add_f32 v[54:55], v[54:55], v[62:63]\n"
    "v_pk_fma_f32 v[0:1], v[192:193], v[216:217], v[220:221] neg_lo:[1,0,0] neg_hi:[1,0,0]\n"
    "v_pk_fma_f32 v[2:3], v[192:193], v[218:219], v[222:223] neg_lo:[1,0,0] neg_hi:[1,0,0]\n"
    "v_pk_fma_f32 v[4:5], v[196:197], v[216:217], v[220:221] neg_lo:[1,0,0] neg_hi:[1,0,0]\n"
    "v_pk_fma_f32 v[6:7], v[196:197], v[218:219], v[222:223] neg_lo:[1,0,0] neg_hi:[1,0,0]\n"
    "v_pk_fma_f32 v[0:1], v[194:195], v[48:49], v[0:1]\n"
    "v_pk_fma_f32 v[2:3], v[194:195], v[50:51], v[2:3]\n"
    "v_pk_fma_f32 v[4:5], v[198:199], v[52:53], v[4:5]\n"
    "v_pk_fma_f32 v[6:7], v[198:199], v[54:55], v[6:7]\n"
    "v_pk_mul_f32 v[8:9], v[0:1], v[0:1]\n"
    "v_pk_mul_f32 v[10:11], v[2:3], v[2:3]\n"
    "v_pk_mul_f32 v[12:13], v[4:5], v[4:5]\n"
    "v_pk_mul_f32 v[14:15], v[6:7], v[6:7]\n"
    "v_pk_fma_f32 v[8:9], v[8:9], v[186:187], v[184:185]\n"
    "v_pk_fma_f32 v[10:11], v[10:11], v[186:187], v[184:185]\n"
    "v_pk_fma_f32 v[12:13], v[12:13], v[186:187], v[184:185]\n"
    "v_pk_fma_f32 v[14:15], v[14:15], v[186:187], v[184:185]\n"
    "v_pk_mul_f32 v[8:9], v[8:9], v[0:1]\n"
    "v_pk_mul_f32 v[10:11], v[10:11], v[2:3]\n"
    "v_pk_mul_f32 v[12:13], v[12:13], v[4:5]\n"
    "v_pk_mul_f32 v[14:15], v[14:15], v[6:7]\n"
    "v_exp_f32 v8, v8\n"
    "v_exp_f32 v9, v9\n"
    "v_exp_f32 v10, v10\n"
    "v_exp_f32 v11, v11\n"
    "v_exp_f32 v12, v12\n"
    "v_exp_f32 v13, v13\n"
    "v_exp_f32 v14, v14\n"
    "v_exp_f32 v15, v15\n"
    "v_pk_add_f32 v[8:9], v[8:9], v[188:189]\n"
    "v_pk_add_f32 v[10:11], v[10:11], v[188:189]\n"
    "v_pk_add_f32 v[12:13], v[12:13], v[188:189]\n"
    "v_pk_add_f32 v[14:15], v[14:15], v[188:189]\n"
    "v_rcp_f32 v8, v8\n"
    "v_rcp_f32 v9, v9\n"
    "v_rcp_f32 v10, v10\n"
    "v_rcp_f32 v11, v11\n"
    "v_rcp_f32 v12, v12\n"
    "v_rcp_f32 v13, v13\n"
    "v_rcp_f32 v14, v14\n"
    "v_rcp_f32 v15, v15\n"
    "v_pk_mul_f32 v[0:1], v[0:1], v[8:9]\n"
    "v_pk_mul_f32 v[2:3], v[2:3], v[10:11]\n"
    "v_pk_mul_f32 v[4:5], v[4:5], v[12:13]\n"
    "v_pk_mul_f32 v[6:7], v[6:7], v[14:15]\n"
    "v_cvt_pk_bf16_f32 v24, v0, v1\n"
    "v_cvt_pk_bf16_f32 v25, v2, v3\n"
    "v_cvt_pk_bf16_f32 v26, v4, v5\n"
    "v_cvt_pk_bf16_f32 v27, v6, v7\n"
    "s_nop 0\n"
    "v_permlane16_swap_b32 v24, v26\n"
    "v_permlane16_swap_b32 v25, v27\n"
    "buffer_store_dwordx4 v[24:27], v182, s[8:11], 0 offen sc1\n"
    "ds_add_u32 v254, v253 offset:116\n"
    "s_setprio 3\n"
    "s_waitcnt vmcnt(0)\n"
    "ds_add_u32 v254, v253 offset:64\n"
    "s_cmp_lg_u32 s46, 0\n"
    "s_cbranch_scc1 .Lk_f132\n"
    "s_waitcnt lgkmcnt(0)\n"
    ".Lk_w133:\n"
    "ds_read_b32 v252, v254 offset:64\n"
    "s_waitcnt lgkmcnt(0)\n"
    "v_readfirstlane_b32 s71, v252\n"
    "s_cmp_ge_u32 s71, 4\n"
    "s_cbranch_scc1 .Lk_d134\n"
    "s_sleep 1\n"
    "s_sub_u32 s54, s54, 1\n"
    "s_cbranch_scc0 .Lk_w133\n"
    "s_mov_b32 s54, 0\n"
    ".Lk_d134:\n"
    "buffer_store_dwordx2 v[242:243], v240, s[8:11], 0 offen offset:3072 sc1\n"
    ".Lk_f132:\n"
    "s_lshl_b32 s64, s46, 2\n"
    "v_add_u32 v247, s64, v254\n"
    "v_mul_u32_u24 v236, 160, v248\n"
    "v_add_u32_e64 v236, s72, v236\n"
    "v_lshl_add_u32 v238, v249, 3, v236\n"
    "v_lshl_add_u32 v236, v249, 4, v236\n"
    "v_mov_b32 v250, 0x3f80\n"
    "v_lshrrev_b32 v60, 2, v248\n"
    "v_cmp_eq_u32 vcc, v60, v249\n"
    "v_and_b32 v60, 1, v248\n"
    "v_lshlrev_b32 v60, 4, v60\n"
    "v_lshlrev_b32 v60, v60, v250\n"
    "v_cndmask_b32 v62, 0, v60, vcc\n"
    "v_and_b32 v60, 2, v248\n"
    "v_cmp_eq_u32 vcc, 0, v60\n"
    "s_nop 1\n"
    "v_cndmask_b32 v60, 0, v62, vcc\n"
    "v_cndmask_b32 v61, v62, 0, vcc\n"
    "v_mov_b64_e64 v[62:63], v[60:61]\n"
    "v_mov_b32 v246, 160000\n"
    "v_lshl_add_u32 v250, v249, 4, v246\n"
    "v_lshl_add_u32 v249, v249, 5, v246\n"
    "v_lshrrev_b32 v235, 1, v255\n"
    "v_add_u32 v232, s28, v235\n"
    "v_add_u32 v233, s29, v235\n"
    "v_add_u32 v234, s30, v235\n"
    "v_add_u32 v248, s72, v235\n"
    "s_cmp_lg_u32 s46, 0\n"
    "s_cbranch_scc1 .Lk_n135\n"
    "s_sleep 8\n"
    ".Lk_pg137:\n"
    "buffer_load_dwordx2 v[244:245], v241, s[8:11], 0 offen offset:3072 sc1\n"
    "s_waitcnt vmcnt(0)\n"
    "v_cmp_eq_u64 vcc, v[244:245], v[242:243]\n"
    "s_cmp_eq_u64 vcc, exec\n"
    "s_cbranch_scc1 .Lk_pgd138\n"
    "s_sub_u32 s54, s54, 1\n"
    "s_cbranch_scc0 .Lk_pg137\n"
    "s_mov_b32 s54, 0\n"
    ".Lk_pgd138:\n"
    "ds_add_u32 v254, v253 offset:68\n"
    "s_branch .Lk_j136\n"
    ".Lk_n135:\n"
    "s_sleep 14\n"
    "s_waitcnt lgkmcnt(0)\n"
    ".Lk_w139:\n"
    "ds_read_b32 v252, v254 offset:68\n"
    "s_waitcnt lgkmcnt(0)\n"
    "v_readfirstlane_b32 s71, v252\n"
    "s_cmp_ge_u32 s71, 1\n"
    "s_cbranch_scc1 .Lk_d140\n"
    "s_sleep 1\n"
    "s_sub_u32 s54, s54, 1\n"
    "s_cbranch_scc0 .Lk_w139\n"
    "s_mov_b32 s54, 0\n"
    ".Lk_d140:\n"
    ".Lk_j136:\n"
    "buffer_load_dwordx4 v[180:183], v255, s[8:11], s78 offen sc1\n"
    "buffer_load_dwordx4 v[184:187], v255, s[8:11], s78 offen offset:1024 sc1\n"
    "buffer_load_dwordx4 v[188:191], v255, s[8:11], s78 offen offset:2048 sc1\n"
    "buffer_load_dwordx4 v[192:195], v255, s[8:11], s78 offen offset:3072 sc1\n"
    "buffer_load_dwordx4 v[196:199], v255, s[8:11], s82 offen sc1\n"
    "buffer_load_dwordx4 v[200:203], v237, s[8:11], s82 offen offset:1024 sc1\n"
    "buffer_load_dwordx4 v[204:207], v255, s[8:11], s79 offen sc1\n"
    "buffer_load_dwordx4 v[208:211], v255, s[8:11], s79 offen offset:1024 sc1\n"
    "buffer_load_dwordx4 v[212:215], v255, s[8:11], s79 offen offset:2048 sc1\n"
    "buffer_load_dwordx4 v[216:219], v255, s[8:11], s79 offen offset:3072 sc1\n"
    "buffer_load_dwordx4 v[220:223], v255, s[8:11], s83 offen sc1\n"
    "buffer_load_dwordx4 v[224:227], v237, s[8:11], s83 offen offset:1024 sc1\n"
    "buffer_load_dwordx4 v[40:43], v255, s[8:11], s80 offen sc1\n"
    "buffer_load_dwordx4 v[44:47], v255, s[8:11], s80 offen offset:1024 sc1\n"
    "s_setprio 1\n"
    "s_waitcnt vmcnt(13)\n"
    "v_mfma_f32_16x16x32_bf16 v[0:3], v[64:67], v[180:183], 0\n"
    "v_lshrrev_b32_e64 v240, 4, v255\n"
    "v_mfma_f32_16x16x32_bf16 v[4:7], v[68:71], v[180:183], 0\n"
    "v_add_u32_e64 v240, 0, v240\n"
    "v_mfma_f32_16x16x32_bf16 v[8:11], v[72:75], v[180:183], 0\n"
    "v_mul_u32_u24 v243, 205, v240\n"
    "v_mfma_f32_16x16x32_bf16 v[12:15], v[76:79], v[180:183], 0\n"
    "v_lshrrev_b32_e64 v243, 11, v243\n"
    "v_mfma_f32_16x16x32_bf16 v[16:19], v[80:83], v[180:183], 0\n"
    "v_mul_u32_u24 v243, 1120, v243\n"
    "buffer_load_dwordx4 v[48:51], v255, s[8:11], s80 offen offset:2048 sc1\n"
    "s_waitcnt vmcnt(13)\n"
    "v_mfma_f32_16x16x32_bf16 v[0:3], v[84:87], v[184:187], v[0:3]\n"
    "v_lshl_add_u32 v240, v240, 4, v243\n"
    "v_mfma_f32_16x16x32_bf16 v[4:7], v[88:91], v[184:187], v[4:7]\n"
    "v_add_u32 v240, s77, v240\n"
    "v_mfma_f32_16x16x32_bf16 v[8:11], v[92:95], v[184:187], v[8:11]\n"
    "v_lshrrev_b32_e64 v241, 4, v255\n"
    "v_mfma_f32_16x16x32_bf16 v[12:15], v[96:99], v[184:187], v[12:15]\n"
    "v_add_u32_e64 v241, 64, v241\n"
    "v_mfma_f32_16x16x32_bf16 v[16:19], v[100:103], v[184:187], v[16:19]\n"
    "v_mul_u32_u24 v243, 205, v241\n"
    "buffer_load_dwordx4 v[52:55], v255, s[8:11], s80 offen offset:3072 sc1\n"
    "s_waitcnt vmcnt(13)\n"
    "v_mfma_f32_16x16x32_bf16 v[0:3], v[104:107], v[188:191], v[0:3]\n"
    "v_lshrrev_b32 v243, 11, v243\n"
    "v_mfma_f32_16x16x32_bf16 v[4:7], v[228:231], v[188:191], v[4:7]\n"
    "v_mul_u32_u24 v243, 1120, v243\n"
    "v_mfma_f32_16x16x32_bf16 v[8:11], v[108:111], v[188:191], v[8:11]\n"
    "v_lshl_add_u32 v241, v241, 4, v243\n"
    "v_mfma_f32_16x16x32_bf16 v[12:15], v[112:115], v[188:191], v[12:15]\n"
    "v_add_u32_e64 v241, s77, v241\n"
    "v_mfma_f32_16x16x32_bf16 v[16:19], v[116:119], v[188:191], v[16:19]\n"
    "v_lshrrev_b32_e64 v242, 4, v255\n"
    "buffer_load_dwordx4 v[56:59], v255, s[8:11], s84 offen sc1\n"
    "s_waitcnt vmcnt(13)\n"
    "v_mfma_f32_16x16x32_bf16 v[0:3], v[120:123], v[192:195], v[0:3]\n"
    "v_add_u32 v242, 128, v242\n"
    "v_mfma_f32_16x16x32_bf16 v[4:7], v[124:127], v[192:195], v[4:7]\n"
    "v_mul_u32_u24 v243, 205, v242\n"
    "v_mfma_f32_16x16x32_bf16 v[8:11], v[128:131], v[192:195], v[8:11]\n"
    "v_lshrrev_b32 v243, 11, v243\n"
    "v_mfma_f32_16x16x32_bf16 v[12:15], v[132:135], v[192:195], v[12:15]\n"
    "v_mul_u32_u24 v243, 1120, v243\n"
    "v_mfma_f32_16x16x32_bf16 v[16:19], v[136:139], v[192:195], v[16:19]\n"
    "v_lshl_add_u32 v242, v242, 4, v243\n"
    "buffer_load_dwordx4 v[180:183], v237, s[8:11], s84 offen offset:1024 sc1\n"
    "s_waitcnt vmcnt(13)\n"
    "v_mfma_f32_16x16x32_bf16 v[0:3], v[140:143], v[196:199], v[0:3]\n"
    "v_add_u32 v242, s77, v242\n"
    "v_mfma_f32_16x16x32_bf16 v[4:7], v[144:147], v[196:199], v[4:7]\n"
    "v_mfma_f32_16x16x32_bf16 v[8:11], v[148:151], v[196:199], v[8:11]\n"
    "v_mfma_f32_16x16x32_bf16 v[12:15], v[152:155], v[196:199], v[12:15]\n"
    "v_mfma_f32_16x16x32_bf16 v[16:19], v[156:159], v[196:199], v[16:19]\n"
    "buffer_load_dwordx4 v[184:187], v255, s[8:11], s81 offen sc1\n"
    "s_waitcnt vmcnt(13)\n"
    "v_mfma_f32_16x16x32_bf16 v[0:3], v[160:163], v[200:203], v[0:3]\n"
    "v_mfma_f32_16x16x32_bf16 v[4:7], v[164:167], v[200:203], v[4:7]\n"
    "v_mfma_f32_16x16x32_bf16 v[8:11], v[168:171], v[200:203], v[8:11]\n"
    "v_mfma_f32_16x16x32_bf16 v[12:15], v[172:175], v[200:203], v[12:15]\n"
    "v_mfma_f32_16x16x32_bf16 v[16:19], v[176:179], v[200:203], v[16:19]\n"
    "buffer_load_dwordx4 v[188:191], v255, s[8:11], s81 offen offset:1024 sc1\n"
    "s_waitcnt vmcnt(13)\n"
    "v_mfma_f32_16x16x32_bf16 v[20:23], v[64:67], v[204:207], 0\n"
    "s_nop 0\n"
    "v_mfma_f32_16x16x32_bf16 v[24:27], v[68:71], v[204:207], 0\n"
    "s_nop 0\n"
    "v_mfma_f32_16x16x32_bf16 v[28:31], v[72:75], v[204:207], 0\n"
    "v_cvt_pk_bf16_f32 v0, v0, v1\n"
    "v_mfma_f32_16x16x32_bf16 v[32:35], v[76:79], v[204:207], 0\n"
    "v_cvt_pk_bf16_f32 v1, v2, v3\n"
    "v_mfma_f32_16x16x32_bf16 v[36:39], v[80:83], v[204:207], 0\n"
    "ds_write_b64 v232, v[0:1]\n"
    "buffer_load_dwordx4 v[192:195], v255, s[8:11], s81 offen offset:2048 sc1\n"
    "s_waitcnt vmcnt(13)\n"
    "s_nop 0\n"
    "v_mfma_f32_16x16x32_bf16 v[20:23], v[84:87], v[208:211], v[20:23]\n"
    "v_cvt_pk_bf16_f32 v4, v4, v5\n"
    "v_mfma_f32_16x16x32_bf16 v[24:27], v[88:91], v[208:211], v[24:27]\n"
    "v_cvt_pk_bf16_f32 v5, v6, v7\n"
    "v_mfma_f32_16x16x32_bf16 v[28:31], v[92:95], v[208:211], v[28:31]\n"
    "ds_write_b64 v232, v[4:5] offset:4096\n"
    "v_mfma_f32_16x16x32_bf16 v[32:35], v[96:99], v[208:211], v[32:35]\n"
    "v_cvt_pk_bf16_f32 v8, v8, v9\n"
    "v_mfma_f32_16x16x32_bf16 v[36:39], v[100:103], v[208:211], v[36:39]\n"
    "v_cvt_pk_bf16_f32 v9, v10, v11\n"
    "buffer_load_dwordx4 v[196:199], v255, s[8:11], s81 offen offset:3072 sc1\n"
    "s_waitcnt vmcnt(13)\n"
    "s_nop 0\n"
    "v_mfma_f32_16x16x32_bf16 v[20:23], v[104:107], v[212:215], v[20:23]\n"
    "ds_write_b64 v232, v[8:9] offset:8192\n"
    "v_mfma_f32_16x16x32_bf16 v[24:27], v[228:231], v[212:215], v[24:27]\n"
    "v_cvt_pk_bf16_f32 v12, v12, v13\n"
    "v_mfma_f32_16x16x32_bf16 v[28:31], v[108:111], v[212:215], v[28:31]\n"
    "v_cvt_pk_bf16_f32 v13, v14, v15\n"
    "v_mfma_f32_16x16x32_bf16 v[32:35], v[112:115], v[212:215], v[32:35]\n"
    "ds_write_b64 v232, v[12:13] offset:12288\n"
    "v_mfma_f32_16x16x32_bf16 v[36:39], v[116:119], v[212:215], v[36:39]\n"
    "v_cvt_pk_bf16_f32 v16, v16, v17\n"
    "buffer_load_dwordx4 v[200:203], v255, s[8:11], s85 offen sc1\n"
    "s_waitcnt vmcnt(13)\n"
    "v_mfma_f32_16x16x32_bf16 v[20:23], v[120:123], v[216:219], v[20:23]\n"
    "v_cvt_pk_bf16_f32 v17, v18, v19\n"
    "v_mfma_f32_16x16x32_bf16 v[24:27], v[124:127], v[216:219], v[24:27]\n"
    "ds_write_b64 v232, v[16:17] offset:16384\n"
    "v_mfma_f32_16x16x32_bf16 v[28:31], v[128:131], v[216:219], v[28:31]\n"
    "v_add_u32 v246, s88, v254\n"
    "v_mfma_f32_16x16x32_bf16 v[32:35], v[132:135], v[216:219], v[32:35]\n"
    "v_mov_b32_e64 v252, 16\n"
    "v_mfma_f32_16x16x32_bf16 v[36:39], v[136:139], v[216:219], v[36:39]\n"
    "ds_add_u32 v246, v252 offset:76\n"
    "buffer_load_dwordx4 v[204:207], v237, s[8:11], s85 offen offset:1024 sc1\n"
    "s_waitcnt vmcnt(13)\n"
    "v_mfma_f32_16x16x32_bf16 v[20:23], v[140:143], v[220:223], v[20:23]\n"
    "v_mfma_f32_16x16x32_bf16 v[24:27], v[144:147], v[220:223], v[24:27]\n"
    ".Lk_pad10:\n"
    "s_cbranch_execz .Lk_tramp\n"
    "v_mfma_f32_16x16x32_bf16 v[28:31], v[148:151], v[220:223], v[28:31]\n"
    "v_mfma_f32_16x16x32_bf16 v[32:35], v[152:155], v[220:223], v[32:35]\n"
    "v_mfma_f32_16x16x32_bf16 v[36:39], v[156:159], v[220:223], v[36:39]\n"
    "s_waitcnt vmcnt(12)\n"
    "v_mfma_f32_16x16x32_bf16 v[20:23], v[160:163], v[224:227], v[20:23]\n"
    "v_mfma_f32_16x16x32_bf16 v[24:27], v[164:167], v[224:227], v[24:27]\n"
    "v_mfma_f32_16x16x32_bf16 v[28:31], v[168:171], v[224:227], v[28:31]\n"
    "v_mfma_f32_16x16x32_bf16 v[32:35], v[172:175], v[224:227], v[32:35]\n"
    "v_mfma_f32_16x16x32_bf16 v[36:39], v[176:179], v[224:227], v[36:39]\n"
    "s_waitcnt vmcnt(11)\n"
    "v_mfma_f32_16x16x32_bf16 v[0:3], v[64:67], v[40:43], 0\n"
    "s_nop 0\n"
    "v_mfma_f32_16x16x32_bf16 v[4:7], v[68:71], v[40:43], 0\n"
    "s_nop 0\n"
    "v_mfma_f32_16x16x32_bf16 v[8:11], v[72:75], v[40:43], 0\n"
    "v_cvt_pk_bf16_f32 v20, v20, v21\n"
    "v_mfma_f32_16x16x32_bf16 v[12:15], v[76:79], v[40:43], 0\n"
    "v_cvt_pk_bf16_f32 v21, v22, v23\n"
    "v_mfma_f32_16x16x32_bf16 v[16:19], v[80:83], v[40:43], 0\n"
    "ds_write_b64 v233, v[20:21]\n"
    "s_waitcnt vmcnt(10)\n"
    "s_nop 0\n"
    "v_mfma_f32_16x16x32_bf16 v[0:3], v[84:87], v[44:47], v[0:3]\n"
    "v_cvt_pk_bf16_f32 v24, v24, v25\n"
    "v_mfma_f32_16x16x32_bf16 v[4:7], v[88:91], v[44:47], v[4:7]\n"
    "v_cvt_pk_bf16_f32 v25, v26, v27\n"
    "v_mfma_f32_16x16x32_bf16 v[8:11], v[92:95], v[44:47], v[8:11]\n"
    "ds_write_b64 v233, v[24:25] offset:4096\n"
    "v_mfma_f32_16x16x32_bf16 v[12:15], v[96:99], v[44:47], v[12:15]\n"
    "v_cvt_pk_bf16_f32 v28, v28, v29\n"
    "v_mfma_f32_16x16x32_bf16 v[16:19], v[100:103], v[44:47], v[16:19]\n"
    "v_cvt_pk_bf16_f32 v29, v30, v31\n"
    "s_waitcnt vmcnt(9)\n"
    "s_nop 0\n"
    "v_mfma_f32_16x16x32_bf16 v[0:3], v[104:107], v[48:51], v[0:3]\n"
    "ds_write_b64 v233, v[28:29] offset:8192\n"
    "v_mfma_f32_16x16x32_bf16 v[4:7], v[228:231], v[48:51], v[4:7]\n"
    "v_cvt_pk_bf16_f32 v32, v32, v33\n"
    "v_mfma_f32_16x16x32_bf16 v[8:11], v[108:111], v[48:51], v[8:11]\n"
    "v_cvt_pk_bf16_f32 v33, v34, v35\n"
    "v_mfma_f32_16x16x32_bf16 v[12:15], v[112:115], v[48:51], v[12:15]\n"
    "ds_write_b64 v233, v[32:33] offset:12288\n"
    "v_mfma_f32_16x16x32_bf16 v[16:19], v[116:119], v[48:51], v[16:19]\n"
    "v_cvt_pk_bf16_f32 v36, v36, v37\n"
    "s_waitcnt vmcnt(8)\n"
    "v_mfma_f32_16x16x32_bf16 v[0:3], v[120:123], v[52:55], v[0:3]\n"
    "v_cvt_pk_bf16_f32 v37, v38, v39\n"
    "v_mfma_f32_16x16x32_bf16 v[4:7], v[124:127], v[52:55], v[4:7]\n"
    "ds_write_b64 v233, v[36:37] offset:16384\n"
    "v_mfma_f32_16x16x32_bf16 v[8:11], v[128:131], v[52:55], v[8:11]\n"
    "v_add_u32 v246, s89, v254\n"
    "v_mfma_f32_16x16x32_bf16 v[12:15], v[132:135], v[52:55], v[12:15]\n"
    "v_mov_b32_e64 v252, 32\n"
    "v_mfma_f32_16x16x32_bf16 v[16:19], v[136:139], v[52:55], v[16:19]\n"
    "ds_add_u32 v246, v252 offset:76\n"
    "s_waitcnt vmcnt(7)\n"
    "s_nop 0\n"
    "v_mfma_f32_16x16x32_bf16 v[0:3], v[140:143], v[56:59], v[0:3]\n"
    "ds_read_b128 v[20:23], v249\n"
    "v_mfma_f32_16x16x32_bf16 v[4:7], v[144:147], v[56:59], v[4:7]\n"
    "ds_read_b128 v[24:27], v249 offset:16\n"
    "v_mfma_f32_16x16x32_bf16 v[8:11], v[148:151], v[56:59], v[8:11]\n"
    "ds_read_b128 v[28:31], v249 offset:128\n"
    "v_mfma_f32_16x16x32_bf16 v[12:15], v[152:155], v[56:59], v[12:15]\n"
    "ds_read_b128 v[32:35], v249 offset:144\n"
    "v_mfma_f32_16x16x32_bf16 v[16:19], v[156:159], v[56:59], v[16:19]\n"
    "ds_read_b128 v[36:39], v250 offset:256\n"
    "s_waitcnt vmcnt(6)\n"
    "v_mfma_f32_16x16x32_bf16 v[0:3], v[160:163], v[180:183], v[0:3]\n"
    "v_mfma_f32_16x16x32_bf16 v[4:7], v[164:167], v[180:183], v[4:7]\n"
    "v_mfma_f32_16x16x32_bf16 v[8:11], v[168:171], v[180:183], v[8:11]\n"
    "v_mfma_f32_16x16x32_bf16 v[12:15], v[172:175], v[180:183], v[12:15]\n"
    "v_mfma_f32_16x16x32_bf16 v[16:19], v[176:179], v[180:183], v[16:19]\n"
    "s_waitcnt vmcnt(5) lgkmcnt(4)\n"
    "v_mfma_f32_16x16x32_bf16 v[20:23], v[64:67], v[184:187], v[20:23]\n"
    "s_nop 0\n"
    "s_nop 0\n"
    "s_waitcnt lgkmcnt(3)\n"
    "v_mfma_f32_16x16x32_bf16 v[24:27], v[68:71], v[184:187], v[24:27]\n"
    "v_cvt_pk_bf16_f32 v0, v0, v1\n"
    "v_cvt_pk_bf16_f32 v1, v2, v3\n"
    "s_waitcnt lgkmcnt(2)\n"
    "v_mfma_f32_16x16x32_bf16 v[28:31], v[72:75], v[184:187], v[28:31]\n"
    "ds_write_b64 v234, v[0:1]\n"
    "v_cvt_pk_bf16_f32 v4, v4, v5\n"
    "s_waitcnt lgkmcnt(2)\n"
    "v_mfma_f32_16x16x32_bf16 v[32:35], v[76:79], v[184:187], v[32:35]\n"
    "v_cvt_pk_bf16_f32 v5, v6, v7\n"
    "ds_write_b64 v234, v[4:5] offset:4096\n"
    "s_waitcnt lgkmcnt(2)\n"
    "v_mfma_f32_16x16x32_bf16 v[36:39], v[80:83], v[184:187], v[36:39]\n"
    "v_cvt_pk_bf16_f32 v8, v8, v9\n"
    "v_cvt_pk_bf16_f32 v9, v10, v11\n"
    "s_waitcnt vmcnt(4)\n"
    "s_nop 0\n"
    "v_mfma_f32_16x16x32_bf16 v[20:23], v[84:87], v[188:191], v[20:23]\n"
    "ds_write_b64 v234, v[8:9] offset:8192\n"
    "v_cvt_pk_bf16_f32 v12, v12, v13\n"
    "ds_read_b32 v235, v247 offset:76\n"
    "v_mfma_f32_16x16x32_bf16 v[24:27], v[88:91], v[188:191], v[24:27]\n"
    "v_cvt_pk_bf16_f32 v13, v14, v15\n"
    "ds_write_b64 v234, v[12:13] offset:12288\n"
    "v_mfma_f32_16x16x32_bf16 v[28:31], v[92:95], v[188:191], v[28:31]\n"
    "v_cvt_pk_bf16_f32 v16, v16, v17\n"
    "v_cvt_pk_bf16_f32 v17, v18, v19\n"
    "v_mfma_f32_16x16x32_bf16 v[32:35], v[96:99], v[188:191], v[32:35]\n"
    "ds_write_b64 v234, v[16:17] offset:16384\n"
    "v_add_u32_e64 v246, s90, v254\n"
    "v_mfma_f32_16x16x32_bf16 v[36:39], v[100:103], v[188:191], v[36:39]\n"
    "v_mov_b32_e64 v252, 64\n"
    "ds_add_u32 v246, v252 offset:76\n"
    "s_waitcnt lgkmcnt(3)\n"
    "v_readfirstlane_b32 s71, v235\n"
    "s_and_b32 s71, s71, 63\n"
    "s_cmp_eq_u32 s71, 63\n"
    "s_cbranch_scc0 .Lk_w141\n"
    ".Lk_d142:\n"
    "s_waitcnt vmcnt(3)\n"
    "v_mfma_f32_16x16x32_bf16 v[20:23], v[104:107], v[192:195], v[20:23]\n"
    "ds_read2st64_b64 v[208:211], v248 offset0:0 offset1:1\n"
    "ds_read2st64_b64 v[212:215], v248 offset0:8 offset1:9\n"
    "v_mfma_f32_16x16x32_bf16 v[24:27], v[228:231], v[192:195], v[24:27]\n"
    "ds_read2st64_b64 v[216:219], v248 offset0:16 offset1:17\n"
    "ds_read2st64_b64 v[220:223], v248 offset0:24 offset1:25\n"
    "v_mfma_f32_16x16x32_bf16 v[28:31], v[108:111], v[192:195], v[28:31]\n"
    "ds_read2st64_b64 v[224:227], v248 offset0:32 offset1:33\n"
    "ds_read2st64_b64 v[40:43], v248 offset0:2 offset1:3\n"
    "v_mfma_f32_16x16x32_bf16 v[32:35], v[112:115], v[192:195], v[32:35]\n"
    "ds_read2st64_b64 v[44:47], v248 offset0:10 offset1:11\n"
    "ds_read2st64_b64 v[48:51], v248 offset0:18 offset1:19\n"
    "v_mfma_f32_16x16x32_bf16 v[36:39], v[116:119], v[192:195], v[36:39]\n"
    "ds_read2st64_b64 v[52:55], v248 offset0:26 offset1:27\n"
    "ds_read2st64_b64 v[56:59], v248 offset0:34 offset1:35\n"
    "s_waitcnt vmcnt(2)\n"
    "s_nop 0\n"
    "v_mfma_f32_16x16x32_bf16 v[20:23], v[120:123], v[196:199], v[20:23]\n"
    "ds_read2st64_b64 v[0:3], v248 offset0:4 offset1:5\n"
    "ds_read2st64_b64 v[4:7], v248 offset0:12 offset1:13\n"
    "v_mfma_f32_16x16x32_bf16 v[24:27], v[124:127], v[196:199], v[24:27]\n"
    "ds_read2st64_b64 v[8:11], v248 offset0:20 offset1:21\n"
    "ds_read2st64_b64 v[12:15], v248 offset0:28 offset1:29\n"
    "v_mfma_f32_16x16x32_bf16 v[28:31], v[128:131], v[196:199], v[28:31]\n"
    "ds_read2st64_b64 v[16:19], v248 offset0:36 offset1:37\n"
    "v_mfma_f32_16x16x32_bf16 v[32:35], v[132:135], v[196:199], v[32:35]\n"
    "v_mfma_f32_16x16x32_bf16 v[36:39], v[136:139], v[196:199], v[36:39]\n"
    "s_waitcnt vmcnt(1)\n"
    "v_mfma_f32_16x16x32_bf16 v[20:23], v[140:143], v[200:203], v[20:23]\n"
    "v_mfma_f32_16x16x32_bf16 v[24:27], v[144:147], v[200:203], v[24:27]\n"
    "v_mfma_f32_16x16x32_bf16 v[28:31], v[148:151], v[200:203], v[28:31]\n"
    "v_mfma_f32_16x16x32_bf16 v[32:35], v[152:155], v[200:203], v[32:35]\n"
    "v_mfma_f32_16x16x32_bf16 v[36:39], v[156:159], v[200:203], v[36:39]\n"
    "s_waitcnt lgkmcnt(14)\n"
    "v_mfma_f32_16x16x32_bf16 v[20:23], v[60:63], v[208:211], v[20:23]\n"
    "s_waitcnt lgkmcnt(13)\n"
    "v_mfma_f32_16x16x32_bf16 v[24:27], v[60:63], v[212:215], v[24:27]\n"
    "s_waitcnt lgkmcnt(12)\n"
    "v_mfma_f32_16x16x32_bf16 v[28:31], v[60:63], v[216:219], v[28:31]\n"
    "s_waitcnt lgkmcnt(11)\n"
    "v_mfma_f32_16x16x32_bf16 v[32:35], v[60:63], v[220:223], v[32:35]\n"
    "s_waitcnt lgkmcnt(10)\n"
    "v_mfma_f32_16x16x32_bf16 v[36:39], v[60:63], v[224:227], v[36:39]\n"
    "s_waitcnt vmcnt(0)\n"
    "v_mfma_f32_16x16x32_bf16 v[20:23], v[160:163], v[204:207], v[20:23]\n"
    "ds_read_b32 v235, v247 offset:76\n"
    "v_mfma_f32_16x16x32_bf16 v[24:27], v[164:167], v[204:207], v[24:27]\n"
    "v_mfma_f32_16x16x32_bf16 v[28:31], v[168:171], v[204:207], v[28:31]\n"
    "v_mfma_f32_16x16x32_bf16 v[32:35], v[172:175], v[204:207], v[32:35]\n"
    "v_mfma_f32_16x16x32_bf16 v[36:39], v[176:179], v[204:207], v[36:39]\n"
    "s_waitcnt lgkmcnt(0)\n"
    "v_readfirstlane_b32 s71, v235\n"
    "s_and_b32 s71, s71, 64\n"
    "s_cmp_eq_u32 s71, 64\n"
    "s_cbranch_scc0 .Lk_w143\n"
    ".Lk_d144:\n"
    "ds_read2st64_b64 v[208:211], v248 offset0:6 offset1:7\n"
    "ds_read2st64_b64 v[212:215], v248 offset0:14 offset1:15\n"
    "ds_read2st64_b64 v[216:219], v248 offset0:22 offset1:23\n"
    "ds_read2st64_b64 v[220:223], v248 offset0:30 offset1:31\n"
    "ds_read2st64_b64 v[224:227], v248 offset0:38 offset1:39\n"
    "v_mfma_f32_16x16x32_bf16 v[20:23], v[60:63], v[40:43], v[20:23]\n"
    "v_mfma_f32_16x16x32_bf16 v[24:27], v[60:63], v[44:47], v[24:27]\n"
    "v_mfma_f32_16x16x32_bf16 v[28:31], v[60:63], v[48:51], v[28:31]\n"
    "v_mfma_f32_16x16x32_bf16 v[32:35], v[60:63], v[52:55], v[32:35]\n"
    "v_mfma_f32_16x16x32_bf16 v[36:39], v[60:63], v[56:59], v[36:39]\n"
    "v_mfma_f32_16x16x32_bf16 v[20:23], v[60:63], v[0:3], v[20:23]\n"
    "v_mfma_f32_16x16x32_bf16 v[24:27], v[60:63], v[4:7], v[24:27]\n"
    "v_mfma_f32_16x16x32_bf16 v[28:31], v[60:63], v[8:11], v[28:31]\n"
    "v_mfma_f32_16x16x32_bf16 v[32:35], v[60:63], v[12:15], v[32:35]\n"
    "v_mfma_f32_16x16x32_bf16 v[36:39], v[60:63], v[16:19], v[36:39]\n"
    "s_waitcnt lgkmcnt(4)\n"
    "v_mfma_f32_16x16x32_bf16 v[20:23], v[60:63], v[208:211], v[20:23]\n"
    "s_waitcnt lgkmcnt(3)\n"
    "v_mfma_f32_16x16x32_bf16 v[24:27], v[60:63], v[212:215], v[24:27]\n"
    "s_waitcnt lgkmcnt(2)\n"
    "v_mfma_f32_16x16x32_bf16 v[28:31], v[60:63], v[216:219], v[28:31]\n"
    "s_waitcnt lgkmcnt(1)\n"
    "v_mfma_f32_16x16x32_bf16 v[32:35], v[60:63], v[220:223], v[32:35]\n"
    "s_waitcnt lgkmcnt(0)\n"
    "s_nop 0\n"
    "v_mfma_f32_16x16x32_bf16 v[36:39], v[60:63], v[224:227], v[36:39]\n"
    "v_cvt_pk_bf16_f32 v40, v20, v21\n"
    "v_cvt_pk_bf16_f32 v41, v22, v23\n"
    "v_cvt_pk_bf16_f32 v42, v24, v25\n"
    "v_cvt_pk_bf16_f32 v43, v26, v27\n"
    "ds_write_b128 v236, v[40:43]\n"
    "v_cvt_pk_bf16_f32 v44, v28, v29\n"
    "v_cvt_pk_bf16_f32 v45, v30, v31\n"
    "v_cvt_pk_bf16_f32 v46, v32, v33\n"
    "v_cvt_pk_bf16_f32 v47, v34, v35\n"
    "ds_write_b128 v236, v[44:47] offset:64\n"
    "v_cvt_pk_bf16_f32 v48, v36, v37\n"
    "v_cvt_pk_bf16_f32 v49, v38, v39\n"
    "ds_write_b64 v238, v[48:49] offset:128\n"
    "ds_read_b128 v[0:3], v239\n"
    "ds_read_b128 v[4:7], v239 offset:1024\n"
    "ds_read_b128 v[8:11], v239 offset:2048\n"
    "s_waitcnt lgkmcnt(2)\n"
    "buffer_store_dwordx4 v[0:3], v240, s[12:15], 0 offen\n"
    "s_waitcnt lgkmcnt(1)\n"
    "buffer_store_dwordx4 v[4:7], v241, s[12:15], 0 offen\n"
    "s_mov_b32 exec_hi, 0\n"
    "s_waitcnt lgkmcnt(0)\n"
    "buffer_store_dwordx4 v[8:11], v242, s[12:15], 0 offen\n"
    "s_mov_b32 exec_hi, -1\n"
    "s_endpgm\n"
    ".Lk_pad11:\n"
    "s_cbranch_execz .Lk_tramp\n"
    ".Lk_w104:\n"
    "s_sleep 1\n"
    "ds_read_b32 v252, v254 offset:16\n"
    "s_waitcnt lgkmcnt(0)\n"
    "v_readfirstlane_b32 s71, v252\n"
    "s_cmp_ge_u32 s71, 1\n"
    "s_cbranch_scc1 .Lk_d105\n"
    "s_sub_u32 s54, s54, 1\n"
    "s_cbranch_scc0 .Lk_w104\n"
    "s_mov_b32 s54, 0\n"
    "s_branch .Lk_d105\n"
    ".Lk_w106:\n"
    "ds_read_b32 v252, v254 offset:16\n"
    "s_waitcnt lgkmcnt(0)\n"
    "v_readfirstlane_b32 s71, v252\n"
    "s_cmp_ge_u32 s71, 2\n"
    "s_cbranch_scc1 .Lk_d107\n"
    "s_sub_u32 s54, s54, 1\n"
    "s_cbranch_scc0 .Lk_w106\n"
    "s_mov_b32 s54, 0\n"
    "s_branch .Lk_d107\n"
    ".Lk_w108:\n"
    "ds_read_b32 v252, v254 offset:16\n"
    "s_waitcnt lgkmcnt(0)\n"
    "v_readfirstlane_b32 s71, v252\n"
    "s_cmp_ge_u32 s71, 3\n"
    "s_cbranch_scc1 .Lk_d109\n"
    "s_sub_u32 s54, s54, 1\n"
    "s_cbranch_scc0 .Lk_w108\n"
    "s_mov_b32 s54, 0\n"
    "s_branch .Lk_d109\n"
    ".Lk_w110:\n"
    "ds_read_b32 v252, v254 offset:16\n"
    "s_waitcnt lgkmcnt(0)\n"
    "v_readfirstlane_b32 s71, v252\n"
    "s_cmp_ge_u32 s71, 4\n"
    "s_cbranch_scc1 .Lk_d111\n"
    "s_sub_u32 s54, s54, 1\n"
    "s_cbranch_scc0 .Lk_w110\n"
    "s_mov_b32 s54, 0\n"
    "s_branch .Lk_d111\n"
    ".Lk_w112:\n"
    "ds_read_b32 v252, v254 offset:16\n"
    "s_waitcnt lgkmcnt(0)\n"
    "v_readfirstlane_b32 s71, v252\n"
    "s_cmp_ge_u32 s71, 5\n"
    "s_cbranch_scc1 .Lk_d113\n"
    "s_sub_u32 s54, s54, 1\n"
    "s_cbranch_scc0 .Lk_w112\n"
    "s_mov_b32 s54, 0\n"
    "s_branch .Lk_d113\n"
    ".Lk_w114:\n"
    "ds_read_b32 v252, v254 offset:108\n"
    "s_waitcnt lgkmcnt(0)\n"
    "v_readfirstlane_b32 s71, v252\n"
    "s_cmp_ge_u32 s71, 1\n"
    "s_cbranch_scc1 .Lk_d115\n"
    "s_sub_u32 s54, s54, 1\n"
    "s_cbranch_scc0 .Lk_w114\n"
    "s_mov_b32 s54, 0\n"
    "s_branch .Lk_d115\n"
    ".Lk_w116:\n"
    "s_sleep 1\n"
    "ds_read_b32 v252, v254 offset:24\n"
    "s_waitcnt lgkmcnt(0)\n"
    "v_readfirstlane_b32 s71, v252\n"
    "s_andn2_b32 s70, 1, s71\n"
    "s_cmp_eq_u32 s70, 0\n"
    "s_cbranch_scc1 .Lk_d117\n"
    "s_sub_u32 s54, s54, 1\n"
    "s_cbranch_scc0 .Lk_w116\n"
    "s_mov_b32 s54, 0\n"
    "s_branch .Lk_d117\n"
    ".Lk_w118:\n"
    "s_sleep 1\n"
    "ds_read_b32 v252, v254 offset:24\n"
    "s_waitcnt lgkmcnt(0)\n"
    "v_readfirstlane_b32 s71, v252\n"
    "s_andn2_b32 s70, 512, s71\n"
    "s_cmp_eq_u32 s70, 0\n"
    "s_cbranch_scc1 .Lk_d119\n"
    "s_sub_u32 s54, s54, 1\n"
    "s_cbranch_scc0 .Lk_w118\n"
    "s_mov_b32 s54, 0\n"
    "s_branch .Lk_d119\n"
    ".Lk_w123:\n"
    "s_sleep 1\n"
    "ds_read_b32 v252, v254 offset:24\n"
    "s_waitcnt lgkmcnt(0)\n"
    "v_readfirstlane_b32 s71, v252\n"
    "s_andn2_b32 s70, 256, s71\n"
    "s_cmp_eq_u32 s70, 0\n"
    "s_cbranch_scc1 .Lk_d124\n"
    "s_sub_u32 s54, s54, 1\n"
    "s_cbranch_scc0 .Lk_w123\n"
    "s_mov_b32 s54, 0\n"
    "s_branch .Lk_d124\n"
    ".Lk_kspin121:\n"
    "s_sleep 1\n"
    "ds_read_b32 v221, v254 offset:24\n"
    "s_waitcnt lgkmcnt(0)\n"
    "v_readfirstlane_b32 s71, v221\n"
    "s_bitcmp1_b32 s71, s87\n"
    "s_cbranch_scc1 .Lk_kback122\n"
    "s_sub_u32 s54, s54, 1\n"
    "s_cbranch_scc0 .Lk_kspin121\n"
    "s_mov_b32 s54, 0\n"
    "s_branch .Lk_kback122\n"
    ".Lk_w128:\n"
    "ds_read_b32 v252, v254 offset:144\n"
    "s_waitcnt lgkmcnt(0)\n"
    "v_readfirstlane_b32 s71, v252\n"
    "s_cmp_ge_u32 s71, 4\n"
    "s_cbranch_scc1 .Lk_d129\n"
    "s_sub_u32 s54, s54, 1\n"
    "s_cbranch_scc0 .Lk_w128\n"
    "s_mov_b32 s54, 0\n"
    "s_branch .Lk_d129\n"
    ".Lk_w130:\n"
    "ds_read_b32 v252, v247 offset:148\n"
    "s_waitcnt lgkmcnt(0)\n"
    "v_readfirstlane_b32 s71, v252\n"
    "s_cmp_ge_u32 s71, 2\n"
    "s_cbranch_scc1 .Lk_d131\n"
    "s_sub_u32 s54, s54, 1\n"
    "s_cbranch_scc0 .Lk_w130\n"
    "s_mov_b32 s54, 0\n"
    "s_branch .Lk_d131\n"
    ".Lk_w141:\n"
    "ds_read_b32 v252, v247 offset:76\n"
    "s_waitcnt lgkmcnt(0)\n"
    "v_readfirstlane_b32 s71, v252\n"
    "s_and_b32 s71, s71, 63\n"
    "s_cmp_eq_u32 s71, 63\n"
    "s_cbranch_scc1 .Lk_d142\n"
    "s_sub_u32 s54, s54, 1\n"
    "s_cbranch_scc0 .Lk_w141\n"
    "s_mov_b32 s54, 0\n"
    "s_branch .Lk_d142\n"
    ".Lk_w143:\n"
    "ds_read_b32 v252, v247 offset:76\n"
    "s_waitcnt lgkmcnt(0)\n"
    "v_readfirstlane_b32 s71, v252\n"
    "s_and_b32 s71, s71, 64\n"
    "s_cmp_eq_u32 s71, 64\n"
    "s_cbranch_scc1 .Lk_d144\n"
    "s_sub_u32 s54, s54, 1\n"
    "s_cbranch_scc0 .Lk_w143\n"
    "s_mov_b32 s54, 0\n"
    "s_branch .Lk_d144\n"
    ".Lk_slowp125:\n"
    "s_waitcnt vmcnt(0)\n"
    "s_waitcnt lgkmcnt(0)\n"
    "v_subrev_u32 v37, 124928, v218\n"
    "v_subrev_u32 v38, 124928, v219\n"
    "v_mov_b32 v32, 0xff61b1e6\n"
    "v_mov_b32 v33, 0xff61b1e6\n"
    "v_mov_b64 v[184:185], 0\n"
    "v_mov_b64 v[186:187], 0\n"
    "v_mov_b64 v[188:189], 0\n"
    "v_mov_b64 v[190:191], 0\n"
    "v_mov_b64 v[192:193], 0\n"
    "v_mov_b64 v[194:195], 0\n"
    "v_mov_b64 v[196:197], 0\n"
    "v_mov_b64 v[198:199], 0\n"
    "v_mov_b64 v[200:201], 0\n"
    "v_mov_b64 v[202:203], 0\n"
    "v_mov_b64 v[204:205], 0\n"
    "v_mov_b64 v[206:207], 0\n"
    "v_mov_b64 v[208:209], 0\n"
    "v_mov_b64 v[210:211], 0\n"
    "v_mov_b64 v[212:213], 0\n"
    "v_mov_b64 v[214:215], 0\n"
    "s_mov_b32 s56, 0\n"
    ".Lk_slow145:\n"
    "s_lshr_b32 s57, s56, 2\n"
    "s_and_b32 s58, s56, 3\n"
    "s_mul_i32 s60, s57, 0x2c00\n"
    "s_add_u32 s60, s60, 0x30000\n"
    "s_mul_i32 s61, s58, 0x400\n"
    "s_add_u32 s61, s61, s60\n"
    "s_mul_i32 s62, s58, 0x300\n"
    "s_add_u32 s62, s62, s60\n"
    "s_lshr_b32 s57, s58, 1\n"
    "s_and_b32 s58, s58, 1\n"
    "s_mul_i32 s63, s57, 0x200\n"
    "s_mul_i32 s70, s58, 0x8\n"
    "s_add_u32 s63, s63, s70\n"
    "s_add_u32 s63, s63, s60\n"
    "buffer_load_dwordx4 v[64:67], v255, s[4:7], s61 offen sc1\n"
    "buffer_load_dwordx2 v[72:73], v37, s[4:7], s63 offen sc1\n"
    "buffer_load_dwordx4 v[80:83], v38, s[4:7], s62 offen sc1\n"
    "buffer_load_dwordx4 v[88:91], v38, s[4:7], s62 offen offset:3072 sc1\n"
    "s_waitcnt vmcnt(0)\n"
    "v_mfma_f32_32x32x16_bf16 v[0:15], v[64:67], v[96:99], 0\n"
    "v_mfma_f32_32x32x8_bf16 v[0:15], v[72:73], v[104:105], v[0:15]\n"
    "s_nop 11\n"
    "v_max3_f32 v34, v32, v0, v1\n"
    "v_max3_f32 v34, v34, v2, v3\n"
    "v_max3_f32 v34, v34, v4, v5\n"
    "v_max3_f32 v34, v34, v6, v7\n"
    "v_max3_f32 v34, v34, v8, v9\n"
    "v_max3_f32 v34, v34, v10, v11\n"
    "v_max3_f32 v34, v34, v12, v13\n"
    "v_max3_f32 v34, v34, v14, v15\n"
    "v_mov_b32 v36, v34\n"
    "s_nop 1\n"
    "v_permlane32_swap_b32 v34, v36\n"
    "v_max_f32 v34, v34, v36\n"
    "v_sub_f32 v35, v32, v34\n"
    "v_exp_f32 v35, v35\n"
    "v_mov_b32 v32, v34\n"
    "v_sub_f32 v0, v0, v34\n"
    "v_sub_f32 v1, v1, v34\n"
    "v_sub_f32 v2, v2, v34\n"
    "v_sub_f32 v3, v3, v34\n"
    "v_sub_f32 v4, v4, v34\n"
    "v_sub_f32 v5, v5, v34\n"
    "v_sub_f32 v6, v6, v34\n"
    "v_sub_f32 v7, v7, v34\n"
    "v_sub_f32 v8, v8, v34\n"
    "v_sub_f32 v9, v9, v34\n"
    "v_sub_f32 v10, v10, v34\n"
    "v_sub_f32 v11, v11, v34\n"
    "v_sub_f32 v12, v12, v34\n"
    "v_sub_f32 v13, v13, v34\n"
    "v_sub_f32 v14, v14, v34\n"
    "v_sub_f32 v15, v15, v34\n"
    "v_exp_f32 v0, v0\n"
    "v_exp_f32 v1, v1\n"
    "v_exp_f32 v2, v2\n"
    "v_exp_f32 v3, v3\n"
    "v_exp_f32 v4, v4\n"
    "v_exp_f32 v5, v5\n"
    "v_exp_f32 v6, v6\n"
    "v_exp_f32 v7, v7\n"
    "v_exp_f32 v8, v8\n"
    "v_exp_f32 v9, v9\n"
    "v_exp_f32 v10, v10\n"
    "v_exp_f32 v11, v11\n"
    "v_exp_f32 v12, v12\n"
    "v_exp_f32 v13, v13\n"
    "v_exp_f32 v14, v14\n"
    "v_exp_f32 v15, v15\n"
    "v_mul_f32 v184, v184, v35\n"
    "v_mul_f32 v185, v185, v35\n"
    "v_mul_f32 v186, v186, v35\n"
    "v_mul_f32 v187, v187, v35\n"
    "v_mul_f32 v188, v188, v35\n"
    "v_mul_f32 v189, v189, v35\n"
    "v_mul_f32 v190, v190, v35\n"
    "v_mul_f32 v191, v191, v35\n"
    "v_mul_f32 v192, v192, v35\n"
    "v_mul_f32 v193, v193, v35\n"
    "v_mul_f32 v194, v194, v35\n"
    "s_nop 1\n"
    "v_smfmac_f32_32x32x32_bf16 v[184:199], v[80:83], v[0:7], v216\n"
    "v_smfmac_f32_32x32x32_bf16 v[184:199], v[88:91], v[8:15], v216\n"
    "v_mfma_f32_32x32x16_bf16 v[16:31], v[64:67], v[100:103], 0\n"
    "v_mfma_f32_32x32x8_bf16 v[16:31], v[72:73], v[106:107], v[16:31]\n"
    "s_nop 11\n"
    "v_max3_f32 v34, v33, v16, v17\n"
    "v_max3_f32 v34, v34, v18, v19\n"
    "v_max3_f32 v34, v34, v20, v21\n"
    "v_max3_f32 v34, v34, v22, v23\n"
    "v_max3_f32 v34, v34, v24, v25\n"
    "v_max3_f32 v34, v34, v26, v27\n"
    "v_max3_f32 v34, v34, v28, v29\n"
    "v_max3_f32 v34, v34, v30, v31\n"
    "v_mov_b32 v36, v34\n"
    "s_nop 1\n"
    "v_permlane32_swap_b32 v34, v36\n"
    "v_max_f32 v34, v34, v36\n"
    "v_sub_f32 v35, v33, v34\n"
    "v_exp_f32 v35, v35\n"
    "v_mov_b32 v33, v34\n"
    "v_sub_f32 v16, v16, v34\n"
    "v_sub_f32 v17, v17, v34\n"
    "v_sub_f32 v18, v18, v34\n"
    "v_sub_f32 v19, v19, v34\n"
    "v_sub_f32 v20, v20, v34\n"
    "v_sub_f32 v21, v21, v34\n"
    "v_sub_f32 v22, v22, v34\n"
    "v_sub_f32 v23, v23, v34\n"
    "v_sub_f32 v24, v24, v34\n"
    "v_sub_f32 v25, v25, v34\n"
    "v_sub_f32 v26, v26, v34\n"
    "v_sub_f32 v27, v27, v34\n"
    "v_sub_f32 v28, v28, v34\n"
    "v_sub_f32 v29, v29, v34\n"
    "v_sub_f32 v30, v30, v34\n"
    "v_sub_f32 v31, v31, v34\n"
    "v_exp_f32 v16, v16\n"
    "v_exp_f32 v17, v17\n"
    "v_exp_f32 v18, v18\n"
    "v_exp_f32 v19, v19\n"
    "v_exp_f32 v20, v20\n"
    "v_exp_f32 v21, v21\n"
    "v_exp_f32 v22, v22\n"
    "v_exp_f32 v23, v23\n"
    "v_exp_f32 v24, v24\n"
    "v_exp_f32 v25, v25\n"
    "v_exp_f32 v26, v26\n"
    "v_exp_f32 v27, v27\n"
    "v_exp_f32 v28, v28\n"
    "v_exp_f32 v29, v29\n"
    "v_exp_f32 v30, v30\n"
    "v_exp_f32 v31, v31\n"
    "v_mul_f32 v200, v200, v35\n"
    "v_mul_f32 v201, v201, v35\n"
    "v_mul_f32 v202, v202, v35\n"
    "v_mul_f32 v203, v203, v35\n"
    "v_mul_f32 v204, v204, v35\n"
    "v_mul_f32 v205, v205, v35\n"
    "v_mul_f32 v206, v206, v35\n"
    "v_mul_f32 v207, v207, v35\n"
    "v_mul_f32 v208, v208, v35\n"
    "v_mul_f32 v209, v209, v35\n"
    "v_mul_f32 v210, v210, v35\n"
    "s_nop 1\n"
    "v_smfmac_f32_32x32x32_bf16 v[200:215], v[80:83], v[16:23], v216\n"
    "v_smfmac_f32_32x32x32_bf16 v[200:215], v[88:91], v[24:31], v216\n"
    "s_add_u32 s56, s56, 1\n"
    "s_cmp_lt_u32 s56, 64\n"
    "s_cbranch_scc1 .Lk_slow145\n"
    "s_branch .Lk_joinp126\n"
    ".Lk_end:\n"
    : "+{s[0:1]}"(ka), "+{s2}"(bid), "+{v0}"(tid)
    :
    : "v1", "v2", "v3", "v4", "v5", "v6", "v7", "v8", "v9", "v10", "v11", "v12", "v13", "v14", "v15", "v16", "v17", "v18", "v19", "v20", "v21", "v22", "v23", "v24", "v25", "v26", "v27", "v28", "v29", "v30", "v31", "v32", "v33", "v34", "v35", "v36", "v37", "v38", "v39", "v40", "v41", "v42", "v43", "v44", "v45", "v46", "v47", "v48", "v49", "v50", "v51", "v52", "v53", "v54", "v55", "v56", "v57", "v58", "v59", "v60", "v61", "v62", "v63", "v64", "v65", "v66", "v67", "v68", "v69", "v70", "v71", "v72", "v73", "v74", "v75", "v76", "v77", "v78", "v79", "v80", "v81", "v82", "v83", "v84", "v85", "v86", "v87", "v88", "v89", "v90", "v91", "v92", "v93", "v94", "v95", "v96", "v97", "v98", "v99", "v100", "v101", "v102", "v103", "v104", "v105", "v106", "v107", "v108", "v109", "v110", "v111", "v112", "v113", "v114", "v115", "v116", "v117", "v118", "v119", "v120", "v121", "v122", "v123", "v124", "v125", "v126", "v127", "v128", "v129", "v130", "v131", "v132", "v133", "v134", "v135", "v136", "v137", "v138", "v139", "v140", "v141", "v142", "v143", "v144", "v145", "v146", "v147", "v148", "v149", "v150", "v151", "v152", "v153", "v154", "v155", "v156", "v157", "v158", "v159", "v160", "v161", "v162", "v163", "v164", "v165", "v166", "v167", "v168", "v169", "v170", "v171", "v172", "v173", "v174", "v175", "v176", "v177", "v178", "v179", "v180", "v181", "v182", "v183", "v184", "v185", "v186", "v187", "v188", "v189", "v190", "v191", "v192", "v193", "v194", "v195", "v196", "v197", "v198", "v199", "v200", "v201", "v202", "v203", "v204", "v205", "v206", "v207", "v208", "v209", "v210", "v211", "v212", "v213", "v214", "v215", "v216", "v217", "v218", "v219", "v220", "v221", "v222", "v223", "v224", "v225", "v226", "v227", "v228", "v229", "v230", "v231", "v232", "v233", "v234", "v235", "v236", "v237", "v238", "v239", "v240", "v241", "v242", "v243", "v244", "v245", "v246", "v247", "v248", "v249", "v250", "v251", "v252", "v253", "v254", "v255", "s3", "s4", "s5", "s6", "s7", "s8", "s9", "s10", "s11", "s12", "s13", "s14", "s15", "s16", "s17", "s18", "s19", "s20", "s21", "s22", "s23", "s24", "s25", "s26", "s27", "s28", "s29", "s30", "s31", "s32", "s33", "s34", "s35", "s36", "s37", "s38", "s39", "s40", "s41", "s42", "s43", "s44", "s45", "s46", "s47", "s48", "s49", "s50", "s51", "s52", "s53", "s54", "s55", "s56", "s57", "s58", "s59", "s60", "s61", "s62", "s63", "s64", "s65", "s66", "s67", "s68", "s69", "s70", "s71", "s72", "s73", "s74", "s75", "s76", "s77", "s78", "s79", "s80", "s81", "s82", "s83", "s84", "s85", "s86", "s87", "s88", "s89", "s90", "s91", "s92", "s93", "s94", "s95", "s96", "s97", "s98", "s99", "s100", "s101", "vcc", "scc", "memory");
}
}

extern "C" void kernel_launch(void* const* d_in, const int* in_sizes, int n_in, void* d_out, int out_size, void* d_ws, size_t ws_size,
                              hipStream_t stream) {
  mk::fused_block<<<256, 512, 0, stream>>>((const u16*)d_in[0], (const u16*)d_in[1], (const u16*)d_in[2], (const u16*)d_in[3],
                                           (const u16*)d_in[4], (const u16*)d_in[6], (const u16*)d_in[7], (const u16*)d_in[8],
                                           (const u16*)d_in[9], (u16*)d_out, (char*)d_ws);
}
```
